```python
import torch

def _softmax(x, axis=-1):
    return torch.softmax(x, dim=axis)

def _gelu(x):
    return torch.nn.functional.gelu(x, approximate='tanh')

def _silu(x):
    return torch.nn.functional.silu(x)

def _sigmoid(x):
    return torch.sigmoid(x)

def _layernorm(x, axis=-1, eps=1e-05):
    m = x.mean(dim=axis, keepdim=True)
    v = x.var(dim=axis, keepdim=True, unbiased=False)
    return (x - m) / torch.sqrt(v + eps)

def _scatter_add(tgt, idx, src, axis=0):
    return tgt.clone().index_add_(axis, idx, src)

def _segment_sum(src, seg, n):
    out = torch.zeros((n,) + src.shape[1:], dtype=src.dtype)
    return out.index_add_(0, seg, src)

def _pad(x, pw):
    flat = tuple((v for pair in reversed(pw) for v in pair))
    return torch.nn.functional.pad(x, flat)
kernel_name = 'adaLN_cond__mha_block_cached__glu_gated_geglu__decode'

def setup_inputs():

    _w = lambda a: (lambda t: t.to(torch.bfloat16) if t.is_floating_point() else t)(torch.as_tensor(a))

    def _A_setup():
        B, Sq, D = (1, 1, 512)
        H, Sk, Dh = (32, 65536, 128)
        return {'x': _w((lambda *s: torch.randn(*s, dtype=torch.bfloat16) * 0.05)(B, Sq, D)), 'k_cache': _w((lambda *s: torch.randn(*s, dtype=torch.bfloat16) * 0.8)(B, H, Sk, Dh)), 'v_cache': _w((lambda *s: torch.randn(*s, dtype=torch.bfloat16) * 3.2)(B, H, Sk, Dh)), 'wq': _w((lambda *s: torch.randn(*s, dtype=torch.bfloat16) * 0.8)(D, H * Dh) * D ** (-0.5)), 'wo': _w((lambda *s: torch.randn(*s, dtype=torch.bfloat16) * 3.2)(H * Dh, D) * (H * Dh) ** (-0.5))}
    _A_in = _A_setup()

    def _B_setup():
        B, S, Dm, Df = (1, 1, 512, 1792)
        return {'x': _w((lambda *s: torch.randn(*s, dtype=torch.bfloat16) * 0.05)(B, S, Dm)), 'w_gate': _w((lambda *s: torch.randn(*s, dtype=torch.bfloat16) * 3.2)(Dm, Df) * Dm ** (-0.5)), 'w_up': _w((lambda *s: torch.randn(*s, dtype=torch.bfloat16) * 3.2)(Dm, Df) * Dm ** (-0.5)), 'w_down': _w((lambda *s: torch.randn(*s, dtype=torch.bfloat16) * 3.2)(Df, Dm) * Df ** (-0.5))}
    _B_in = _B_setup()
    return {'A_x': _A_in['x'], 'A_k_cache': _A_in['k_cache'], 'A_v_cache': _A_in['v_cache'], 'A_wq': _A_in['wq'], 'A_wo': _A_in['wo'], 'B_x': _B_in['x'], 'B_w_gate': _B_in['w_gate'], 'B_w_up': _B_in['w_up'], 'B_w_down': _B_in['w_down'], 'c': _w((lambda *s: torch.randn(*s, dtype=torch.bfloat16) * 0.4)(1, 512) * 8.0), 'w_mod': _w((lambda *s: torch.randn(*s, dtype=torch.bfloat16) * 0.4)(512, 6 * 512) * 512 ** (-0.5) * 8.0)}

def reference(A_x, A_k_cache, A_v_cache, A_wq, A_wo, B_x, B_w_gate, B_w_up, B_w_down, c, w_mod):

    def _A_ref(x, k_cache, v_cache, wq, wo):
        B, Sq, D = x.shape
        H, Dh = (32, 128)
        q = (lambda x, *d: x.permute(*d))((x @ wq).reshape(B, Sq, H, Dh), 0, 2, 1, 3)
        scores = torch.einsum('bhqd,bhkd->bhqk', q, k_cache) * Dh ** (-0.5)
        p = _softmax(scores, axis=-1)
        o = torch.einsum('bhqk,bhkd->bhqd', p, v_cache)
        return (lambda x, *d: x.permute(*d))(o, 0, 2, 1, 3).reshape(B, Sq, H * Dh) @ wo

    def _B_ref(x, w_gate, w_up, w_down):
        g = torch.einsum('bsd,df->bsf', x, w_gate)
        u = torch.einsum('bsd,df->bsf', x, w_up)
        h = _gelu(g) * u
        return torch.einsum('bsf,fd->bsd', h, w_down)
    x = A_x
    mod = (_silu(c) @ w_mod).reshape(c.shape[0], 6, 1, -1)
    sa, ca, ga, sb, cb, gb = (mod[:, 0].clone(), mod[:, 1].clone(), mod[:, 2].clone(), mod[:, 3].clone(), mod[:, 4].clone(), mod[:, 5].clone())
    h = _layernorm(x, axis=-1) * (1 + ca) + sa
    x = x + ga * _A_ref(h, A_k_cache, A_v_cache, A_wq, A_wo)
    h = _layernorm(x, axis=-1) * (1 + cb) + sb
    x = x + gb * _B_ref(h, B_w_gate, B_w_up, B_w_down)
    return x
```

```cpp
#include <hip/hip_runtime.h>
#include <hip/hip_bf16.h>
#define ROUND_SCORES 1
typedef unsigned short u16;
typedef unsigned v4u __attribute__((ext_vector_type(4)));
typedef __bf16 bf16x2 __attribute__((ext_vector_type(2)));
__device__ __forceinline__ float bf2f(u16 b) { return __builtin_bit_cast(float, (unsigned)b << 16); }
__device__ __forceinline__ u16 f2bf(float f) { return __builtin_bit_cast(u16, (__bf16)f); }
__device__ __forceinline__ float rbf(float f) { return bf2f(f2bf(f)); }
__device__ __forceinline__ float dot2(unsigned a, unsigned b, float c) { return __builtin_amdgcn_fdot2_f32_bf16(__builtin_bit_cast(bf16x2, a), __builtin_bit_cast(bf16x2, b), c, false); }
constexpr int D = 512, H = 32, SK = 65536, DH = 128, DF = 1792, NS = 32;

__global__ void k_mod(const u16* c, const u16* wm, float* mod) {
  __shared__ float sc[512]; __shared__ float red[4][64];
  for (int i = threadIdx.x; i < 512; i += 256) { float v = bf2f(c[i]); sc[i] = rbf(v / (1.f + __expf(-v))); }
  __syncthreads();
  int col = blockIdx.x * 64 + (threadIdx.x & 63), kq = threadIdx.x >> 6; float a = 0;
  for (int k = kq * 128; k < kq * 128 + 128; ++k) a += sc[k] * bf2f(wm[(size_t)k * 3072 + col]);
  red[kq][threadIdx.x & 63] = a; __syncthreads();
  if (threadIdx.x < 64) mod[col] = rbf(red[0][threadIdx.x] + red[1][threadIdx.x] + red[2][threadIdx.x] + red[3][threadIdx.x]);
}
__device__ void layernorm_mod(const float* xs, const float* sc, const float* sh, float* h, float* tmp) {
  float s = 0; for (int i = 0; i < 512; ++i) s += xs[i]; float m = rbf(s / 512.f);
  float mf = s / 512.f; float v = 0; for (int i = 0; i < 512; ++i) { float d = xs[i] - mf; v += d * d; } v = rbf(v / 512.f);
  float sd = rbf(sqrtf(rbf(v + 1e-5f)));
  for (int i = threadIdx.x; i < 512; i += 256) { float n = rbf(rbf(xs[i] - m) / sd); h[i] = rbf(rbf(n * rbf(1.f + sc[i])) + sh[i]); }
}
__global__ void k_q(const u16* x, const float* mod, const u16* wq, u16* q) {
  __shared__ float xs[512], h[512], red[4][64];
  for (int i = threadIdx.x; i < 512; i += 256) xs[i] = bf2f(x[i]);
  __syncthreads();
  layernorm_mod(xs, mod + 512, mod, h, nullptr); __syncthreads();
  int col = blockIdx.x * 64 + (threadIdx.x & 63), kq = threadIdx.x >> 6; float a = 0;
  for (int k = kq * 128; k < kq * 128 + 128; ++k) a += h[k] * bf2f(wq[(size_t)k * 4096 + col]);
  red[kq][threadIdx.x & 63] = a; __syncthreads();
  if (threadIdx.x < 64) q[col] = f2bf(red[0][threadIdx.x] + red[1][threadIdx.x] + red[2][threadIdx.x] + red[3][threadIdx.x]);
}
__global__ void __launch_bounds__(256) k_attn(const u16* q, const u16* K, const u16* V, float* part) {
  __shared__ float sm[4][132];
  int s = blockIdx.x, h = blockIdx.y, lane = threadIdx.x & 63, w = threadIdx.x >> 6, g = lane >> 4, c = lane & 15;
  v4u qv = *(const v4u*)(q + h * 128 + c * 8);
  const int per = SK / NS; float m = -INFINITY, l = 0, acc[8] = {0};
  for (int t = w; t < per / 16; t += 4) {
    size_t base = ((size_t)h * SK + s * per + t * 16 + g) * 128 + c * 8;
    v4u kk[4], vv[4]; float sc[4];
    for (int j = 0; j < 4; ++j) { kk[j] = __builtin_nontemporal_load((const v4u*)(K + base + j * 512)); vv[j] = __builtin_nontemporal_load((const v4u*)(V + base + j * 512)); }
    float mx = m;
    for (int j = 0; j < 4; ++j) { float d = 0; for (int e = 0; e < 4; ++e) d = dot2(kk[j][e], qv[e], d);
      for (int o = 8; o; o >>= 1) d += __shfl_xor(d, o);
      d = ROUND_SCORES ? rbf(rbf(d) * 0.08838834764831845f) : d * 0.08838834764831845f; sc[j] = d; mx = fmaxf(mx, d); }
    mx = fmaxf(mx, __shfl_xor(mx, 16)); mx = fmaxf(mx, __shfl_xor(mx, 32));
    float al = __expf(m - mx); m = mx; l *= al; for (int e = 0; e < 8; ++e) acc[e] *= al;
    for (int j = 0; j < 4; ++j) { float p = __expf(sc[j] - m); l += p;
      for (int e = 0; e < 4; ++e) { acc[2 * e] += p * __builtin_bit_cast(float, vv[j][e] << 16); acc[2 * e + 1] += p * __builtin_bit_cast(float, vv[j][e] & 0xffff0000u); } }
  }
  l += __shfl_xor(l, 16); l += __shfl_xor(l, 32);
  for (int e = 0; e < 8; ++e) { acc[e] += __shfl_xor(acc[e], 16); acc[e] += __shfl_xor(acc[e], 32); }
  if (g == 0) { for (int e = 0; e < 8; ++e) sm[w][4 + c * 8 + e] = acc[e]; if (c == 0) { sm[w][0] = m; sm[w][1] = l; } }
  __syncthreads();
  if (threadIdx.x < 128) { float M = fmaxf(fmaxf(sm[0][0], sm[1][0]), fmaxf(sm[2][0], sm[3][0])); float L = 0, o = 0;
    for (int i = 0; i < 4; ++i) { float wt = __expf(sm[i][0] - M); L += wt * sm[i][1]; o += wt * sm[i][4 + threadIdx.x]; }
    float* p = part + ((size_t)h * NS + s) * 132; p[4 + threadIdx.x] = o; if (threadIdx.x == 0) { p[0] = M; p[1] = L; } }
}
__global__ void k_wo(const float* part, const u16* wo, float* yp) {
  __shared__ float o[128];
  int h = blockIdx.x;
  if (threadIdx.x < 128) { const float* p = part + (size_t)h * NS * 132; float M = -INFINITY; for (int s = 0; s < NS; ++s) M = fmaxf(M, p[s * 132]);
    float L = 0, a = 0; for (int s = 0; s < NS; ++s) { float wt = __expf(p[s * 132] - M); L += wt * p[s * 132 + 1]; a += wt * p[s * 132 + 4 + threadIdx.x]; } o[threadIdx.x] = rbf(a / L); }
  __syncthreads();
  for (int n = threadIdx.x; n < 512; n += 256) { float a = 0; for (int k = 0; k < 128; ++k) a += o[k] * bf2f(wo[(size_t)(h * 128 + k) * 512 + n]); yp[h * 512 + n] = a; }
}
__global__ void k_gu(const u16* x, const float* mod, const float* yp, const u16* wg, const u16* wu, float* h2, float* x1out) {
  __shared__ float xs[512], h[512], red[2][4][64];
  for (int i = threadIdx.x; i < 512; i += 256) { float a = 0; for (int hh = 0; hh < 32; ++hh) a += yp[hh * 512 + i]; xs[i] = rbf(bf2f(x[i]) + rbf(mod[1024 + i] * rbf(a))); if (blockIdx.x == 0) x1out[i] = xs[i]; }
  __syncthreads();
  layernorm_mod(xs, mod + 2048, mod + 1536, h, nullptr); __syncthreads();
  int col = blockIdx.x * 64 + (threadIdx.x & 63), kq = threadIdx.x >> 6; float a = 0, b = 0;
  for (int k = kq * 128; k < kq * 128 + 128; ++k) { a += h[k] * bf2f(wg[(size_t)k * DF + col]); b += h[k] * bf2f(wu[(size_t)k * DF + col]); }
  red[0][kq][threadIdx.x & 63] = a; red[1][kq][threadIdx.x & 63] = b; __syncthreads();
  if (threadIdx.x < 64) { int t = threadIdx.x; float g = rbf(red[0][0][t] + red[0][1][t] + red[0][2][t] + red[0][3][t]), u = rbf(red[1][0][t] + red[1][1][t] + red[1][2][t] + red[1][3][t]);
    float ge = rbf(0.5f * g * (1.f + tanhf(0.7978845608028654f * (g + 0.044715f * g * g * g)))); h2[col] = rbf(ge * u); }
}
__global__ void k_down(const float* h2, const u16* wd, const float* x1, const float* mod, u16* out) {
  __shared__ float red[4][64];
  int col = blockIdx.x * 64 + (threadIdx.x & 63), kq = threadIdx.x >> 6; float a = 0;
  for (int k = kq * 448; k < kq * 448 + 448; ++k) a += h2[k] * bf2f(wd[(size_t)k * 512 + col]);
  red[kq][threadIdx.x & 63] = a; __syncthreads();
  if (threadIdx.x < 64) { int t = threadIdx.x; float y = rbf(red[0][t] + red[1][t] + red[2][t] + red[3][t]); out[col] = f2bf(x1[col] + rbf(mod[2560 + col] * y)); }
}
extern "C" void kernel_launch(void* const* d_in, const int* in_sizes, int n_in, void* d_out, int out_size, void* d_ws, size_t ws_size, hipStream_t stream) {
  const u16 *x = (const u16*)d_in[0], *K = (const u16*)d_in[1], *V = (const u16*)d_in[2], *wq = (const u16*)d_in[3], *wo = (const u16*)d_in[4];
  const u16 *wg = (const u16*)d_in[6], *wu = (const u16*)d_in[7], *wd = (const u16*)d_in[8], *c = (const u16*)d_in[9], *wm = (const u16*)d_in[10];
  float* ws = (float*)d_ws; float* mod = ws; u16* q = (u16*)(ws + 4096); float* part = ws + 8192; float* yp = part + H * NS * 132; float* h2 = yp + 32 * 512; float* x1 = h2 + 2048;
  k_mod<<<48, 256, 0, stream>>>(c, wm, mod);
  k_q<<<64, 256, 0, stream>>>(x, mod, wq, q);
  k_attn<<<dim3(NS, H), 256, 0, stream>>>(q, K, V, part);
  k_wo<<<H, 256, 0, stream>>>(part, wo, yp);
  k_gu<<<28, 256, 0, stream>>>(x, mod, yp, wg, wu, h2, x1);
  k_down<<<8, 256, 0, stream>>>(h2, wd, x1, mod, (u16*)d_out);
}
```

```cpp
#include <hip/hip_runtime.h>
#include <hip/hip_bf16.h>

#define GAS __attribute__((address_space(1)))
#define LAS __attribute__((address_space(3)))
typedef unsigned short u16;
typedef unsigned long long u64;
typedef unsigned v4u __attribute__((ext_vector_type(4)));
typedef unsigned v2u __attribute__((ext_vector_type(2)));
typedef int v4i __attribute__((ext_vector_type(4)));
typedef float f32x4 __attribute__((ext_vector_type(4)));
typedef __bf16 bf16x8 __attribute__((ext_vector_type(8)));
typedef __bf16 bf16x2 __attribute__((ext_vector_type(2)));
typedef float f32x2 __attribute__((ext_vector_type(2)));

#define DBG_TS 0
#define INFL 2
#define PQ 16
#define LSLEEP 6
#define NSLOT 9
#define WIT0 6
#define M2IT 48
#define PSLEEP 1
#define STAT 216

namespace {
constexpr int DM = 512, SK = 65536, DH = 128, DF = 1792;
constexpr int NWG = 256, NTHR = 384;
constexpr int SLOT = 16384, TILES_H = SK / 32, TG = 4;
constexpr int DYN0 = 8 * STAT, NTICK = (TILES_H - DYN0) / TG;
static_assert((TILES_H - DYN0) % TG == 0 && STAT >= NSLOT + TG && TG > INFL && INFL >= 1 && INFL <= 3 && PQ <= 16, "tile plan");
static_assert(4 * (WIT0 + 31 + 4) + 3 < STAT && 4 * M2IT + 3 < STAT, "the tail operands are requested inside the static share");
constexpr unsigned HEAD_BYTES = (unsigned)SK * DH * 2;
constexpr unsigned EMPTY = 0xFFFFFFFFu;
constexpr unsigned OOB = 0x7FFF0000u;
constexpr u64 TLIMIT = 5000000ull;
constexpr float LOG2E = 1.4426950408889634f, SCALE = 0.08838834764831845f;
constexpr int NGU = DF / 16, REC = 640;
constexpr unsigned WS_EPOCH = 0, WS_P1 = 4096, WS_QS = WS_P1 + 32768, WS_M2 = WS_QS + 131072, WS_CTR = WS_M2 + 4096,
                   WS_YP = WS_CTR + 8192, WS_PO = WS_YP + 16384, WS_DP = WS_PO + NWG * REC, WS_END = WS_DP + NGU * 2048,
                   WS_DBG = 1u << 20;
constexpr unsigned L_RING = 0, L_CTL = NSLOT * SLOT, L_FT = L_CTL, L_DONE = L_CTL + 16, L_QCNT = L_CTL + 32, L_AUXPUB = L_CTL + 36,
                   L_RED = L_CTL + 64, L_QSC = L_RED + 1024, L_RED2 = L_QSC + 1024, L_RED3 = L_RED2 + 256, L_ST = L_RED3 + 512,
                   L_X1 = L_ST + 64, L_MRG = L_X1 + 2048, L_END = L_MRG + 4 * 528;
constexpr unsigned LDS_BYTES = L_END + (DBG_TS ? 2048 : 0);
static_assert(LDS_BYTES <= 160 * 1024 && LDS_BYTES > 80 * 1024, "one workgroup per CU");

struct Args {
  const u16 *x, *K, *V, *wq, *wo, *wg, *wu, *wd, *c, *wm;
  u16* out;
  unsigned char* ws;
};

__device__ __forceinline__ u64 rt() { return __builtin_amdgcn_s_memrealtime(); }
__device__ __forceinline__ float asf(unsigned u) { return __builtin_bit_cast(float, u); }
__device__ __forceinline__ unsigned asu(float f) { return __builtin_bit_cast(unsigned, f); }
__device__ __forceinline__ float lo16(unsigned u) { return asf(u << 16); }
__device__ __forceinline__ float hi16(unsigned u) { return asf(u & 0xffff0000u); }
__device__ __forceinline__ unsigned cvtpk(float a, float b) {
  const f32x2 v = {a, b};
  return __builtin_bit_cast(unsigned, __builtin_convertvector(v, bf16x2));
}
__device__ __forceinline__ float rbf(float f) { return lo16(cvtpk(f, f)); }
__device__ __forceinline__ unsigned noempty(unsigned v) { return v == EMPTY ? 0x7FC07FC0u : v; }
__device__ __forceinline__ float dot2(unsigned a, unsigned b, float c) {
  return __builtin_amdgcn_fdot2_f32_bf16(__builtin_bit_cast(bf16x2, a), __builtin_bit_cast(bf16x2, b), c, false);
}
template <int CTRL> __device__ __forceinline__ float dpp(float v) {
  return asf(__builtin_amdgcn_mov_dpp(asu(v), CTRL, 0xf, 0xf, true));
}
constexpr int ROR8 = 0x128, ROR4 = 0x124, ROR2 = 0x122, ROR1 = 0x121, XOR2 = 0x4E, XOR1 = 0xB1;
__device__ __forceinline__ float sw32_add(float a, float b) {
  const auto r = __builtin_amdgcn_permlane32_swap(asu(a), asu(b), false, false);
  return asf(r[0]) + asf(r[1]);
}
__device__ __forceinline__ float sw16_add(float a, float b) {
  const auto r = __builtin_amdgcn_permlane16_swap(asu(a), asu(b), false, false);
  return asf(r[0]) + asf(r[1]);
}
__device__ __forceinline__ float xrow_max(float v) {
  const auto r = __builtin_amdgcn_permlane16_swap(asu(v), asu(v), false, false);
  v = fmaxf(asf(r[0]), asf(r[1]));
  const auto s = __builtin_amdgcn_permlane32_swap(asu(v), asu(v), false, false);
  return fmaxf(asf(s[0]), asf(s[1]));
}
__device__ __forceinline__ unsigned pack_halves(float v) {
  const auto r = __builtin_amdgcn_permlane32_swap(asu(v), asu(v), false, false);
  return __builtin_amdgcn_perm(r[1], r[0], 0x07060302u);
}
__device__ __forceinline__ float row_sum(float v) { v += dpp<ROR8>(v); v += dpp<ROR4>(v); v += dpp<ROR2>(v); v += dpp<ROR1>(v); return v; }
__device__ __forceinline__ float row_max(float v) { v = fmaxf(v, dpp<ROR8>(v)); v = fmaxf(v, dpp<ROR4>(v)); v = fmaxf(v, dpp<ROR2>(v)); return fmaxf(v, dpp<ROR1>(v)); }
__device__ __forceinline__ float wave_sum(float v) { v = row_sum(v); v = sw16_add(v, v); return sw32_add(v, v); }
__device__ __forceinline__ void rows_reduce8(const float (&a)[8], float& u0, float& u1) {
  const float r0 = sw32_add(a[0], a[4]), r1 = sw32_add(a[1], a[5]), r2 = sw32_add(a[2], a[6]), r3 = sw32_add(a[3], a[7]);
  u0 = sw16_add(r0, r2); u1 = sw16_add(r1, r3);
}
__device__ __forceinline__ void fma8(float (&a)[8], float h, v4u w) {
#pragma unroll
  for (int e = 0; e < 4; ++e) { a[2 * e] = fmaf(h, lo16(w[e]), a[2 * e]); a[2 * e + 1] = fmaf(h, hi16(w[e]), a[2 * e + 1]); }
}
__device__ __forceinline__ void kpair(v4u& a, v4u& b) {
  const v4u r0 = a, r1 = b;
  a = v4u{__builtin_amdgcn_perm(r1[0], r0[0], 0x05040100u), __builtin_amdgcn_perm(r1[0], r0[0], 0x07060302u),
          __builtin_amdgcn_perm(r1[1], r0[1], 0x05040100u), __builtin_amdgcn_perm(r1[1], r0[1], 0x07060302u)};
  b = v4u{__builtin_amdgcn_perm(r1[2], r0[2], 0x05040100u), __builtin_amdgcn_perm(r1[2], r0[2], 0x07060302u),
          __builtin_amdgcn_perm(r1[3], r0[3], 0x05040100u), __builtin_amdgcn_perm(r1[3], r0[3], 0x07060302u)};
}
__device__ __forceinline__ void dot8(float (&acc)[8], unsigned hp, v4u a, v4u b) {
#pragma unroll
  for (int e = 0; e < 4; ++e) { acc[e] = dot2(a[e], hp, acc[e]); acc[4 + e] = dot2(b[e], hp, acc[4 + e]); }
}
__device__ __forceinline__ f32x4 mfma(v4u a, v4u b, f32x4 c) {
  return __builtin_amdgcn_mfma_f32_16x16x32_bf16(__builtin_bit_cast(bf16x8, a), __builtin_bit_cast(bf16x8, b), c, 0, 0, 0);
}
typedef __amdgpu_buffer_rsrc_t rsrc_t;
__device__ __forceinline__ rsrc_t mkrsrc(const void* p, unsigned bytes) { return __builtin_amdgcn_make_buffer_rsrc((void*)p, 0, bytes, 0x00020000); }
constexpr int SC1 = 16, NT = 2;
template <int AUX> __device__ __forceinline__ v4u ld128(rsrc_t r, unsigned off) { return __builtin_amdgcn_raw_buffer_load_b128(r, off, 0, AUX); }
template <int AUX> __device__ __forceinline__ v2u ld64(rsrc_t r, unsigned off) { return __builtin_amdgcn_raw_buffer_load_b64(r, off, 0, AUX); }
template <int AUX> __device__ __forceinline__ unsigned ld32(rsrc_t r, unsigned off) { return __builtin_amdgcn_raw_buffer_load_b32(r, off, 0, AUX); }
__device__ __forceinline__ void st128(rsrc_t r, unsigned off, v4u v) { __builtin_amdgcn_raw_buffer_store_b128(v, r, off, 0, SC1); }
__device__ __forceinline__ void st64(rsrc_t r, unsigned off, v2u v) { __builtin_amdgcn_raw_buffer_store_b64(v, r, off, 0, SC1); }
__device__ __forceinline__ void st32(rsrc_t r, unsigned off, unsigned v) { __builtin_amdgcn_raw_buffer_store_b32(v, r, off, 0, SC1); }
__device__ __forceinline__ unsigned umax(unsigned a, unsigned b) { return a > b ? a : b; }
__device__ __forceinline__ unsigned mx4(unsigned m, v4u v) { return umax(umax(umax(m, v[0]), v[1]), umax(v[2], v[3])); }
__device__ __forceinline__ unsigned mx2(unsigned m, v2u v) { return umax(umax(m, v[0]), v[1]); }
__device__ __forceinline__ v4u noempty4(f32x4 v) { return v4u{noempty(asu(v[0])), noempty(asu(v[1])), noempty(asu(v[2])), noempty(asu(v[3]))}; }
__device__ __forceinline__ unsigned gr_lo(float v, unsigned tlo) { return ((asu(v) + 8u) & 0xFFFFFFF0u) | tlo; }
__device__ __forceinline__ unsigned gr_bad(v4u g, unsigned tlo, unsigned thi) { return (((g[0] ^ tlo) | (g[2] ^ tlo)) & 0xFu) | (g[1] ^ thi) | (g[3] ^ thi); }
__device__ __forceinline__ float gr_sum(v4u g) { const float s = sw32_add(asf(g[0] & 0xFFFFFFF0u), asf(g[2] & 0xFFFFFFF0u)); return sw16_add(s, s); }
#define MEMFENCE() asm volatile("" ::: "memory")
#define LGKM0() asm volatile("s_waitcnt lgkmcnt(0)" ::: "memory")
#define VM0() asm volatile("s_waitcnt vmcnt(0)" ::: "memory")
#define PIN4(a) asm volatile("" : "+v"((a)[0]), "+v"((a)[1]), "+v"((a)[2]), "+v"((a)[3]))
template <int N> __device__ __forceinline__ void vmwait() { asm volatile("s_waitcnt vmcnt(%0)" ::"n"(N) : "memory"); }
__device__ __forceinline__ void wg_barrier() { LGKM0(); __builtin_amdgcn_s_barrier(); MEMFENCE(); }
template <typename T> __device__ __forceinline__ volatile LAS T* lds_at(unsigned off) { return (volatile LAS T*)(size_t)off; }
template <typename T> __device__ __forceinline__ LAS T* lds_pl(unsigned off) { return (LAS T*)(size_t)off; }
__device__ __forceinline__ void lds_wait_ge(unsigned addr, unsigned need, u64 t0) {
  for (unsigned sp = 0; *lds_at<unsigned>(addr) < need; ++sp) { __builtin_amdgcn_s_sleep(1); if ((sp & 63u) == 63u && rt() - t0 > TLIMIT) break; }
  MEMFENCE();
}

#define STAMP(k) do {} while (0)
#define STAMP_INIT() do {} while (0)
#define STAMP_FLUSH() do {} while (0)

__device__ __forceinline__ void dma4(v4i srd, unsigned soff, unsigned ldsb, unsigned v0, unsigned v1, unsigned v2, unsigned v3) {
  asm volatile("s_mov_b32 m0, %0\n\ts_nop 0\n\t"
               "buffer_load_dwordx4 %1, %5, %6 offen nt lds\n\t"
               "buffer_load_dwordx4 %2, %5, %6 offen offset:1024 nt lds\n\t"
               "buffer_load_dwordx4 %3, %5, %6 offen offset:2048 nt lds\n\t"
               "buffer_load_dwordx4 %4, %5, %6 offen offset:3072 nt lds"
               ::"s"(ldsb), "v"(v0), "v"(v1), "v"(v2), "v"(v3), "s"(srd), "s"(soff) : "memory");
}

__device__ __forceinline__ float silu_bf(float v) { return rbf(v / (1.f + __expf(-v))); }
__device__ __forceinline__ void ln_round(float mean, float var, float& mb, float& sd) {
  mb = rbf(mean);
  sd = rbf(__builtin_sqrtf(rbf(rbf(var) + 1e-5f)));
}
__device__ __forceinline__ float mod_round(float n, float sc, float sh) { return rbf(rbf(rbf(n) * rbf(1.f + sc)) + sh); }

__global__ void __launch_bounds__(NTHR, 2) block_decode(Args a) {
  extern __shared__ __attribute__((aligned(16))) unsigned char smem[];
  const int tid = threadIdx.x, lane = tid & 63, wave = __builtin_amdgcn_readfirstlane(tid >> 6);
  const int b = blockIdx.x, x = b & 7, j = b >> 3;
  const u64 t0 = rt();
  const unsigned lb = (unsigned)(size_t)(LAS unsigned char*)smem;
  const rsrc_t ws = mkrsrc(a.ws, WS_END);
  const int kq = j >> 3, cb = x + 8 * (j & 7);
  const int gq = j >> 2, oc0 = 64 * x + 16 * (j & 3);
  const bool has_gu = x + 8 * gq < 28;
  const int up = 4 * (x + 8 * gq) + (j & 3);
  const int m2c = 512 * kq + 64 * x + 8 * (j & 7);
  const int grp = 4 * x + kq;

  if (tid < 16) *lds_pl<unsigned>(lb + L_CTL + 4 * tid) = (tid == 1) ? 0x7FFFFFFFu : 0u;
  STAMP_INIT();
  wg_barrier();
  STAMP(0);

  if (wave == 4) {
    const u64 kb = (u64)(a.K + (size_t)j * SK * DH), vb = (u64)(a.V + (size_t)j * SK * DH);
    const v4i ksrd = {(int)kb, (int)(kb >> 32), (int)HEAD_BYTES, 0x00020000}, vsrd = {(int)vb, (int)(vb >> 32), (int)HEAD_BYTES, 0x00020000};
    const unsigned g = lane >> 4, c = lane & 15;
    const unsigned k0 = g * 256 + ((c ^ g) << 4), k1 = g * 256 + ((c ^ (4 + g)) << 4), k2 = g * 256 + ((c ^ (8 + g)) << 4), k3 = g * 256 + ((c ^ (12 + g)) << 4);
    const unsigned vl = lane * 16;
    const GAS unsigned* ctr = (const GAS unsigned*)(a.ws + WS_CTR + j * 256);
    unsigned sl = lb + L_RING;
    auto fill = [&](int tile) {
      const unsigned so = (unsigned)tile * 8192u;
      dma4(ksrd, so, sl, k0, k1, k2, k3);
      dma4(ksrd, so + 4096, sl + 4096, k0, k1, k2, k3);
      dma4(vsrd, so, sl + 8192, vl, vl, vl, vl);
      dma4(vsrd, so + 4096, sl + 12288, vl, vl, vl, vl);
      sl = sl + SLOT == lb + L_RING + NSLOT * SLOT ? lb + L_RING : sl + SLOT;
    };
    volatile LAS u64* FT = lds_at<u64>(lb + L_FT);
    int pub = 0;
    auto publish = [&](int n) { pub = n > pub ? n : pub; *FT = (u64)(unsigned)pub | (0x7FFFFFFFull << 32); };
    auto wait_free = [&](int i) {
      const int p = i - NSLOT;
      const unsigned dn = lb + L_DONE + 4 * (p & 3), need = (unsigned)(p >> 2) + 1u;
      if (*lds_at<unsigned>(dn) < need) {
        VM0(); publish(i);
        lds_wait_ge(dn, need, t0);
      }
      MEMFENCE();
    };
    unsigned tk;
    const unsigned one = 1u, zero = 0u;
    auto ticket = [&]() {
      u64 sv;
      asm volatile("v_mov_b32 %0, -1\n\ts_mov_b64 %1, exec\n\ts_mov_b64 exec, 1\n\tglobal_atomic_add %0, %2, %3, %4 sc0 sc1\n\ts_mov_b64 exec, %1"
                   : "=&v"(tk), "=&s"(sv) : "v"(zero), "v"(one), "s"(ctr) : "memory");
    };
    const int sb = x * STAT;
    if (LSLEEP) __builtin_amdgcn_s_sleep(LSLEEP);
    STAMP(26);
    int i = 0;
    for (; i < NSLOT; ++i) {
      const unsigned so = (unsigned)(sb + i) * 8192u;
      dma4(ksrd, so, sl, k0, k1, k2, k3); vmwait<PQ>();
      dma4(ksrd, so + 4096, sl + 4096, k0, k1, k2, k3); vmwait<PQ>();
      dma4(vsrd, so, sl + 8192, vl, vl, vl, vl); vmwait<PQ>();
      dma4(vsrd, so + 4096, sl + 12288, vl, vl, vl, vl); vmwait<PQ>();
      sl += SLOT;
      if (i == 1) STAMP(19);
      publish(i);
    }
    sl = lb + L_RING;
    STAMP(24);
    for (; i < STAT - TG; ++i) {
      wait_free(i);
      if (i == NSLOT) STAMP(27);
      fill(sb + i);
      vmwait<16 * INFL>();
      publish(i - INFL + 1);
    }
    STAMP(17);
    int base = sb + i;
    for (;;) {
      wait_free(i);
      ticket();
#pragma unroll
      for (int r = 0; r < TG; ++r) {
        if (r) wait_free(i + r);
        fill(base + r);
        if (r < INFL) vmwait<16 * INFL + 1>(); else vmwait<16 * INFL>();
        publish(i + r - INFL + 1);
      }
      i += TG;
      asm volatile("" : "+v"(tk));
      unsigned t = __builtin_amdgcn_readfirstlane(tk);
      if (t == EMPTY) { VM0(); asm volatile("" : "+v"(tk)); t = __builtin_amdgcn_readfirstlane(tk); }
      if (t >= (unsigned)NTICK) break;
      base = DYN0 + (int)t * TG;
    }
    if (INFL >= 3) { vmwait<32>(); publish(i - 2); }
    if (INFL >= 2) { vmwait<16>(); publish(i - 1); }
    VM0();
    *FT = (u64)(unsigned)i | ((u64)(unsigned)i << 32);
    STAMP(18);
    STAMP_FLUSH();
    return;
  }

  if (wave == 5) {
    __builtin_amdgcn_s_setprio(3);
    const rsrc_t rwm = mkrsrc(a.wm, DM * 3072 * 2), rc = mkrsrc(a.c, DM * 2);
    const int n0 = 512 * (gq & 1) + oc0;
    const int ar = 128 * kq + 4 * (lane >> 1);
    v4u w1[4];
#pragma unroll
    for (int i = 0; i < 4; ++i) w1[i] = ld128<NT>(rwm, (unsigned)((ar + i) * 3072 + n0 + 8 * (lane & 1)) * 2);
    const v2u c4 = ld64<0>(rc, ar * 2);
    const v2u ep = ld64<SC1>(ws, WS_EPOCH);
    const v4u cv = ld128<0>(rc, lane * 16);
    {
      const v4u e4 = {EMPTY, EMPTY, EMPTY, EMPTY};
      unsigned o = OOB;
      if (lane < 33) o = WS_PO + b * REC + lane * 16;
      else if (lane == 34) o = WS_M2 + m2c * 2;
      else if (lane >= 36 && lane < 40) o = WS_YP + (gq * 512 + oc0) * 4 + (lane - 36) * 16;
      st128(ws, o, e4);
      const unsigned d = has_gu ? WS_DP + up * 2048 + lane * 16 : OOB;
      st128(ws, d, e4);
      st128(ws, d + 1024, e4);
      st32(ws, (lane == 0 && x == 0) ? WS_CTR + j * 256 : OOB, 0u);
    }
    STAMP(22);
    {
      float s8[8] = {0.f, 0.f, 0.f, 0.f, 0.f, 0.f, 0.f, 0.f};
      fma8(s8, silu_bf(lo16(c4[0])), w1[0]); fma8(s8, silu_bf(hi16(c4[0])), w1[1]);
      fma8(s8, silu_bf(lo16(c4[1])), w1[2]); fma8(s8, silu_bf(hi16(c4[1])), w1[3]);
      float u0, u1;
      rows_reduce8(s8, u0, u1);
      u0 += dpp<ROR8>(u0); u1 += dpp<ROR8>(u1); u0 += dpp<ROR4>(u0); u1 += dpp<ROR4>(u1); u0 += dpp<XOR2>(u0); u1 += dpp<XOR2>(u1);
      const u64 e1 = (((u64)ep[1] << 32) | ep[0]) + 1ull;
      const unsigned tlo = (unsigned)e1 & 0xFu, thi = (unsigned)(e1 >> 4);
      const int col = n0 + 8 * (lane & 1) + 2 * (lane >> 4);
      VM0();
      STAMP(23);
      st128(ws, (lane & 15) < 2 ? WS_P1 + (kq * 1024 + col) * 8 : OOB, v4u{gr_lo(u0, tlo), thi, gr_lo(u1, tlo), thi});
      *lds_at<unsigned>(lb + L_AUXPUB) = 1u;
    }
    STAMP(20);
    for (unsigned sp = 0; *lds_at<unsigned>(lb + L_FT) < (unsigned)(4 * (WIT0 + grp) + 8); ++sp) { __builtin_amdgcn_s_sleep(8); if ((sp & 63u) == 63u && rt() - t0 > TLIMIT) break; }
    MEMFENCE();
    v4u w2[8];
#pragma unroll
    for (int i = 0; i < 8; ++i) w2[i] = ld128<NT>(rwm, (unsigned)((8 * lane + i) * 3072 + 1024 + m2c) * 2);
    float s8[8] = {0.f, 0.f, 0.f, 0.f, 0.f, 0.f, 0.f, 0.f};
#pragma unroll
    for (int e = 0; e < 4; ++e) { fma8(s8, silu_bf(lo16(cv[e])), w2[2 * e]); fma8(s8, silu_bf(hi16(cv[e])), w2[2 * e + 1]); }
    float u0, u1;
    rows_reduce8(s8, u0, u1);
    u0 = row_sum(u0); u1 = row_sum(u1);
    st32(ws, (lane & 15) == 0 ? WS_M2 + m2c * 2 + (lane >> 4) * 4 : OOB, noempty(cvtpk(u0, u1)));
    STAMP(21);
    STAMP_FLUSH();
    return;
  }

  const int pr = tid >> 1, half = tid & 1, g = lane >> 4, c = lane & 15;
  const rsrc_t rx = mkrsrc(a.x, DM * 2), rwq = mkrsrc(a.wq, DM * 4096 * 2);
  const rsrc_t rwo = mkrsrc(a.wo, 4096 * DM * 2), rwd = mkrsrc(a.wd, DF * DM * 2);
  const int r0 = 128 * kq + 32 * wave;
  v4u wqv[4];
#pragma unroll
  for (int i = 0; i < 4; ++i) wqv[i] = ld128<NT>(rwq, (unsigned)((r0 + 4 * (lane >> 3) + i) * 4096 + 64 * cb + 8 * (lane & 7)) * 2);
  const v2u ep = ld64<SC1>(ws, WS_EPOCH);
  const v4u x8 = ld128<0>(rx, lane * 16);
  const v2u x4 = ld64<0>(rx, pr * 8);
  const unsigned xh = ld32<0>(rx, (r0 + 2 * c) * 2);
  float mb, sd;
  {
    float xs[8];
#pragma unroll
    for (int e = 0; e < 4; ++e) { xs[2 * e] = lo16(x8[e]); xs[2 * e + 1] = hi16(x8[e]); }
    float s = 0.f;
#pragma unroll
    for (int e = 0; e < 8; ++e) s += xs[e];
    const float mean = wave_sum(s) * (1.f / 512.f);
    float v = 0.f;
#pragma unroll
    for (int e = 0; e < 8; ++e) { const float d = xs[e] - mean; v = fmaf(d, d, v); }
    ln_round(mean, wave_sum(v) * (1.f / 512.f), mb, sd);
  }
  float nrow = rbf((lane >= 32 ? hi16(xh) : lo16(xh)) - mb) / sd;
  kpair(wqv[0], wqv[1]); kpair(wqv[2], wqv[3]);
  PIN4(wqv); asm volatile("" : "+v"(nrow));
  const u64 epoch1 = (((u64)ep[1] << 32) | ep[0]) + 1ull;
  const unsigned tlo = (unsigned)epoch1 & 0xFu, thi = (unsigned)(epoch1 >> 4);
  lds_wait_ge(lb + L_AUXPUB, 1u, t0);
  {
    v4u gs, gc;
    for (;;) {
      MEMFENCE();
      gs = ld128<SC1>(ws, WS_P1 + (g * 1024 + r0 + 2 * c) * 8);
      gc = ld128<SC1>(ws, WS_P1 + (g * 1024 + 512 + r0 + 2 * c) * 8);
      if (__all((gr_bad(gs, tlo, thi) | gr_bad(gc, tlo, thi)) == 0u) || rt() - t0 > TLIMIT) break;
      __builtin_amdgcn_s_sleep(PSLEEP);
    }
    STAMP(1);
    const unsigned hp = pack_halves(mod_round(nrow, rbf(gr_sum(gc)), rbf(gr_sum(gs))));
    float acc[8] = {0.f, 0.f, 0.f, 0.f, 0.f, 0.f, 0.f, 0.f};
    dot8(acc, __builtin_amdgcn_ds_bpermute((lane >> 3) * 8, hp), wqv[0], wqv[1]);
    dot8(acc, __builtin_amdgcn_ds_bpermute((lane >> 3) * 8 + 4, hp), wqv[2], wqv[3]);
    float u0, u1;
    rows_reduce8(acc, u0, u1);
    u0 += dpp<ROR8>(u0); u1 += dpp<ROR8>(u1);
    if ((lane & 8) == 0) *lds_pl<f32x2>(lb + L_RED + wave * 256 + (8 * (lane & 7) + 2 * g) * 4) = f32x2{u0, u1};
    LGKM0();
    unsigned old = 0;
    if (lane == 0) old = __hip_atomic_fetch_add(lds_pl<unsigned>(lb + L_QCNT), 1u, __ATOMIC_RELAXED, __HIP_MEMORY_SCOPE_WORKGROUP);
    old = __builtin_amdgcn_readfirstlane(old);
    MEMFENCE();
    if (old == 3u) {
      const unsigned ra = lb + L_RED + lane * 4;
      const float qp = (*lds_pl<float>(ra) + *lds_pl<float>(ra + 256)) + (*lds_pl<float>(ra + 512) + *lds_pl<float>(ra + 768));
      st64(ws, WS_QS + (kq * 4096 + 64 * cb + lane) * 8, v2u{gr_lo(qp, tlo), thi});
      STAMP(2);
    }
  }
  v4u qf[4];
  {
    for (;;) {
      MEMFENCE();
#pragma unroll
      for (int i = 0; i < 4; ++i) qf[i] = ld128<SC1>(ws, WS_QS + (g * 4096 + 128 * j + 32 * i + 2 * c) * 8);
      if (__all((gr_bad(qf[0], tlo, thi) | gr_bad(qf[1], tlo, thi) | gr_bad(qf[2], tlo, thi) | gr_bad(qf[3], tlo, thi)) == 0u) || rt() - t0 > TLIMIT) break;
      __builtin_amdgcn_s_sleep(PSLEEP);
    }
    const unsigned qs = lb + L_QSC + wave * 256;
#pragma unroll
    for (int i = 0; i < 4; ++i) *lds_pl<unsigned>(qs + (16 * i + c) * 4) = pack_halves(rbf(gr_sum(qf[i])));
    LGKM0();
#pragma unroll
    for (int st = 0; st < 4; ++st) qf[st] = *lds_pl<v4u>(qs + (st + 4 * g) * 16);
  }
  STAMP(3);

  const int fc0 = 32 * (x + 8 * kq) + 4 * (j & 7);
  const int r8 = 8 * (tid >> 2);
  v4u wov[4], wgv[8], sbv = {EMPTY, EMPTY, EMPTY, EMPTY}, cbv = {EMPTY, EMPTY, EMPTY, EMPTY};
  unsigned wdv[16];
  v2u gav = {EMPTY, EMPTY}, gbv = {EMPTY, EMPTY};
#pragma unroll
  for (int i = 0; i < 8; ++i) { wov[i & 3] = v4u{0u, 0u, 0u, 0u}; wgv[i] = v4u{0u, 0u, 0u, 0u}; wdv[2 * i] = 0u; wdv[2 * i + 1] = 0u; }
  auto load_m2 = [&]() {
    gav = ld64<SC1>(ws, WS_M2 + pr * 8); sbv = ld128<SC1>(ws, WS_M2 + 1024 + r8 * 2); cbv = ld128<SC1>(ws, WS_M2 + 2048 + r8 * 2);
    gbv = ld64<SC1>(ws, WS_M2 + 3072 + (fc0 & 511) * 2);
  };
  const int iw = wave + 4 * (WIT0 + grp);

  float m2 = -INFINITY, lsum = 0.f;
  f32x4 acc = {0.f, 0.f, 0.f, 0.f};
  {
    const unsigned key = 4 * (c & 3) + (c >> 2);
    unsigned ka[4], msk[4];
#pragma unroll
    for (int st = 0; st < 4; ++st) {
      ka[st] = lb + L_RING + key * 256 + (((st + 4 * g) ^ key) << 4);
      msk[st] = ((c & 7) >> 1) == st ? ((c & 1) ? 0xffff0000u : 0x0000ffffu) : 0u;
    }
    const unsigned va = lb + L_RING + 8192 + lane * 16;
    const bool is_hi = c < 8;
    volatile LAS u64* FT = lds_at<u64>(lb + L_FT);
    volatile LAS unsigned* DN = lds_at<unsigned>(lb + L_DONE + 4 * wave);
    unsigned so = wave * SLOT;
    for (int i = wave;; i += 4) {
      bool more = true;
      for (unsigned sp = 0;; ++sp) {
        const u64 ft = *FT;
        const int full = __builtin_amdgcn_readfirstlane((int)(unsigned)ft), total = __builtin_amdgcn_readfirstlane((int)(unsigned)(ft >> 32));
        if (full > i) break;
        if (i >= total || ((sp & 63u) == 63u && rt() - t0 > TLIMIT)) { more = false; break; }
        __builtin_amdgcn_s_sleep(1);
      }
      if (!more) break;
      MEMFENCE();
      v4u kf[8], vf[8];
#pragma unroll
      for (int st = 0; st < 4; ++st) { kf[st] = *lds_pl<v4u>(ka[st] + so); kf[4 + st] = *lds_pl<v4u>(ka[st] + so + 4096); }
#pragma unroll
      for (int u = 0; u < 8; ++u) vf[u] = *lds_pl<v4u>(va + so + u * 1024);
      LGKM0();
      *DN = (unsigned)(i >> 2) + 1u;
      asm volatile("" : "+v"(kf[0]), "+v"(kf[1]), "+v"(kf[2]), "+v"(kf[3]), "+v"(kf[4]), "+v"(kf[5]), "+v"(kf[6]), "+v"(kf[7]),
                        "+v"(vf[0]), "+v"(vf[1]), "+v"(vf[2]), "+v"(vf[3]), "+v"(vf[4]), "+v"(vf[5]), "+v"(vf[6]), "+v"(vf[7]) :: "memory");
      so += 4 * SLOT; so = so >= NSLOT * SLOT ? so - NSLOT * SLOT : so;
      f32x4 sa = {0.f, 0.f, 0.f, 0.f}, sb4 = {0.f, 0.f, 0.f, 0.f};
#pragma unroll
      for (int st = 0; st < 4; ++st) { sa = mfma(kf[st], qf[st], sa); sb4 = mfma(kf[4 + st], qf[st], sb4); }
      float s[8] = {sa[0], sa[1], sa[2], sa[3], sb4[0], sb4[1], sb4[2], sb4[3]};
#pragma unroll
      for (int u = 0; u < 4; ++u) {
        const unsigned p1 = cvtpk(s[2 * u], s[2 * u + 1]);
        const unsigned p2 = cvtpk(lo16(p1) * SCALE, hi16(p1) * SCALE);
        s[2 * u] = lo16(p2); s[2 * u + 1] = hi16(p2);
      }
      const float mx = xrow_max(fmaxf(fmaxf(fmaxf(s[0], s[1]), fmaxf(s[2], s[3])), fmaxf(fmaxf(s[4], s[5]), fmaxf(s[6], s[7]))));
      const float mn = fmaxf(m2, mx * LOG2E);
      const float alpha = __builtin_amdgcn_exp2f(m2 - mn);
      m2 = mn;
      acc *= alpha;
      float ps = 0.f;
#pragma unroll
      for (int u = 0; u < 8; ++u) {
        const float p = __builtin_amdgcn_exp2f(fmaf(s[u], LOG2E, -mn));
        ps += p;
        const float ph = hi16(asu(p));
        const float xv = is_hi ? ph : p - ph;
        const unsigned pk = cvtpk(xv, xv);
        const v4u pa = {pk & msk[0], pk & msk[1], pk & msk[2], pk & msk[3]};
        acc = mfma(pa, vf[u], acc);
      }
      lsum = fmaf(lsum, alpha, ps);
      MEMFENCE();
      if (i == iw) {
#pragma unroll
        for (int u = 0; u < 4; ++u) wov[u] = ld128<NT>(rwo, (unsigned)((512 * gq + 4 * pr + u) * DM + oc0 + 8 * half) * 2);
        if (has_gu) {
          const GAS v4u* gp = (const GAS v4u*)(((tid & 2) ? a.wu : a.wg) + (r8 * DF + 16 * up + 8 * half));
#pragma unroll
          for (int u = 0; u < 8; ++u) wgv[u] = __builtin_nontemporal_load(gp + u * (DF / 8));
#pragma unroll
          for (int u = 0; u < 16; ++u) wdv[u] = ld32<NT>(rwd, (unsigned)((16 * up + u) * DM + 2 * tid) * 2);
        }
      }
      if (i == iw + 16) {
        kpair(wov[0], wov[1]); kpair(wov[2], wov[3]);
#pragma unroll
        for (int u = 0; u < 8; u += 2) kpair(wgv[u], wgv[u + 1]);
#pragma unroll
        for (int u = 0; u < 16; u += 2) {
          const unsigned lo = __builtin_amdgcn_perm(wdv[u + 1], wdv[u], 0x05040100u), hi = __builtin_amdgcn_perm(wdv[u + 1], wdv[u], 0x07060302u);
          wdv[u] = lo; wdv[u + 1] = hi;
        }
      }
      if (i == wave + 4 * M2IT && j < 16) load_m2();
      MEMFENCE();
    }
  }
  STAMP(4);
  {
    f32x4 o;
#pragma unroll
    for (int r = 0; r < 4; ++r) o[r] = sw32_add(acc[r], acc[r]);
    float l = sw16_add(lsum, lsum); l = sw32_add(l, l);
    const unsigned rec = lb + L_MRG + wave * 528;
    if (lane < 32) *lds_pl<f32x4>(rec + 16 + (8 * c + 4 * g) * 4) = o;
    if (lane == 0) *lds_pl<f32x2>(rec) = f32x2{m2, l};
    wg_barrier();
    if (wave == 0) {
      f32x2 ml[4];
#pragma unroll
      for (int w = 0; w < 4; ++w) ml[w] = *lds_pl<f32x2>(lb + L_MRG + w * 528);
      const float M = fmaxf(fmaxf(ml[0][0], ml[1][0]), fmaxf(ml[2][0], ml[3][0]));
      float L = 0.f; f32x4 O = {0.f, 0.f, 0.f, 0.f};
#pragma unroll
      for (int w = 0; w < 4; ++w) {
        const float wt = __builtin_amdgcn_exp2f(ml[w][0] - M);
        L = fmaf(wt, ml[w][1], L);
        O += wt * *lds_pl<f32x4>(lb + L_MRG + w * 528 + 16 + (lane & 31) * 16);
      }
      if (lane >= 32) O = f32x4{M, L, 0.f, 0.f};
      st128(ws, lane < 33 ? WS_PO + b * REC + lane * 16 : OOB, noempty4(O));
      STAMP(5);
    }
  }
  {
    const unsigned hb = WS_PO + (4 * gq + wave) * 8 * REC;
    v4u po[8];
    v2u ml = {0u, 0u};
#pragma unroll
    for (int s = 0; s < 8; ++s) po[s] = v4u{0u, 0u, 0u, 0u};
    unsigned miss = 0x1FFu;
    for (;;) {
      MEMFENCE();
      if (miss & 0x100u) ml = ld64<SC1>(ws, lane < 8 ? hb + lane * REC + 512 : OOB);
#pragma unroll
      for (int s = 0; s < 8; ++s) if (miss & (1u << s)) po[s] = ld128<SC1>(ws, hb + s * REC + (lane >> 1) * 16);
      unsigned nm = __all(mx2(0u, ml) != EMPTY) ? 0u : 0x100u;
#pragma unroll
      for (int s = 0; s < 8; ++s) nm |= __all(mx4(0u, po[s]) != EMPTY) ? 0u : (1u << s);
      miss = nm;
      if (miss == 0u || rt() - t0 > TLIMIT) break;
      __builtin_amdgcn_s_sleep(PSLEEP);
    }
    STAMP(6);
    const float ms = lane < 8 ? asf(ml[0]) : -INFINITY;
    const float wt = __builtin_amdgcn_exp2f(ms - row_max(ms));
    const float wn = wt * __builtin_amdgcn_rcpf(row_sum(wt * asf(ml[1])));
    float o[4] = {0.f, 0.f, 0.f, 0.f};
#pragma unroll
    for (int s = 0; s < 8; ++s) {
      const float w = asf(__builtin_amdgcn_readlane(asu(wn), s));
#pragma unroll
      for (int r = 0; r < 4; ++r) o[r] = fmaf(w, asf(po[s][r]), o[r]);
    }
    float y[8] = {0.f, 0.f, 0.f, 0.f, 0.f, 0.f, 0.f, 0.f};
    dot8(y, cvtpk(o[0], o[1]), wov[0], wov[1]);
    dot8(y, cvtpk(o[2], o[3]), wov[2], wov[3]);
    float u0, u1;
    rows_reduce8(y, u0, u1);
    u0 += dpp<ROR8>(u0); u1 += dpp<ROR8>(u1); u0 += dpp<ROR4>(u0); u1 += dpp<ROR4>(u1); u0 += dpp<XOR2>(u0); u1 += dpp<XOR2>(u1);
    if (c < 2) *lds_pl<f32x2>(lb + L_RED2 + wave * 64 + (8 * half + 2 * g) * 4) = f32x2{u0, u1};
    wg_barrier();
    if (wave == 0) {
      const unsigned ra = lb + L_RED2 + (lane & 3) * 16;
      const f32x4 t = (*lds_pl<f32x4>(ra) + *lds_pl<f32x4>(ra + 64)) + (*lds_pl<f32x4>(ra + 128) + *lds_pl<f32x4>(ra + 192));
      st128(ws, lane < 4 ? WS_YP + (gq * 512 + oc0) * 4 + lane * 16 : OOB, noempty4(t));
      STAMP(7);
    }
  }
  if (j >= 16) { STAMP_FLUSH(); return; }
  {
    while (!__all(mx4(mx4(mx2(mx2(0u, gav), gbv), sbv), cbv) != EMPTY) && rt() - t0 <= TLIMIT) { MEMFENCE(); load_m2(); }
    v4u yp[8];
#pragma unroll
    for (int s = 0; s < 8; ++s) yp[s] = v4u{0u, 0u, 0u, 0u};
    unsigned miss = 0xFFu;
    for (;;) {
      MEMFENCE();
#pragma unroll
      for (int s = 0; s < 8; ++s) if (miss & (1u << s)) yp[s] = ld128<SC1>(ws, WS_YP + s * 2048 + pr * 16);
      unsigned nm = 0u;
#pragma unroll
      for (int s = 0; s < 8; ++s) nm |= __all(mx4(0u, yp[s]) != EMPTY) ? 0u : (1u << s);
      miss = nm;
      if (miss == 0u || rt() - t0 > TLIMIT) break;
      __builtin_amdgcn_s_sleep(PSLEEP);
    }
    STAMP(8);
    const float xr[4] = {lo16(x4[0]), hi16(x4[0]), lo16(x4[1]), hi16(x4[1])};
    const float ga[4] = {lo16(gav[0]), hi16(gav[0]), lo16(gav[1]), hi16(gav[1])};
    float x1[4];
#pragma unroll
    for (int r = 0; r < 4; ++r) {
      const float at = ((asf(yp[0][r]) + asf(yp[1][r])) + (asf(yp[2][r]) + asf(yp[3][r]))) + ((asf(yp[4][r]) + asf(yp[5][r])) + (asf(yp[6][r]) + asf(yp[7][r])));
      x1[r] = rbf(xr[r] + rbf(ga[r] * rbf(at)));
    }
    const float mw = wave_sum((x1[0] + x1[1]) + (x1[2] + x1[3])) * (1.f / 256.f);
    float q2 = 0.f;
#pragma unroll
    for (int r = 0; r < 4; ++r) { const float d = x1[r] - mw; q2 = fmaf(d, d, q2); }
    q2 = wave_sum(q2) * 0.5f;
    if (lane == 0) *lds_pl<f32x2>(lb + L_ST + wave * 8) = f32x2{mw, q2};
    if (!half) *lds_pl<f32x4>(lb + L_X1 + pr * 16) = f32x4{x1[0], x1[1], x1[2], x1[3]};
    wg_barrier();
    STAMP(12);
    if (b == 0 && tid == 0) st64(ws, WS_EPOCH, v2u{(unsigned)epoch1, (unsigned)(epoch1 >> 32)});
    if (has_gu) {
      const f32x4 sA = *lds_pl<f32x4>(lb + L_ST), sB = *lds_pl<f32x4>(lb + L_ST + 16);
      const f32x4 xa = *lds_pl<f32x4>(lb + L_X1 + r8 * 4), xb = *lds_pl<f32x4>(lb + L_X1 + r8 * 4 + 16);
      const float mean = ((sA[0] + sA[2]) + (sB[0] + sB[2])) * 0.25f;
      const float d0 = sA[0] - mean, d1 = sA[2] - mean, d2 = sB[0] - mean, d3 = sB[2] - mean;
      const float var = (((sA[1] + sA[3]) + (sB[1] + sB[3])) + 128.f * ((d0 * d0 + d1 * d1) + (d2 * d2 + d3 * d3))) * (1.f / 512.f);
      float mb2, sd2;
      ln_round(mean, var, mb2, sd2);
      const float rs = 1.f / sd2;
      float y[8] = {0.f, 0.f, 0.f, 0.f, 0.f, 0.f, 0.f, 0.f};
#pragma unroll
      for (int e = 0; e < 4; ++e) {
        const float xl = e < 2 ? xa[2 * e] : xb[2 * e - 4], xu = e < 2 ? xa[2 * e + 1] : xb[2 * e - 3];
        const float h0 = mod_round(rbf(xl - mb2) * rs, lo16(cbv[e]), lo16(sbv[e])), h1 = mod_round(rbf(xu - mb2) * rs, hi16(cbv[e]), hi16(sbv[e]));
        dot8(y, __builtin_amdgcn_perm(asu(h1), asu(h0), 0x07060302u), wgv[2 * e], wgv[2 * e + 1]);
      }
      float u0, u1;
      rows_reduce8(y, u0, u1);
      u0 += dpp<ROR8>(u0); u1 += dpp<ROR8>(u1); u0 += dpp<ROR4>(u0); u1 += dpp<ROR4>(u1);
      if (c < 4) *lds_pl<f32x2>(lb + L_RED3 + wave * 128 + (8 * c + 2 * g) * 4) = f32x2{u0, u1};
      wg_barrier();
      const unsigned ra = lb + L_RED3 + c * 4;
      const float gs = rbf((*lds_pl<float>(ra) + *lds_pl<float>(ra + 128)) + (*lds_pl<float>(ra + 256) + *lds_pl<float>(ra + 384)));
      const float us = rbf((*lds_pl<float>(ra + 64) + *lds_pl<float>(ra + 192)) + (*lds_pl<float>(ra + 320) + *lds_pl<float>(ra + 448)));
      const float z = 0.7978845608028654f * fmaf(0.044715f * gs * gs, gs, gs);
      const float th = 1.f - 2.f / (1.f + __expf(2.f * z));
      const float h2 = rbf(rbf(0.5f * gs * (1.f + th)) * us);
      const unsigned hp = __builtin_amdgcn_perm(asu(dpp<XOR1>(h2)), asu(h2), 0x07060302u);
      float d0a = 0.f, d1a = 0.f;
#pragma unroll
      for (int e = 0; e < 16; e += 2) {
        const unsigned he = __builtin_amdgcn_readlane(hp, e);
        d0a = dot2(wdv[e], he, d0a); d1a = dot2(wdv[e + 1], he, d1a);
      }
      st64(ws, WS_DP + up * 2048 + tid * 8, v2u{noempty(asu(d0a)), noempty(asu(d1a))});
      STAMP(9);
    }
  }
  if (wave != 0) { STAMP_FLUSH(); return; }
  {
    v4u da = {EMPTY, 0u, 0u, 0u}, db = {lane < NGU - 64 ? EMPTY : 0u, 0u, 0u, 0u};
    for (;;) {
      MEMFENCE();
      const bool ma = mx4(0u, da) == EMPTY, mb_ = mx4(0u, db) == EMPTY;
      if (__all(!ma && !mb_) || rt() - t0 > TLIMIT) break;
      const v4u na = ld128<SC1>(ws, ma ? WS_DP + lane * 2048 + fc0 * 4 : OOB), nb = ld128<SC1>(ws, mb_ ? WS_DP + (lane + 64) * 2048 + fc0 * 4 : OOB);
      da = ma ? na : da; db = mb_ ? nb : db;
    }
    STAMP(10);
    float t4[4];
#pragma unroll
    for (int r = 0; r < 4; ++r) t4[r] = asf(da[r]) + asf(db[r]);
    const float r = row_sum(sw16_add(sw32_add(t4[0], t4[2]), sw32_add(t4[1], t4[3])));
    const unsigned gw = g < 2 ? gbv[0] : gbv[1];
    const float gbf = (g & 1) ? hi16(gw) : lo16(gw);
    const float res = *lds_pl<float>(lb + L_X1 + (fc0 + g) * 4) + rbf(gbf * rbf(r));
    if (c == 0) a.out[fc0 + g] = (u16)(cvtpk(res, res) & 0xffffu);
    STAMP(11);
    STAMP_FLUSH();
  }
}
}

extern "C" void kernel_launch(void* const* d_in, const int* in_sizes, int n_in, void* d_out, int out_size, void* d_ws, size_t ws_size,
                              hipStream_t stream) {
  Args a;
  a.x = (const u16*)d_in[0]; a.K = (const u16*)d_in[1]; a.V = (const u16*)d_in[2]; a.wq = (const u16*)d_in[3]; a.wo = (const u16*)d_in[4];
  a.wg = (const u16*)d_in[6]; a.wu = (const u16*)d_in[7]; a.wd = (const u16*)d_in[8]; a.c = (const u16*)d_in[9]; a.wm = (const u16*)d_in[10];
  a.out = (u16*)d_out; a.ws = (unsigned char*)d_ws;
  (void)hipFuncSetAttribute((const void*)block_decode, hipFuncAttributeMaxDynamicSharedMemorySize, (int)LDS_BYTES);
  hipLaunchKernelGGL(block_decode, dim3(NWG), dim3(NTHR), LDS_BYTES, stream, a);
}
```

```cpp
#include <hip/hip_runtime.h>
#include <hip/hip_bf16.h>

#define GAS __attribute__((address_space(1)))
#define LAS __attribute__((address_space(3)))
typedef unsigned short u16;
typedef unsigned long long u64;
typedef unsigned v4u __attribute__((ext_vector_type(4)));
typedef unsigned v2u __attribute__((ext_vector_type(2)));
typedef int v4i __attribute__((ext_vector_type(4)));
typedef float f32x4 __attribute__((ext_vector_type(4)));
typedef __bf16 bf16x8 __attribute__((ext_vector_type(8)));
typedef __bf16 bf16x2 __attribute__((ext_vector_type(2)));
typedef float f32x2 __attribute__((ext_vector_type(2)));

#define DBG_TS 0
#define PQ 16
#define LSLEEP 6
#define NSLOT 9
#define TW 10800
#define TD 110
#define BWAIT 0
#define NSTASH 3
#define BURST 4
#define USEPQ 1
#define PRETOUCH 0
#define NOORDER 0
#define P1WAIT 24
#define P1SLP 0
#define QWAIT 0
#define QSLP 0
#define T4 48
#define POFF 6
#define PSLEEP 1
#define CSLEEP 1
#define XP_NOW 0
#define STAT 224

namespace {
constexpr int DM = 512, SK = 65536, DH = 128, DF = 1792;
constexpr int NWG = 256, NTHR = 384;
constexpr int SLOT = 16384, TILES_H = SK / 32;
constexpr int DYN0 = 8 * STAT, DYN2 = DYN0 + 4 * T4, NTICK = T4 + (TILES_H - DYN2) / 2;
static_assert(DYN2 <= TILES_H && (TILES_H - DYN2) % 2 == 0 && STAT % 4 == 0 && PQ <= 16, "tile plan");
constexpr unsigned HEAD_BYTES = (unsigned)SK * DH * 2;
constexpr unsigned EMPTY = 0xFFFFFFFFu;
constexpr unsigned OOB = 0x7FFF0000u;
constexpr u64 TLIMIT = 5000000ull;
constexpr float LOG2E = 1.4426950408889634f, SCALE = 0.08838834764831845f;
constexpr int NGU = DF / 16, REC = 640;
constexpr unsigned WS_EPOCH = 0, WS_P1 = 4096, WS_QS = WS_P1 + 32768, WS_M2P = WS_QS + 131072, WS_M2 = WS_M2P + 65536, WS_CTR = WS_M2 + 4096,
                   WS_CLR = WS_CTR + 8192, WS_YP = WS_CLR + 2048, WS_PO = WS_YP + 16384, WS_DP = WS_PO + NWG * REC, WS_END = WS_DP + NGU * 2048,
                   WS_DBG = 1u << 20;
constexpr unsigned L_RING = 0, L_CTL = NSLOT * SLOT, L_FT = L_CTL, L_DONE = L_CTL + 16, L_QCNT = L_CTL + 32, L_AUXPUB = L_CTL + 36,
                   L_QIN = L_CTL + 40, L_ISSUED = L_CTL + 44, L_M2RDY = L_CTL + 48, L_TKC = L_CTL + 52, L_TK = L_CTL + 56,
                   L_RED = L_CTL + 64, L_QSC = L_RED + 1024, L_RED3 = L_QSC + 256, L_ST = L_RED3 + 512,
                   L_X1 = L_ST + 64, L_M2 = L_X1 + 2048, L_MRG = L_M2 + 4096, L_END = L_MRG + 4 * 528;
constexpr unsigned LDS_BYTES = L_END + (DBG_TS ? 2048 : 0);
static_assert(LDS_BYTES <= 160 * 1024 && LDS_BYTES > 80 * 1024, "one workgroup per CU");

struct Args {
  const u16 *x, *K, *V, *wq, *wo, *wg, *wu, *wd, *c, *wm;
  u16* out;
  unsigned char* ws;
};

__device__ __forceinline__ u64 rt() { return __builtin_amdgcn_s_memrealtime(); }
__device__ __forceinline__ float asf(unsigned u) { return __builtin_bit_cast(float, u); }
__device__ __forceinline__ unsigned asu(float f) { return __builtin_bit_cast(unsigned, f); }
__device__ __forceinline__ float lo16(unsigned u) { return asf(u << 16); }
__device__ __forceinline__ float hi16(unsigned u) { return asf(u & 0xffff0000u); }
__device__ __forceinline__ unsigned cvtpk(float a, float b) {
  const f32x2 v = {a, b};
  return __builtin_bit_cast(unsigned, __builtin_convertvector(v, bf16x2));
}
__device__ __forceinline__ float rbf(float f) { return lo16(cvtpk(f, f)); }
__device__ __forceinline__ unsigned noempty(unsigned v) { return v == EMPTY ? 0x7FC07FC0u : v; }
__device__ __forceinline__ float dot2(unsigned a, unsigned b, float c) {
  return __builtin_amdgcn_fdot2_f32_bf16(__builtin_bit_cast(bf16x2, a), __builtin_bit_cast(bf16x2, b), c, false);
}
template <int CTRL> __device__ __forceinline__ float dpp(float v) {
  return asf(__builtin_amdgcn_mov_dpp(asu(v), CTRL, 0xf, 0xf, true));
}
constexpr int ROR8 = 0x128, ROR4 = 0x124, ROR2 = 0x122, ROR1 = 0x121, XOR2 = 0x4E, XOR1 = 0xB1;
template <int CTRL> __device__ __forceinline__ unsigned dppu(unsigned v) { return __builtin_amdgcn_mov_dpp(v, CTRL, 0xf, 0xf, true); }
__device__ __forceinline__ float sw32_add(float a, float b) {
  const auto r = __builtin_amdgcn_permlane32_swap(asu(a), asu(b), false, false);
  return asf(r[0]) + asf(r[1]);
}
__device__ __forceinline__ float sw16_add(float a, float b) {
  const auto r = __builtin_amdgcn_permlane16_swap(asu(a), asu(b), false, false);
  return asf(r[0]) + asf(r[1]);
}
__device__ __forceinline__ float xrow_max(float v) {
  const auto r = __builtin_amdgcn_permlane16_swap(asu(v), asu(v), false, false);
  v = fmaxf(asf(r[0]), asf(r[1]));
  const auto s = __builtin_amdgcn_permlane32_swap(asu(v), asu(v), false, false);
  return fmaxf(asf(s[0]), asf(s[1]));
}
__device__ __forceinline__ unsigned pack_halves(float v) {
  const auto r = __builtin_amdgcn_permlane32_swap(asu(v), asu(v), false, false);
  return __builtin_amdgcn_perm(r[1], r[0], 0x07060302u);
}
__device__ __forceinline__ float row_sum(float v) { v += dpp<ROR8>(v); v += dpp<ROR4>(v); v += dpp<ROR2>(v); v += dpp<ROR1>(v); return v; }
__device__ __forceinline__ float row_max(float v) { v = fmaxf(v, dpp<ROR8>(v)); v = fmaxf(v, dpp<ROR4>(v)); v = fmaxf(v, dpp<ROR2>(v)); return fmaxf(v, dpp<ROR1>(v)); }
__device__ __forceinline__ float wave_sum(float v) { v = row_sum(v); v = sw16_add(v, v); return sw32_add(v, v); }
__device__ __forceinline__ void rows_reduce8(const float (&a)[8], float& u0, float& u1) {
  const float r0 = sw32_add(a[0], a[4]), r1 = sw32_add(a[1], a[5]), r2 = sw32_add(a[2], a[6]), r3 = sw32_add(a[3], a[7]);
  u0 = sw16_add(r0, r2); u1 = sw16_add(r1, r3);
}
__device__ __forceinline__ void fma8(float (&a)[8], float h, v4u w) {
#pragma unroll
  for (int e = 0; e < 4; ++e) { a[2 * e] = fmaf(h, lo16(w[e]), a[2 * e]); a[2 * e + 1] = fmaf(h, hi16(w[e]), a[2 * e + 1]); }
}
__device__ __forceinline__ void kpair(v4u& a, v4u& b) {
  const v4u r0 = a, r1 = b;
  a = v4u{__builtin_amdgcn_perm(r1[0], r0[0], 0x05040100u), __builtin_amdgcn_perm(r1[0], r0[0], 0x07060302u),
          __builtin_amdgcn_perm(r1[1], r0[1], 0x05040100u), __builtin_amdgcn_perm(r1[1], r0[1], 0x07060302u)};
  b = v4u{__builtin_amdgcn_perm(r1[2], r0[2], 0x05040100u), __builtin_amdgcn_perm(r1[2], r0[2], 0x07060302u),
          __builtin_amdgcn_perm(r1[3], r0[3], 0x05040100u), __builtin_amdgcn_perm(r1[3], r0[3], 0x07060302u)};
}
__device__ __forceinline__ void dot8(float (&acc)[8], unsigned hp, v4u a, v4u b) {
#pragma unroll
  for (int e = 0; e < 4; ++e) { acc[e] = dot2(a[e], hp, acc[e]); acc[4 + e] = dot2(b[e], hp, acc[4 + e]); }
}
__device__ __forceinline__ f32x4 mfma(v4u a, v4u b, f32x4 c) {
  return __builtin_amdgcn_mfma_f32_16x16x32_bf16(__builtin_bit_cast(bf16x8, a), __builtin_bit_cast(bf16x8, b), c, 0, 0, 0);
}
typedef __amdgpu_buffer_rsrc_t rsrc_t;
__device__ __forceinline__ rsrc_t mkrsrc(const void* p, unsigned bytes) { return __builtin_amdgcn_make_buffer_rsrc((void*)p, 0, bytes, 0x00020000); }
constexpr int SC1 = 16, NT = 2;
template <int AUX> __device__ __forceinline__ v4u ld128(rsrc_t r, unsigned off) { return __builtin_amdgcn_raw_buffer_load_b128(r, off, 0, AUX); }
template <int AUX> __device__ __forceinline__ v2u ld64(rsrc_t r, unsigned off) { return __builtin_amdgcn_raw_buffer_load_b64(r, off, 0, AUX); }
template <int AUX> __device__ __forceinline__ unsigned ld32(rsrc_t r, unsigned off) { return __builtin_amdgcn_raw_buffer_load_b32(r, off, 0, AUX); }
__device__ __forceinline__ void st128(rsrc_t r, unsigned off, v4u v) { __builtin_amdgcn_raw_buffer_store_b128(v, r, off, 0, SC1); }
__device__ __forceinline__ void st64(rsrc_t r, unsigned off, v2u v) { __builtin_amdgcn_raw_buffer_store_b64(v, r, off, 0, SC1); }
__device__ __forceinline__ void st32(rsrc_t r, unsigned off, unsigned v) { __builtin_amdgcn_raw_buffer_store_b32(v, r, off, 0, SC1); }
__device__ __forceinline__ unsigned umax(unsigned a, unsigned b) { return a > b ? a : b; }
__device__ __forceinline__ unsigned mx4(unsigned m, v4u v) { return umax(umax(umax(m, v[0]), v[1]), umax(v[2], v[3])); }
__device__ __forceinline__ unsigned mx2(unsigned m, v2u v) { return umax(umax(m, v[0]), v[1]); }
__device__ __forceinline__ v4u noempty4(f32x4 v) { return v4u{noempty(asu(v[0])), noempty(asu(v[1])), noempty(asu(v[2])), noempty(asu(v[3]))}; }
__device__ __forceinline__ unsigned gr_lo(float v, unsigned tlo) { return ((asu(v) + 8u) & 0xFFFFFFF0u) | tlo; }
__device__ __forceinline__ unsigned gr_bad(v4u g, unsigned tlo, unsigned thi) { return (((g[0] ^ tlo) | (g[2] ^ tlo)) & 0xFu) | (g[1] ^ thi) | (g[3] ^ thi); }
__device__ __forceinline__ float gr_sum(v4u g) { const float s = sw32_add(asf(g[0] & 0xFFFFFFF0u), asf(g[2] & 0xFFFFFFF0u)); return sw16_add(s, s); }
__device__ __forceinline__ v4u sel4(bool t, v4u a, v4u b) { return v4u{t ? a[0] : b[0], 0u, t ? a[2] : b[2], 0u}; }
#define MEMFENCE() asm volatile("" ::: "memory")
#define LGKM0() asm volatile("s_waitcnt lgkmcnt(0)" ::: "memory")
#define VM0() asm volatile("s_waitcnt vmcnt(0)" ::: "memory")
#define PIN4(a) asm volatile("" : "+v"((a)[0]), "+v"((a)[1]), "+v"((a)[2]), "+v"((a)[3]))
template <int N> __device__ __forceinline__ void vmwait() { asm volatile("s_waitcnt vmcnt(%0)" ::"n"(N) : "memory"); }
__device__ __forceinline__ void wg_barrier() { LGKM0(); __builtin_amdgcn_s_barrier(); MEMFENCE(); }
template <typename T> __device__ __forceinline__ volatile LAS T* lds_at(unsigned off) { return (volatile LAS T*)(size_t)off; }
template <typename T> __device__ __forceinline__ LAS T* lds_pl(unsigned off) { return (LAS T*)(size_t)off; }
__device__ __forceinline__ void lds_wait_ge(unsigned addr, unsigned need, u64 t0) {
  for (unsigned sp = 0; *lds_at<unsigned>(addr) < need; ++sp) { __builtin_amdgcn_s_sleep(1); if ((sp & 63u) == 63u && rt() - t0 > TLIMIT) break; }
  MEMFENCE();
}

#define STAMP(k) do {} while (0)
#define FSTAMP(k) do {} while (0)
#define STAMPC(k) do {} while (0)
#define STAMP_INIT() do {} while (0)
#define STAMP_FLUSH() do {} while (0)

__device__ __forceinline__ void dma4(v4i srd, unsigned soff, unsigned ldsb, unsigned v0, unsigned v1, unsigned v2, unsigned v3) {
  asm volatile("s_mov_b32 m0, %0\n\ts_nop 0\n\t"
               "buffer_load_dwordx4 %1, %5, %6 offen nt lds\n\t"
               "buffer_load_dwordx4 %2, %5, %6 offen offset:1024 nt lds\n\t"
               "buffer_load_dwordx4 %3, %5, %6 offen offset:2048 nt lds\n\t"
               "buffer_load_dwordx4 %4, %5, %6 offen offset:3072 nt lds"
               ::"s"(ldsb), "v"(v0), "v"(v1), "v"(v2), "v"(v3), "s"(srd), "s"(soff) : "memory");
}

__device__ __forceinline__ float silu_bf(float v) { return rbf(v / (1.f + __expf(-v))); }
__device__ __forceinline__ void ln_round(float mean, float var, float& mb, float& sd) {
  mb = rbf(mean);
  sd = rbf(__builtin_sqrtf(rbf(rbf(var) + 1e-5f)));
}
__device__ __forceinline__ float mod_round(float n, float sc, float sh) { return rbf(rbf(rbf(n) * rbf(1.f + sc)) + sh); }

__global__ void __launch_bounds__(NTHR, 2) block_decode(Args a) {
  extern __shared__ __attribute__((aligned(16))) unsigned char smem[];
  const int tid = threadIdx.x, lane = tid & 63, wave = __builtin_amdgcn_readfirstlane(tid >> 6);
  const int b = blockIdx.x, x = b & 7, j = b >> 3;
  const u64 t0 = rt();
  const unsigned lb = (unsigned)(size_t)(LAS unsigned char*)smem;
  const rsrc_t ws = mkrsrc(a.ws, WS_END);
  const int kq = j >> 3, cb = x + 8 * (j & 7);
  const int gq = j >> 2, rk = 4 * (gq >> 1) + (j & 3);
  const bool mlp = ((x ^ gq) & 1) == 0;
  const int og = rk >> 1, oc0 = 64 * x + 32 * (rk & 1);
  const bool has_gu = mlp && 4 * gq + (x >> 1) < 28;
  const int up = 4 * (4 * gq + (x >> 1)) + (j & 3);
  const int fc0 = 64 * x + 4 * rk;

  if (tid < 16) *lds_pl<unsigned>(lb + L_CTL + 4 * tid) = (tid == 1) ? 0x7FFFFFFFu : 0u;
  asm volatile("" :: "s"(a.K), "s"(a.ws));
  STAMP_INIT();
  wg_barrier();
  STAMP(0);

  int w_a = wave, w_b = wave;
  asm volatile("" : "+s"(w_a));
  if (w_a == 4) {
    const u64 kb = (u64)(a.K + (size_t)j * SK * DH), vb = (u64)(a.V + (size_t)j * SK * DH);
    const v4i ksrd = {(int)kb, (int)(kb >> 32), (int)HEAD_BYTES, 0x00020000}, vsrd = {(int)vb, (int)(vb >> 32), (int)HEAD_BYTES, 0x00020000};
    const unsigned g = lane >> 4, c = lane & 15;
    const unsigned k0 = g * 256 + ((c ^ g) << 4), k1 = g * 256 + ((c ^ (4 + g)) << 4), k2 = g * 256 + ((c ^ (8 + g)) << 4), k3 = g * 256 + ((c ^ (12 + g)) << 4);
    const unsigned vl = lane * 16;
    unsigned sl = lb + L_RING;
    unsigned vo1 = OOB, vo2 = OOB;
    auto fill = [&](unsigned ko, int pq) {
      v4i kd = ksrd, vd = vsrd;
      kd[2] = ko == OOB ? 0 : (int)HEAD_BYTES; vd[2] = vo2 == OOB ? 0 : (int)HEAD_BYTES;
      const unsigned kso = ko == OOB ? 0u : ko, vso = vo2 == OOB ? 0u : vo2;
      dma4(kd, kso, sl, k0, k1, k2, k3); if (pq) vmwait<PQ>();
      dma4(kd, kso + 32768, sl + 4096, k0, k1, k2, k3); if (pq) vmwait<PQ>();
      dma4(vd, vso, sl + 8192, vl, vl, vl, vl); if (pq) vmwait<PQ>();
      dma4(vd, vso + 32768, sl + 12288, vl, vl, vl, vl);
      vo2 = vo1; vo1 = ko;
      sl = sl + SLOT == lb + L_RING + NSLOT * SLOT ? lb + L_RING : sl + SLOT;
    };
    auto toff = [](int t) { return ((unsigned)t >> 3) * 65536u + ((unsigned)t & 7u) * 4096u; };
    volatile LAS u64* FT = lds_at<u64>(lb + L_FT);
    int pub = 0;
    unsigned total = 0x7FFFFFFFu;
    auto publish = [&](int n) { pub = n > pub ? n : pub; *FT = (u64)(unsigned)pub | ((u64)total << 32); };
    auto wait_free = [&](int i) {
      const int p = i - NSLOT;
      volatile LAS u64* dn = lds_at<u64>(lb + L_DONE + 8 * (p & 1));
      const unsigned need = (unsigned)(p >> 1) + 1u;
      auto freed = [&]() { const u64 d = *dn; return (unsigned)d >= need && (unsigned)(d >> 32) >= need; };
      if (!freed()) {
        VM0(); publish(i);
        for (unsigned sp = 0; !freed(); ++sp) { __builtin_amdgcn_s_sleep(1); if ((sp & 63u) == 63u && rt() - t0 > TLIMIT) break; }
      }
      MEMFENCE();
    };
    const int sb = x * STAT;
    for (unsigned sp = 0; NOORDER == 0 && *lds_at<unsigned>(lb + L_ISSUED) < 5u && sp < 4096u; ++sp) {}
    STAMP(26);
    unsigned ko = toff(sb), tn = 1;
    int next = 0, left = 0;
    for (int i = 0;; ++i) {
      fill(ko, USEPQ && i >= BURST && i < NSLOT);
      if (i >= BURST) publish(i - 1);
      if (i + 1 < 28) FSTAMP(i + 1);
      if (i == 2) STAMP(19);
      if (i == NSLOT - 1) STAMP(24);
      if (i == NSLOT) STAMP(27);
      if (i == 24) STAMP(13); if (i == 56) STAMP(14); if (i == 88) STAMP(15); if (i == 120) STAMP(16); if (i == 152) STAMP(23); if (i == 184) STAMP(25);
      if (i + 1 < STAT) {
        ko = toff(sb + i + 1);
        if (i == STAT - 5) { *lds_at<unsigned>(lb + L_TKC) = 1u; STAMP(17); }
      } else if (total == 0x7FFFFFFFu) {
        if (left == 0) {
          u64 tv;
          for (unsigned sp = 0; (unsigned)((tv = *lds_at<u64>(lb + L_TK)) >> 32) < tn; ++sp) if ((sp & 63u) == 63u && rt() - t0 > TLIMIT) { tv = NTICK; break; }
          const unsigned t = __builtin_amdgcn_readfirstlane((unsigned)tv);
          if (t < (unsigned)NTICK) {
            *lds_at<unsigned>(lb + L_TKC) = ++tn;
            left = t < (unsigned)T4 ? 4 : 2;
            next = t < (unsigned)T4 ? DYN0 + 4 * (int)t : DYN2 + 2 * ((int)t - T4);
          } else total = (unsigned)i + 3u;
        }
        ko = OOB;
        if (left) { ko = toff(next); ++next; --left; }
      } else if ((unsigned)i + 1u == total) break;
      if (i + 1 >= NSLOT) wait_free(i + 1);
      if (i + 1 >= BURST) vmwait<16>();
    }
    vmwait<16>();
    publish((int)total - 1);
    VM0();
    publish((int)total);
    STAMP(18);
    STAMP_FLUSH();
    return;
  }

  asm volatile("" : "+s"(w_b));
  if (w_b == 5) {
    __builtin_amdgcn_s_setprio(3);
    const rsrc_t rwm = mkrsrc(a.wm, DM * 3072 * 2), rc = mkrsrc(a.c, DM * 2);
    const int n0 = 512 * (gq & 1) + 64 * x + 16 * (j & 3);
    const int ar = 128 * kq + 4 * (lane >> 1);
    v4u w1[4];
#pragma unroll
    for (int i = 0; i < 4; ++i) w1[i] = ld128<NT>(rwm, (unsigned)((ar + i) * 3072 + n0 + 8 * (lane & 1)) * 2);
    const v2u c4 = ld64<0>(rc, ar * 2);
    const v2u ep = ld64<SC1>(ws, WS_EPOCH);
    const int mr = 64 * gq + 8 * (lane >> 3), mc = 64 * (x + 8 * (j & 3));
    const v4u cv = ld128<0>(rc, mr * 2);
    MEMFENCE();
    if (lane == 0) __hip_atomic_fetch_add(lds_pl<unsigned>(lb + L_ISSUED), 1u, __ATOMIC_RELAXED, __HIP_MEMORY_SCOPE_WORKGROUP);
    STAMP(22);
    const u64 e1 = (((u64)ep[1] << 32) | ep[0]) + 1ull;
    {
      float s8[8] = {0.f, 0.f, 0.f, 0.f, 0.f, 0.f, 0.f, 0.f};
      fma8(s8, silu_bf(lo16(c4[0])), w1[0]); fma8(s8, silu_bf(hi16(c4[0])), w1[1]);
      fma8(s8, silu_bf(lo16(c4[1])), w1[2]); fma8(s8, silu_bf(hi16(c4[1])), w1[3]);
      float u0, u1;
      rows_reduce8(s8, u0, u1);
      u0 += dpp<ROR8>(u0); u1 += dpp<ROR8>(u1); u0 += dpp<ROR4>(u0); u1 += dpp<ROR4>(u1); u0 += dpp<XOR2>(u0); u1 += dpp<XOR2>(u1);
      const unsigned tlo = (unsigned)e1 & 0xFu, thi = (unsigned)(e1 >> 4);
      const int col = n0 + 8 * (lane & 1) + 2 * (lane >> 4);
      st128(ws, (lane & 15) < 2 ? WS_P1 + (kq * 1024 + col) * 8 : OOB, v4u{gr_lo(u0, tlo), thi, gr_lo(u1, tlo), thi});
      *lds_at<unsigned>(lb + L_AUXPUB) = 1u;
    }
    STAMP(20);
    __builtin_amdgcn_s_setprio(0);
    auto stream_at = [&](unsigned fillno) {
      for (unsigned sp = 0; *lds_at<unsigned>(lb + L_FT) < fillno; ++sp) { __builtin_amdgcn_s_sleep(8); if ((sp & 63u) == 63u && rt() - t0 > TLIMIT) break; }
      MEMFENCE();
    };
    stream_at(NSLOT + 4);
    {
      const v4u e4 = {EMPTY, EMPTY, EMPTY, EMPTY};
      unsigned o = OOB;
      if (lane < 33) o = WS_PO + b * REC + lane * 16;
      else if (lane == 33) o = WS_M2 + b * 16;
      else if (lane < 42) o = mlp ? OOB : WS_YP + (og * 512 + oc0) * 4 + (lane - 34) * 16;
      else if (lane >= 48) o = WS_M2P + (gq * 2048 + mc) * 4 + (lane - 48) * 16;
      st128(ws, o, e4);
      const unsigned d = has_gu ? WS_DP + up * 2048 + lane * 16 : OOB;
      st128(ws, d, e4);
      st128(ws, d + 1024, e4);
      st32(ws, (lane == 0 && x == 0) ? WS_CTR + j * 256 : OOB, 0u);
      VM0();
      st64(ws, lane == 0 ? WS_CLR + b * 8 : OOB, v2u{(unsigned)e1, (unsigned)(e1 >> 32)});
    }
    {
      v4u w2[8];
#pragma unroll
      for (int i = 0; i < 8; ++i) w2[i] = ld128<NT>(rwm, (unsigned)((mr + i) * 3072 + 1024 + mc + 8 * (lane & 7)) * 2);
      float s8[8] = {0.f, 0.f, 0.f, 0.f, 0.f, 0.f, 0.f, 0.f};
#pragma unroll
      for (int e = 0; e < 4; ++e) { fma8(s8, silu_bf(lo16(cv[e])), w2[2 * e]); fma8(s8, silu_bf(hi16(cv[e])), w2[2 * e + 1]); }
      float u0, u1;
      rows_reduce8(s8, u0, u1);
      u0 += dpp<ROR8>(u0); u1 += dpp<ROR8>(u1);
      st64(ws, (lane & 8) == 0 ? WS_M2P + (gq * 2048 + mc + 8 * (lane & 7) + 2 * (lane >> 4)) * 4 : OOB, v2u{noempty(asu(u0)), noempty(asu(u1))});
    }
    stream_at(NSLOT + 16);
    for (;;) {
      MEMFENCE();
      const v4u f0 = ld128<SC1>(ws, WS_CLR + lane * 16), f1 = ld128<SC1>(ws, WS_CLR + 1024 + lane * 16);
      const unsigned el = (unsigned)e1, eh = (unsigned)(e1 >> 32);
      if (__all(((f0[0] ^ el) | (f0[1] ^ eh) | (f0[2] ^ el) | (f0[3] ^ eh) | (f1[0] ^ el) | (f1[1] ^ eh) | (f1[2] ^ el) | (f1[3] ^ eh)) == 0u) || rt() - t0 > TLIMIT) break;
      __builtin_amdgcn_s_sleep(32);
    }
    {
      v4u m;
      for (;;) {
        MEMFENCE();
        m = ld128<SC1>(ws, lane < 16 ? WS_M2P + ((lane >> 1) * 2048 + 8 * b + 4 * (lane & 1)) * 4 : OOB);
        if (__all(mx4(0u, m) != EMPTY) || rt() - t0 > TLIMIT) break;
        __builtin_amdgcn_s_sleep(32);
      }
      float t[4];
#pragma unroll
      for (int e = 0; e < 4; ++e) { t[e] = asf(m[e]); t[e] += dpp<ROR8>(t[e]); t[e] += dpp<ROR4>(t[e]); t[e] += dpp<XOR2>(t[e]); }
      st64(ws, lane < 2 ? WS_M2 + b * 16 + lane * 8 : OOB, v2u{noempty(cvtpk(t[0], t[1])), noempty(cvtpk(t[2], t[3]))});
    }
    stream_at(NSLOT + 28);
    if (mlp) {
      v4u m[4];
      for (;;) {
        MEMFENCE();
#pragma unroll
        for (int u = 0; u < 4; ++u) m[u] = ld128<SC1>(ws, WS_M2 + u * 1024 + lane * 16);
        if (__all(mx4(mx4(mx4(mx4(0u, m[0]), m[1]), m[2]), m[3]) != EMPTY) || rt() - t0 > TLIMIT) break;
        __builtin_amdgcn_s_sleep(32);
      }
#pragma unroll
      for (int u = 0; u < 4; ++u) *lds_pl<v4u>(lb + L_M2 + u * 1024 + lane * 16) = m[u];
      LGKM0();
      *lds_at<unsigned>(lb + L_M2RDY) = 1u;
    }
    STAMP(21);
    {
      GAS unsigned* ctr = (GAS unsigned*)(a.ws + WS_CTR + j * 256);
      for (unsigned n = 1;; ++n) {
        for (unsigned sp = 0; *lds_at<unsigned>(lb + L_TKC) < n; ++sp) { __builtin_amdgcn_s_sleep(2); if ((sp & 63u) == 63u && rt() - t0 > TLIMIT) break; }
        unsigned v = 0;
        if (lane == 0) v = __hip_atomic_fetch_add(ctr, 1u, __ATOMIC_RELAXED, __HIP_MEMORY_SCOPE_AGENT);
        v = __builtin_amdgcn_readfirstlane(v);
        *lds_at<u64>(lb + L_TK) = ((u64)n << 32) | v;
        if (v >= (unsigned)NTICK || rt() - t0 > TLIMIT) break;
      }
    }
    STAMP_FLUSH();
    return;
  }

  const int g = lane >> 4, c = lane & 15;
  const rsrc_t rx = mkrsrc(a.x, DM * 2), rwq = mkrsrc(a.wq, DM * 4096 * 2);
  const rsrc_t rwd = mkrsrc(a.wd, DF * DM * 2);
  const int r0 = 128 * kq + 32 * wave;
  v4u wqv[4];
#pragma unroll
  for (int i = 0; i < 4; ++i) wqv[i] = ld128<NT>(rwq, (unsigned)((r0 + 4 * (lane >> 3) + i) * 4096 + 64 * cb + 8 * (lane & 7)) * 2);
  const v2u ep = ld64<SC1>(ws, WS_EPOCH);
  const v4u x8 = ld128<0>(rx, lane * 16);
  const unsigned x2 = ld32<0>(rx, tid * 4);
  const unsigned xh = ld32<0>(rx, (r0 + 2 * c) * 2);
  MEMFENCE();
  if (lane == 0) __hip_atomic_fetch_add(lds_pl<unsigned>(lb + L_ISSUED), 1u, __ATOMIC_RELAXED, __HIP_MEMORY_SCOPE_WORKGROUP);
  float mb, sd;
  {
    float xs[8];
#pragma unroll
    for (int e = 0; e < 4; ++e) { xs[2 * e] = lo16(x8[e]); xs[2 * e + 1] = hi16(x8[e]); }
    float s = 0.f;
#pragma unroll
    for (int e = 0; e < 8; ++e) s += xs[e];
    const float mean = wave_sum(s) * (1.f / 512.f);
    float v = 0.f;
#pragma unroll
    for (int e = 0; e < 8; ++e) { const float d = xs[e] - mean; v = fmaf(d, d, v); }
    ln_round(mean, wave_sum(v) * (1.f / 512.f), mb, sd);
  }
  float nrow = rbf((lane >= 32 ? hi16(xh) : lo16(xh)) - mb) / sd;
  kpair(wqv[0], wqv[1]); kpair(wqv[2], wqv[3]);
  PIN4(wqv); asm volatile("" : "+v"(nrow));
  const u64 epoch1 = (((u64)ep[1] << 32) | ep[0]) + 1ull;
  const unsigned tlo = (unsigned)epoch1 & 0xFu, thi = (unsigned)(epoch1 >> 4);
  lds_wait_ge(lb + L_AUXPUB, 1u, t0);
  {
    const unsigned o1 = WS_P1 + (g * 1024 + r0 + 2 * c) * 8;
    v4u gs = {0u, 0u, 0u, 0u}, gc = gs;
    bool bad = true;
    if (P1WAIT) __builtin_amdgcn_s_sleep(P1WAIT);
    for (;;) {
      MEMFENCE();
      const v4u ns_ = ld128<SC1>(ws, bad ? o1 : OOB), nc_ = ld128<SC1>(ws, bad ? o1 + 4096 : OOB);
      const bool tk = bad & ((gr_bad(ns_, tlo, thi) | gr_bad(nc_, tlo, thi)) == 0u);
      gs = sel4(tk, ns_, gs); gc = sel4(tk, nc_, gc); bad = bad & !tk;
      if (!__any(bad) || rt() - t0 > TLIMIT) break;
      if (P1SLP) __builtin_amdgcn_s_sleep(P1SLP);
    }
    STAMP(1);
    const unsigned hp = pack_halves(mod_round(nrow, rbf(gr_sum(gc)), rbf(gr_sum(gs))));
    float acc[8] = {0.f, 0.f, 0.f, 0.f, 0.f, 0.f, 0.f, 0.f};
    dot8(acc, __builtin_amdgcn_ds_bpermute((lane >> 3) * 8, hp), wqv[0], wqv[1]);
    dot8(acc, __builtin_amdgcn_ds_bpermute((lane >> 3) * 8 + 4, hp), wqv[2], wqv[3]);
    float u0, u1;
    rows_reduce8(acc, u0, u1);
    u0 += dpp<ROR8>(u0); u1 += dpp<ROR8>(u1);
    if ((lane & 8) == 0) *lds_pl<f32x2>(lb + L_RED + wave * 256 + (8 * (lane & 7) + 2 * g) * 4) = f32x2{u0, u1};
    LGKM0();
    unsigned old = 0;
    if (lane == 0) old = __hip_atomic_fetch_add(lds_pl<unsigned>(lb + L_QCNT), 1u, __ATOMIC_RELAXED, __HIP_MEMORY_SCOPE_WORKGROUP);
    old = __builtin_amdgcn_readfirstlane(old);
    MEMFENCE();
    if (old == 3u) {
      const unsigned ra = lb + L_RED + lane * 4;
      const float qp = (*lds_pl<float>(ra) + *lds_pl<float>(ra + 256)) + (*lds_pl<float>(ra + 512) + *lds_pl<float>(ra + 768));
      st64(ws, WS_QS + (kq * 4096 + 64 * cb + lane) * 8, v2u{gr_lo(qp, tlo), thi});
      STAMP(2);
    }
  }
  const int r8 = 8 * (tid >> 2);
  v4u wv[8];
  unsigned wdv[16];
#pragma unroll
  for (int i = 0; i < 8; ++i) { wv[i] = v4u{0u, 0u, 0u, 0u}; wdv[2 * i] = 0u; wdv[2 * i + 1] = 0u; }
  const GAS v4u* wp = (const GAS v4u*)(mlp ? ((tid & 2) ? a.wu : a.wg) + (r8 * DF + 16 * (has_gu ? up : 0) + 8 * (tid & 1)) : a.wo + ((512 * og + r8) * DM + oc0 + 8 * (tid & 3)));
  const int wstride = mlp ? DF / 8 : DM / 8;
  const unsigned wdo = has_gu ? (unsigned)(16 * up * DM + 2 * tid) * 2 : OOB;

  const unsigned key = 4 * (c & 3) + (c >> 2);
  unsigned ka[4], msk[4];
#pragma unroll
  for (int st = 0; st < 4; ++st) {
    ka[st] = lb + L_RING + (wave & 1) * 4096 + key * 256 + (((st + 4 * g) ^ key) << 4);
    msk[st] = ((c & 7) >> 1) == st ? ((c & 1) ? 0xffff0000u : 0x0000ffffu) : 0u;
  }
  const unsigned va = lb + L_RING + 8192 + (wave & 1) * 4096 + lane * 16;
  const bool is_hi = c < 8;
  volatile LAS u64* FT = lds_at<u64>(lb + L_FT);
  volatile LAS unsigned* DN = lds_at<unsigned>(lb + L_DONE + 4 * wave);
  unsigned so = (wave >> 1) * SLOT;
  v4u sv[8], sw[8];
#pragma unroll
  for (int i = 0; i < 8; ++i) { sv[i] = v4u{0u, 0u, 0u, 0u}; sw[i] = v4u{0u, 0u, 0u, 0u}; }
  int ns = 0;
  auto read_unit = [&](v4u (&d)[8], int i) {
#pragma unroll
    for (int u = 0; u < 4; ++u) d[u] = *lds_pl<v4u>(va + so + u * 1024);
#pragma unroll
    for (int st = 0; st < 4; ++st) d[4 + st] = *lds_pl<v4u>(ka[st] + so);
    LGKM0();
    *DN = (unsigned)(i >> 1) + 1u;
    asm volatile("" : "+v"(d[0]), "+v"(d[1]), "+v"(d[2]), "+v"(d[3]), "+v"(d[4]), "+v"(d[5]), "+v"(d[6]), "+v"(d[7]) :: "memory");
    so += 2 * SLOT; so = so >= NSLOT * SLOT ? so - NSLOT * SLOT : so;
  };
  v4u qf[4];
  {
    const unsigned o2 = WS_QS + (g * 4096 + 128 * j + 32 * wave + 2 * c) * 8;
    v4u gq4 = {0u, 0u, 0u, 0u};
    bool bad = true;
    {
      const int full = __builtin_amdgcn_readfirstlane((int)*lds_at<unsigned>(lb + L_FT)), i0 = wave >> 1;
      MEMFENCE();
      if (NSTASH >= 1 && full > i0) { read_unit(wv, i0); ns = 1; }
      if (NSTASH >= 2 && full > i0 + 2) { read_unit(sv, i0 + 2); ns = 2; }
      if (NSTASH >= 3 && full > i0 + 4) { read_unit(sw, i0 + 4); ns = 3; }
    }
    if (QWAIT) __builtin_amdgcn_s_sleep(QWAIT);
    for (;;) {
      MEMFENCE();
      const v4u nq = ld128<SC1>(ws, bad ? o2 : OOB);
      const bool tk = bad & (gr_bad(nq, tlo, thi) == 0u);
      gq4 = sel4(tk, nq, gq4); bad = bad & !tk;
      if (!__any(bad) || rt() - t0 > TLIMIT) break;
      if (QSLP) __builtin_amdgcn_s_sleep(QSLP);
    }
    *lds_pl<unsigned>(lb + L_QSC + (16 * wave + c) * 4) = pack_halves(rbf(gr_sum(gq4)));
    LGKM0();
    if (lane == 0) __hip_atomic_fetch_add(lds_pl<unsigned>(lb + L_QIN), 1u, __ATOMIC_RELAXED, __HIP_MEMORY_SCOPE_WORKGROUP);
    lds_wait_ge(lb + L_QIN, 4u, t0);
#pragma unroll
    for (int st = 0; st < 4; ++st) qf[st] = *lds_pl<v4u>(lb + L_QSC + (st + 4 * g) * 16);
  }
  STAMP(3); STAMPC(28);

  float m2 = -INFINITY, lsum = 0.f;
  f32x4 acc = {0.f, 0.f, 0.f, 0.f};
  {
    int total = 0x7FFFFFFF;
    v4u pa[4] = {v4u{0u, 0u, 0u, 0u}, v4u{0u, 0u, 0u, 0u}, v4u{0u, 0u, 0u, 0u}, v4u{0u, 0u, 0u, 0u}};
    int wk = (mlp && !has_gu) ? 11 : 0;
    bool done = false;
    for (int i = wave >> 1;; i += 2) {
      if (!done) {
        v4u ud[8];
        if ((i >> 1) < ns) {
#pragma unroll
          for (int u = 0; u < 8; ++u) ud[u] = (i >> 1) == 0 ? wv[u] : ((i >> 1) == 1 || NSTASH < 3) ? sv[u] : sw[u];
        } else {
          for (unsigned sp = 0;; ++sp) {
            const u64 ft = *FT;
            const int full = __builtin_amdgcn_readfirstlane((int)(unsigned)ft);
            total = __builtin_amdgcn_readfirstlane((int)(unsigned)(ft >> 32));
            if (full > i) break;
            if ((sp & 63u) == 63u && rt() - t0 > TLIMIT) { total = 0; break; }
            __builtin_amdgcn_s_sleep(CSLEEP);
          }
          MEMFENCE();
          read_unit(ud, i);
        }
        if (i >= 2) {
#pragma unroll
          for (int u = 0; u < 4; ++u) acc = mfma(pa[u], ud[u], acc);
        }
        done = i + 2 >= total;
        if (!done) {
          f32x4 sa = mfma(ud[4], qf[0], f32x4{0.f, 0.f, 0.f, 0.f}), sb4 = mfma(ud[5], qf[1], f32x4{0.f, 0.f, 0.f, 0.f});
          sa = mfma(ud[6], qf[2], sa); sb4 = mfma(ud[7], qf[3], sb4);
          sa += sb4;
          float s[4];
#pragma unroll
          for (int u = 0; u < 2; ++u) {
            const unsigned p1 = cvtpk(sa[2 * u], sa[2 * u + 1]);
            const unsigned p2 = cvtpk(lo16(p1) * SCALE, hi16(p1) * SCALE);
            s[2 * u] = lo16(p2); s[2 * u + 1] = hi16(p2);
          }
          const float mx = xrow_max(fmaxf(fmaxf(s[0], s[1]), fmaxf(s[2], s[3])));
          const float mn = fmaxf(m2, mx * LOG2E);
          const float alpha = __builtin_amdgcn_exp2f(m2 - mn);
          m2 = mn;
          acc *= alpha;
          float ps = 0.f;
#pragma unroll
          for (int u = 0; u < 4; ++u) {
            const float p = __builtin_amdgcn_exp2f(fmaf(s[u], LOG2E, -mn));
            ps += p;
            const float ph = hi16(asu(p));
            const float xv = is_hi ? ph : p - ph;
            const unsigned pk = cvtpk(xv, xv);
            pa[u] = v4u{pk & msk[0], pk & msk[1], pk & msk[2], pk & msk[3]};
          }
          lsum = fmaf(lsum, alpha, ps);
        }
      }
      MEMFENCE();
      if (wk <= 10 && (done || rt() - t0 >= (u64)(TW + TD * wk + (wk == 10 ? 400 : 0)))) {
#pragma unroll
        for (int u = 0; u < 8; ++u) if (wk == u) wv[u] = __builtin_nontemporal_load(wp + (XP_NOW ? 0 : u * wstride));
#pragma unroll
        for (int v = 0; v < 2; ++v) if (wk == 8 + v) {
#pragma unroll
          for (int u = 8 * v; u < 8 * v + 8; ++u) wdv[u] = ld32<NT>(rwd, wdo + u * (DM * 2));
        }
        if (wk == 10) {
#pragma unroll
          for (int u = 0; u < 8; u += 2) kpair(wv[u], wv[u + 1]);
#pragma unroll
          for (int u = 0; u < 16; u += 2) {
            const unsigned lo = __builtin_amdgcn_perm(wdv[u + 1], wdv[u], 0x05040100u), hi = __builtin_amdgcn_perm(wdv[u + 1], wdv[u], 0x07060302u);
            wdv[u] = lo; wdv[u + 1] = hi;
          }
        }
        ++wk;
      }
      MEMFENCE();
      if (done && wk > 10) break;
    }
  }
  STAMP(4); STAMPC(29);
  {
    f32x4 o;
#pragma unroll
    for (int r = 0; r < 4; ++r) o[r] = sw32_add(acc[r], acc[r]);
    float l = sw16_add(lsum, lsum); l = sw32_add(l, l);
    const unsigned rec = lb + L_MRG + wave * 528;
    if (lane < 32) *lds_pl<f32x4>(rec + 16 + (8 * c + 4 * g) * 4) = o;
    if (lane == 0) *lds_pl<f32x2>(rec) = f32x2{m2, l};
    wg_barrier();
    if (wave == 0) {
      f32x2 ml[4];
#pragma unroll
      for (int w = 0; w < 4; ++w) ml[w] = *lds_pl<f32x2>(lb + L_MRG + w * 528);
      const float M = fmaxf(fmaxf(ml[0][0], ml[1][0]), fmaxf(ml[2][0], ml[3][0]));
      float L = 0.f; f32x4 O = {0.f, 0.f, 0.f, 0.f};
#pragma unroll
      for (int w = 0; w < 4; ++w) {
        const float wt = __builtin_amdgcn_exp2f(ml[w][0] - M);
        L = fmaf(wt, ml[w][1], L);
        O += wt * *lds_pl<f32x4>(lb + L_MRG + w * 528 + 16 + (lane & 31) * 16);
      }
      if (lane >= 32) O = f32x4{M, L, 0.f, 0.f};
      st128(ws, lane < 33 ? WS_PO + b * REC + lane * 16 : OOB, noempty4(O));
      STAMP(5);
    }
  }
  if (!mlp) {
    const unsigned hb = WS_PO + (4 * og + wave) * 8 * REC;
    v2u po[8], ml = {EMPTY, EMPTY};
#pragma unroll
    for (int s = 0; s < 8; ++s) po[s] = v2u{0u, 0u};
    unsigned miss = 0x1FFu;
    for (;;) {
      MEMFENCE();
      if (miss & 0x100u) ml = ld64<SC1>(ws, lane < 8 ? hb + lane * REC + 512 : OOB);
#pragma unroll
      for (int s = 0; s < 8; ++s) if (miss & (1u << s)) po[s] = ld64<SC1>(ws, hb + s * REC + lane * 8);
      if ((miss & 0x100u) && __all(mx2(0u, ml) != EMPTY)) miss &= ~0x100u;
#pragma unroll
      for (int s = 0; s < 8; ++s) if ((miss & (1u << s)) && __all(mx2(0u, po[s]) != EMPTY)) miss &= ~(1u << s);
      if (miss == 0u || rt() - t0 > TLIMIT) break;
    }
    STAMP(6);
    const float ms = lane < 8 ? asf(ml[0]) : -INFINITY;
    const float wt = __builtin_amdgcn_exp2f(ms - row_max(ms));
    const float wn = wt * __builtin_amdgcn_rcpf(row_sum(wt * asf(ml[1])));
    float oa = 0.f, ob = 0.f;
#pragma unroll
    for (int s = 0; s < 8; ++s) {
      const float w = asf(__builtin_amdgcn_readlane(asu(wn), s));
      oa = fmaf(w, asf(po[s][0]), oa); ob = fmaf(w, asf(po[s][1]), ob);
    }
    const unsigned op = cvtpk(oa, ob);
    float y[8] = {0.f, 0.f, 0.f, 0.f, 0.f, 0.f, 0.f, 0.f};
    dot8(y, dppu<0x00>(op), wv[0], wv[1]); dot8(y, dppu<0x55>(op), wv[2], wv[3]);
    dot8(y, dppu<0xAA>(op), wv[4], wv[5]); dot8(y, dppu<0xFF>(op), wv[6], wv[7]);
    float u0, u1;
    rows_reduce8(y, u0, u1);
    u0 += dpp<ROR8>(u0); u1 += dpp<ROR8>(u1); u0 += dpp<ROR4>(u0); u1 += dpp<ROR4>(u1);
    if (c < 4) *lds_pl<f32x2>(lb + L_RED3 + wave * 128 + (8 * c + 2 * g) * 4) = f32x2{u0, u1};
    wg_barrier();
    if (wave == 0) {
      const unsigned ra = lb + L_RED3 + (lane & 7) * 16;
      const f32x4 t = (*lds_pl<f32x4>(ra) + *lds_pl<f32x4>(ra + 128)) + (*lds_pl<f32x4>(ra + 256) + *lds_pl<f32x4>(ra + 384));
      st128(ws, lane < 8 ? WS_YP + (og * 512 + oc0) * 4 + lane * 16 : OOB, noempty4(t));
      STAMP(7);
    }
    STAMP_FLUSH();
    return;
  }
  lds_wait_ge(lb + L_M2RDY, 1u, t0);
  float gbf = lo16(*lds_pl<u16>(lb + L_M2 + 3072 + (fc0 + g) * 2));
  {
    const unsigned gaw = *lds_pl<unsigned>(lb + L_M2 + tid * 4), sbw = *lds_pl<unsigned>(lb + L_M2 + 1024 + tid * 4), cbw = *lds_pl<unsigned>(lb + L_M2 + 2048 + tid * 4);
    float ga0 = lo16(gaw), ga1 = hi16(gaw), sb0 = lo16(sbw), sb1 = hi16(sbw), t0c = rbf(1.f + lo16(cbw)), t1c = rbf(1.f + hi16(cbw)), xa = lo16(x2), xb = hi16(x2);
    asm volatile("" : "+v"(ga0), "+v"(ga1), "+v"(sb0), "+v"(sb1), "+v"(t0c), "+v"(t1c), "+v"(xa), "+v"(xb), "+v"(gbf));
    v2u yp[8];
#pragma unroll
    for (int s = 0; s < 8; ++s) yp[s] = v2u{0u, 0u};
    unsigned miss = 0xFFu;
    if (BWAIT) __builtin_amdgcn_s_sleep(BWAIT);
    for (;;) {
      MEMFENCE();
#pragma unroll
      for (int s = 0; s < 8; ++s) if (miss & (1u << s)) yp[s] = ld64<SC1>(ws, WS_YP + s * 2048 + tid * 8);
#pragma unroll
      for (int s = 0; s < 8; ++s) if ((miss & (1u << s)) && __all(mx2(0u, yp[s]) != EMPTY)) miss &= ~(1u << s);
      if (miss == 0u || rt() - t0 > TLIMIT) break;
    }
    STAMP(8);
    const unsigned atp = cvtpk(((asf(yp[0][0]) + asf(yp[1][0])) + (asf(yp[2][0]) + asf(yp[3][0]))) + ((asf(yp[4][0]) + asf(yp[5][0])) + (asf(yp[6][0]) + asf(yp[7][0]))),
                               ((asf(yp[0][1]) + asf(yp[1][1])) + (asf(yp[2][1]) + asf(yp[3][1]))) + ((asf(yp[4][1]) + asf(yp[5][1])) + (asf(yp[6][1]) + asf(yp[7][1]))));
    const unsigned gap = cvtpk(ga0 * lo16(atp), ga1 * hi16(atp));
    const unsigned x1p = cvtpk(xa + lo16(gap), xb + hi16(gap));
    const float x1a = lo16(x1p), x1b = hi16(x1p);
    const float sh = asf(__builtin_amdgcn_readfirstlane(asu(x1a)));
    const float da = x1a - sh, db = x1b - sh;
    const float s1 = wave_sum(da + db), s2 = wave_sum(fmaf(da, da, db * db));
    if (lane == 0) *lds_pl<f32x2>(lb + L_ST + wave * 8) = f32x2{fmaf(s1, 1.f / 128.f, sh), fmaf(-s1 * (1.f / 128.f), s1, s2)};
    *lds_pl<f32x2>(lb + L_X1 + tid * 8) = f32x2{x1a, x1b};
    wg_barrier();
    STAMP(12);
    if (b == 0 && tid == 0) st64(ws, WS_EPOCH, v2u{(unsigned)epoch1, (unsigned)(epoch1 >> 32)});
    if (has_gu) {
      const f32x4 sA = *lds_pl<f32x4>(lb + L_ST), sB = *lds_pl<f32x4>(lb + L_ST + 16);
      const float mean = ((sA[0] + sA[2]) + (sB[0] + sB[2])) * 0.25f;
      const float d0 = sA[0] - mean, d1 = sA[2] - mean, d2 = sB[0] - mean, d3 = sB[2] - mean;
      const float var = fmaxf(((sA[1] + sA[3]) + (sB[1] + sB[3])) + 128.f * ((d0 * d0 + d1 * d1) + (d2 * d2 + d3 * d3)), 0.f) * (1.f / 512.f);
      float mb2, sd2;
      ln_round(mean, var, mb2, sd2);
      const float rs = __builtin_amdgcn_rcpf(sd2);
      const unsigned e0 = cvtpk(x1a - mb2, x1b - mb2), e1 = cvtpk(lo16(e0) * rs, hi16(e0) * rs), e2 = cvtpk(lo16(e1) * t0c, hi16(e1) * t1c);
      const unsigned hq = cvtpk(lo16(e2) + sb0, hi16(e2) + sb1);
      float y[8] = {0.f, 0.f, 0.f, 0.f, 0.f, 0.f, 0.f, 0.f};
      dot8(y, dppu<0x00>(hq), wv[0], wv[1]); dot8(y, dppu<0x55>(hq), wv[2], wv[3]);
      dot8(y, dppu<0xAA>(hq), wv[4], wv[5]); dot8(y, dppu<0xFF>(hq), wv[6], wv[7]);
      float u0, u1;
      rows_reduce8(y, u0, u1);
      u0 += dpp<ROR8>(u0); u1 += dpp<ROR8>(u1); u0 += dpp<ROR4>(u0); u1 += dpp<ROR4>(u1);
      if (c < 4) *lds_pl<f32x2>(lb + L_RED3 + wave * 128 + (8 * c + 2 * g) * 4) = f32x2{u0, u1};
      wg_barrier();
      const unsigned ra = lb + L_RED3 + c * 4;
      const float gs = rbf((*lds_pl<float>(ra) + *lds_pl<float>(ra + 128)) + (*lds_pl<float>(ra + 256) + *lds_pl<float>(ra + 384)));
      const float us = rbf((*lds_pl<float>(ra + 64) + *lds_pl<float>(ra + 192)) + (*lds_pl<float>(ra + 320) + *lds_pl<float>(ra + 448)));
      const float z = 0.7978845608028654f * fmaf(0.044715f * gs * gs, gs, gs);
      const float th = fmaf(-2.f, __builtin_amdgcn_rcpf(1.f + __expf(2.f * z)), 1.f);
      const float h2 = rbf(rbf(0.5f * gs * (1.f + th)) * us);
      const unsigned hp = __builtin_amdgcn_perm(asu(dpp<XOR1>(h2)), asu(h2), 0x07060302u);
      float d0a = 0.f, d1a = 0.f;
#pragma unroll
      for (int e = 0; e < 16; e += 2) {
        const unsigned he = __builtin_amdgcn_readlane(hp, e);
        d0a = dot2(wdv[e], he, d0a); d1a = dot2(wdv[e + 1], he, d1a);
      }
      st64(ws, WS_DP + up * 2048 + tid * 8, v2u{noempty(asu(d0a)), noempty(asu(d1a))});
      STAMP(9);
    }
  }
  if (wave != 0) { STAMP_FLUSH(); return; }
  {
    v4u da = {EMPTY, 0u, 0u, 0u}, db = {lane < NGU - 64 ? EMPTY : 0u, 0u, 0u, 0u};
    for (;;) {
      MEMFENCE();
      const bool ma = mx4(0u, da) == EMPTY, mb_ = mx4(0u, db) == EMPTY;
      if (__all(!ma && !mb_) || rt() - t0 > TLIMIT) break;
      const v4u na = ld128<SC1>(ws, ma ? WS_DP + lane * 2048 + fc0 * 4 : OOB), nb = ld128<SC1>(ws, mb_ ? WS_DP + (lane + 64) * 2048 + fc0 * 4 : OOB);
      da = ma ? na : da; db = mb_ ? nb : db;
    }
    STAMP(10);
    float t4[4];
#pragma unroll
    for (int r = 0; r < 4; ++r) t4[r] = asf(da[r]) + asf(db[r]);
    const float r = row_sum(sw16_add(sw32_add(t4[0], t4[2]), sw32_add(t4[1], t4[3])));
    const float res = *lds_pl<float>(lb + L_X1 + (fc0 + g) * 4) + rbf(gbf * rbf(r));
    if (c == 0) a.out[fc0 + g] = (u16)(cvtpk(res, res) & 0xffffu);
    STAMP(11);
    STAMP_FLUSH();
  }
}
}

extern "C" void kernel_launch(void* const* d_in, const int* in_sizes, int n_in, void* d_out, int out_size, void* d_ws, size_t ws_size,
                              hipStream_t stream) {
  Args a;
  a.x = (const u16*)d_in[0]; a.K = (const u16*)d_in[1]; a.V = (const u16*)d_in[2]; a.wq = (const u16*)d_in[3]; a.wo = (const u16*)d_in[4];
  a.wg = (const u16*)d_in[6]; a.wu = (const u16*)d_in[7]; a.wd = (const u16*)d_in[8]; a.c = (const u16*)d_in[9]; a.wm = (const u16*)d_in[10];
  a.out = (u16*)d_out; a.ws = (unsigned char*)d_ws;
  (void)hipFuncSetAttribute((const void*)block_decode, hipFuncAttributeMaxDynamicSharedMemorySize, (int)LDS_BYTES);
  hipLaunchKernelGGL(block_decode, dim3(NWG), dim3(NTHR), LDS_BYTES, stream, a);
}
```

```cpp
#include <hip/hip_runtime.h>
#include <hip/hip_bf16.h>

#define GAS __attribute__((address_space(1)))
#define LAS __attribute__((address_space(3)))
typedef unsigned short u16;
typedef unsigned long long u64;
typedef unsigned v4u __attribute__((ext_vector_type(4)));
typedef unsigned v2u __attribute__((ext_vector_type(2)));
typedef int v4i __attribute__((ext_vector_type(4)));
typedef float f32x4 __attribute__((ext_vector_type(4)));
typedef __bf16 bf16x8 __attribute__((ext_vector_type(8)));
typedef __bf16 bf16x2 __attribute__((ext_vector_type(2)));
typedef float f32x2 __attribute__((ext_vector_type(2)));

#define DBG_TS 0
#define PQ 16
#define LSLEEP 6
#define NSLOT 9
#define TW 10800
#define TD 110
#define BWAIT 0
#define NSTASH 4
#define BURST 4
#define USEPQ 0
#define PRETOUCH 0
#define NOORDER 0
#define P1WAIT 24
#define P1SLP 0
#define QWAIT 0
#define QSLP 0
#define T4 48
#define POFF 6
#define PSLEEP 1
#define CSLEEP 1
#define XP_NOW 0
#define STAT 224

namespace {
constexpr int DM = 512, SK = 65536, DH = 128, DF = 1792;
constexpr int NWG = 256, NTHR = 384;
constexpr int SLOT = 16384, TILES_H = SK / 32;
constexpr int DYN0 = 8 * STAT, DYN2 = DYN0 + 4 * T4, NTICK = T4 + (TILES_H - DYN2) / 2;
static_assert(DYN2 <= TILES_H && (TILES_H - DYN2) % 2 == 0 && STAT % 4 == 0 && PQ <= 16, "tile plan");
constexpr unsigned HEAD_BYTES = (unsigned)SK * DH * 2;
constexpr unsigned EMPTY = 0xFFFFFFFFu;
constexpr unsigned OOB = 0x7FFF0000u;
constexpr u64 TLIMIT = 5000000ull;
constexpr float LOG2E = 1.4426950408889634f, SCALE = 0.08838834764831845f;
constexpr int NGU = DF / 16, REC = 640;
constexpr unsigned WS_EPOCH = 0, WS_P1 = 4096, WS_QS = WS_P1 + 32768, WS_M2P = WS_QS + 131072, WS_M2 = WS_M2P + 65536, WS_CTR = WS_M2 + 4096,
                   WS_CLR = WS_CTR + 8192, WS_YP = WS_CLR + 2048, WS_PO = WS_YP + 16384, WS_DP = WS_PO + NWG * REC, WS_END = WS_DP + NGU * 2048,
                   WS_DBG = 1u << 20;
constexpr unsigned L_RING = 0, L_CTL = NSLOT * SLOT, L_FT = L_CTL, L_DONE = L_CTL + 16, L_QCNT = L_CTL + 32, L_AUXPUB = L_CTL + 36,
                   L_QIN = L_CTL + 40, L_ISSUED = L_CTL + 44, L_M2RDY = L_CTL + 48, L_TKC = L_CTL + 52, L_TK = L_CTL + 56,
                   L_RED = L_CTL + 64, L_QSC = L_RED + 1024, L_RED3 = L_QSC + 256, L_ST = L_RED3 + 512,
                   L_X1 = L_ST + 64, L_M2 = L_X1 + 2048, L_MRG = L_M2 + 4096, L_ML = L_MRG + 2048, L_END = L_ML + 32;
constexpr unsigned LDS_BYTES = L_END + (DBG_TS ? 2048 : 0);
static_assert(LDS_BYTES <= 160 * 1024 && LDS_BYTES > 80 * 1024, "one workgroup per CU");

struct Args {
  const u16 *x, *K, *V, *wq, *wo, *wg, *wu, *wd, *c, *wm;
  u16* out;
  unsigned char* ws;
};

__device__ __forceinline__ u64 rt() { return __builtin_amdgcn_s_memrealtime(); }
__device__ __forceinline__ float asf(unsigned u) { return __builtin_bit_cast(float, u); }
__device__ __forceinline__ unsigned asu(float f) { return __builtin_bit_cast(unsigned, f); }
__device__ __forceinline__ float lo16(unsigned u) { return asf(u << 16); }
__device__ __forceinline__ float hi16(unsigned u) { return asf(u & 0xffff0000u); }
__device__ __forceinline__ unsigned cvtpk(float a, float b) {
  const f32x2 v = {a, b};
  return __builtin_bit_cast(unsigned, __builtin_convertvector(v, bf16x2));
}
__device__ __forceinline__ float rbf(float f) { return lo16(cvtpk(f, f)); }
__device__ __forceinline__ unsigned noempty(unsigned v) { return v < EMPTY - 1u ? v : EMPTY - 1u; }
__device__ __forceinline__ float dot2(unsigned a, unsigned b, float c) {
  return __builtin_amdgcn_fdot2_f32_bf16(__builtin_bit_cast(bf16x2, a), __builtin_bit_cast(bf16x2, b), c, false);
}
template <int CTRL> __device__ __forceinline__ float dpp(float v) {
  return asf(__builtin_amdgcn_mov_dpp(asu(v), CTRL, 0xf, 0xf, true));
}
constexpr int ROR8 = 0x128, ROR4 = 0x124, ROR2 = 0x122, ROR1 = 0x121, XOR2 = 0x4E, XOR1 = 0xB1;
template <int CTRL> __device__ __forceinline__ unsigned dppu(unsigned v) { return __builtin_amdgcn_mov_dpp(v, CTRL, 0xf, 0xf, true); }
__device__ __forceinline__ float sw32_add(float a, float b) {
  const auto r = __builtin_amdgcn_permlane32_swap(asu(a), asu(b), false, false);
  return asf(r[0]) + asf(r[1]);
}
__device__ __forceinline__ float sw16_add(float a, float b) {
  const auto r = __builtin_amdgcn_permlane16_swap(asu(a), asu(b), false, false);
  return asf(r[0]) + asf(r[1]);
}
__device__ __forceinline__ float xrow_max(float v) {
  const auto r = __builtin_amdgcn_permlane16_swap(asu(v), asu(v), false, false);
  v = fmaxf(asf(r[0]), asf(r[1]));
  const auto s = __builtin_amdgcn_permlane32_swap(asu(v), asu(v), false, false);
  return fmaxf(asf(s[0]), asf(s[1]));
}
__device__ __forceinline__ unsigned pack_halves(float v) {
  const auto r = __builtin_amdgcn_permlane32_swap(asu(v), asu(v), false, false);
  return __builtin_amdgcn_perm(r[1], r[0], 0x07060302u);
}
__device__ __forceinline__ float row_sum(float v) { v += dpp<ROR8>(v); v += dpp<ROR4>(v); v += dpp<ROR2>(v); v += dpp<ROR1>(v); return v; }
__device__ __forceinline__ float row_max(float v) { v = fmaxf(v, dpp<ROR8>(v)); v = fmaxf(v, dpp<ROR4>(v)); v = fmaxf(v, dpp<ROR2>(v)); return fmaxf(v, dpp<ROR1>(v)); }
__device__ __forceinline__ float wave_sum(float v) { v = row_sum(v); v = sw16_add(v, v); return sw32_add(v, v); }
__device__ __forceinline__ void rows_reduce8(const float (&a)[8], float& u0, float& u1) {
  const float r0 = sw32_add(a[0], a[4]), r1 = sw32_add(a[1], a[5]), r2 = sw32_add(a[2], a[6]), r3 = sw32_add(a[3], a[7]);
  u0 = sw16_add(r0, r2); u1 = sw16_add(r1, r3);
}
__device__ __forceinline__ void fma8(float (&a)[8], float h, v4u w) {
#pragma unroll
  for (int e = 0; e < 4; ++e) { a[2 * e] = fmaf(h, lo16(w[e]), a[2 * e]); a[2 * e + 1] = fmaf(h, hi16(w[e]), a[2 * e + 1]); }
}
__device__ __forceinline__ void kpair(v4u& a, v4u& b) {
  const v4u r0 = a, r1 = b;
  a = v4u{__builtin_amdgcn_perm(r1[0], r0[0], 0x05040100u), __builtin_amdgcn_perm(r1[0], r0[0], 0x07060302u),
          __builtin_amdgcn_perm(r1[1], r0[1], 0x05040100u), __builtin_amdgcn_perm(r1[1], r0[1], 0x07060302u)};
  b = v4u{__builtin_amdgcn_perm(r1[2], r0[2], 0x05040100u), __builtin_amdgcn_perm(r1[2], r0[2], 0x07060302u),
          __builtin_amdgcn_perm(r1[3], r0[3], 0x05040100u), __builtin_amdgcn_perm(r1[3], r0[3], 0x07060302u)};
}
__device__ __forceinline__ void dot8(float (&acc)[8], unsigned hp, v4u a, v4u b) {
#pragma unroll
  for (int e = 0; e < 4; ++e) { acc[e] = dot2(a[e], hp, acc[e]); acc[4 + e] = dot2(b[e], hp, acc[4 + e]); }
}
__device__ __forceinline__ f32x4 mfma(v4u a, v4u b, f32x4 c) {
  return __builtin_amdgcn_mfma_f32_16x16x32_bf16(__builtin_bit_cast(bf16x8, a), __builtin_bit_cast(bf16x8, b), c, 0, 0, 0);
}
typedef __amdgpu_buffer_rsrc_t rsrc_t;
__device__ __forceinline__ rsrc_t mkrsrc(const void* p, unsigned bytes) { return __builtin_amdgcn_make_buffer_rsrc((void*)p, 0, bytes, 0x00020000); }
constexpr int SC1 = 16, NT = 2;
template <int AUX> __device__ __forceinline__ v4u ld128(rsrc_t r, unsigned off) { return __builtin_amdgcn_raw_buffer_load_b128(r, off, 0, AUX); }
template <int AUX> __device__ __forceinline__ v2u ld64(rsrc_t r, unsigned off) { return __builtin_amdgcn_raw_buffer_load_b64(r, off, 0, AUX); }
template <int AUX> __device__ __forceinline__ unsigned ld32(rsrc_t r, unsigned off) { return __builtin_amdgcn_raw_buffer_load_b32(r, off, 0, AUX); }
__device__ __forceinline__ void st128(rsrc_t r, unsigned off, v4u v) { __builtin_amdgcn_raw_buffer_store_b128(v, r, off, 0, SC1); }
__device__ __forceinline__ void st64(rsrc_t r, unsigned off, v2u v) { __builtin_amdgcn_raw_buffer_store_b64(v, r, off, 0, SC1); }
__device__ __forceinline__ void st32(rsrc_t r, unsigned off, unsigned v) { __builtin_amdgcn_raw_buffer_store_b32(v, r, off, 0, SC1); }
__device__ __forceinline__ unsigned umax(unsigned a, unsigned b) { return a > b ? a : b; }
__device__ __forceinline__ unsigned mx4(unsigned m, v4u v) { return umax(umax(umax(m, v[0]), v[1]), umax(v[2], v[3])); }
__device__ __forceinline__ unsigned mx2(unsigned m, v2u v) { return umax(umax(m, v[0]), v[1]); }
__device__ __forceinline__ v4u noempty4(f32x4 v) { return v4u{noempty(asu(v[0])), noempty(asu(v[1])), noempty(asu(v[2])), noempty(asu(v[3]))}; }
__device__ __forceinline__ unsigned gr_lo(float v, unsigned tlo) { return ((asu(v) + 8u) & 0xFFFFFFF0u) | tlo; }
__device__ __forceinline__ unsigned gr_bad(v4u g, unsigned tlo, unsigned thi) { return (((g[0] ^ tlo) | (g[2] ^ tlo)) & 0xFu) | (g[1] ^ thi) | (g[3] ^ thi); }
__device__ __forceinline__ float gr_sum(v4u g) { const float s = sw32_add(asf(g[0] & 0xFFFFFFF0u), asf(g[2] & 0xFFFFFFF0u)); return sw16_add(s, s); }
__device__ __forceinline__ v4u sel4(bool t, v4u a, v4u b) { return v4u{t ? a[0] : b[0], 0u, t ? a[2] : b[2], 0u}; }
#define MEMFENCE() asm volatile("" ::: "memory")
#define LGKM0() asm volatile("s_waitcnt lgkmcnt(0)" ::: "memory")
#define VM0() asm volatile("s_waitcnt vmcnt(0)" ::: "memory")
#define PIN4(a) asm volatile("" : "+v"((a)[0]), "+v"((a)[1]), "+v"((a)[2]), "+v"((a)[3]))
template <int N> __device__ __forceinline__ void vmwait() { asm volatile("s_waitcnt vmcnt(%0)" ::"n"(N) : "memory"); }
__device__ __forceinline__ void wg_barrier() { LGKM0(); __builtin_amdgcn_s_barrier(); MEMFENCE(); }
template <typename T> __device__ __forceinline__ volatile LAS T* lds_at(unsigned off) { return (volatile LAS T*)(size_t)off; }
template <typename T> __device__ __forceinline__ LAS T* lds_pl(unsigned off) { return (LAS T*)(size_t)off; }
__device__ __forceinline__ void lds_wait_ge(unsigned addr, unsigned need, u64 t0) {
  for (unsigned sp = 0; *lds_at<unsigned>(addr) < need; ++sp) { __builtin_amdgcn_s_sleep(1); if ((sp & 63u) == 63u && rt() - t0 > TLIMIT) break; }
  MEMFENCE();
}

#define EXPIRED(it) (((it) & 63u) == 63u && rt() - t0 > TLIMIT)

#define STAMP(k) do {} while (0)
#define FSTAMP(k) do {} while (0)
#define STAMPC(k) do {} while (0)
#define STAMP_INIT() do {} while (0)
#define STAMP_FLUSH() do {} while (0)

__device__ __forceinline__ void dma4(v4i srd, unsigned soff, unsigned ldsb, unsigned v0, unsigned v1, unsigned v2, unsigned v3) {
  asm volatile("s_mov_b32 m0, %0\n\ts_nop 0\n\t"
               "buffer_load_dwordx4 %1, %5, %6 offen nt lds\n\t"
               "buffer_load_dwordx4 %2, %5, %6 offen offset:1024 nt lds\n\t"
               "buffer_load_dwordx4 %3, %5, %6 offen offset:2048 nt lds\n\t"
               "buffer_load_dwordx4 %4, %5, %6 offen offset:3072 nt lds"
               ::"s"(ldsb), "v"(v0), "v"(v1), "v"(v2), "v"(v3), "s"(srd), "s"(soff) : "memory");
}

__device__ __forceinline__ float silu_bf(float v) { return rbf(v / (1.f + __expf(-v))); }
__device__ __forceinline__ void ln_round(float mean, float var, float& mb, float& sd) {
  mb = rbf(mean);
  sd = rbf(__builtin_amdgcn_sqrtf(rbf(rbf(var) + 1e-5f)));
}
__device__ __forceinline__ float mod_round(float n, float sc, float sh) { return rbf(rbf(rbf(n) * rbf(1.f + sc)) + sh); }

__global__ void __launch_bounds__(NTHR, 2) block_decode(Args a) {
  extern __shared__ __attribute__((aligned(16))) unsigned char smem[];
  const int tid = threadIdx.x, lane = tid & 63, wave = __builtin_amdgcn_readfirstlane(tid >> 6);
  const int b = blockIdx.x, x = b & 7, j = b >> 3;
  const u64 t0 = rt();
  const unsigned lb = (unsigned)(size_t)(LAS unsigned char*)smem;
  const rsrc_t ws = mkrsrc(a.ws, WS_END);
  const int kq = j >> 3, cb = x + 8 * (j & 7);
  const int gq = j >> 2, rk = 4 * (gq >> 1) + (j & 3);
  const bool mlp = ((x ^ gq) & 1) == 0;
  const int og = rk >> 1, oc0 = 64 * x + 32 * (rk & 1);
  const bool has_gu = mlp && 4 * gq + (x >> 1) < 28;
  const int up = 4 * (4 * gq + (x >> 1)) + (j & 3);
  const int fc0 = 64 * x + 4 * rk;

  if (tid < 16) *lds_pl<unsigned>(lb + L_CTL + 4 * tid) = (tid == 1) ? 0x7FFFFFFFu : 0u;
  asm volatile("" :: "s"(a.K), "s"(a.ws));
  STAMP_INIT();
  wg_barrier();
  STAMP(0);

  int w_a = wave, w_b = wave;
  asm volatile("" : "+s"(w_a));
  if (w_a == 4) {
    const u64 kb = (u64)(a.K + (size_t)j * SK * DH), vb = (u64)(a.V + (size_t)j * SK * DH);
    const v4i ksrd = {(int)kb, (int)(kb >> 32), (int)HEAD_BYTES, 0x00020000}, vsrd = {(int)vb, (int)(vb >> 32), (int)HEAD_BYTES, 0x00020000};
    const unsigned g = lane >> 4, c = lane & 15;
    const unsigned k0 = g * 256 + ((c ^ g) << 4), k1 = g * 256 + ((c ^ (4 + g)) << 4), k2 = g * 256 + ((c ^ (8 + g)) << 4), k3 = g * 256 + ((c ^ (12 + g)) << 4);
    const unsigned vl = lane * 16;
    unsigned sl = lb + L_RING;
    unsigned vo1 = OOB, vo2 = OOB;
    auto fill = [&](unsigned ko, int pq) {
      v4i kd = ksrd, vd = vsrd;
      kd[2] = ko == OOB ? 0 : (int)HEAD_BYTES; vd[2] = vo2 == OOB ? 0 : (int)HEAD_BYTES;
      const unsigned kso = ko == OOB ? 0u : ko, vso = vo2 == OOB ? 0u : vo2;
      dma4(kd, kso, sl, k0, k1, k2, k3); if (pq) vmwait<PQ>();
      dma4(kd, kso + 32768, sl + 4096, k0, k1, k2, k3); if (pq) vmwait<PQ>();
      dma4(vd, vso, sl + 8192, vl, vl, vl, vl); if (pq) vmwait<PQ>();
      dma4(vd, vso + 32768, sl + 12288, vl, vl, vl, vl);
      vo2 = vo1; vo1 = ko;
      sl = sl + SLOT == lb + L_RING + NSLOT * SLOT ? lb + L_RING : sl + SLOT;
    };
    auto toff = [](int t) { return ((unsigned)t >> 3) * 65536u + ((unsigned)t & 7u) * 4096u; };
    volatile LAS u64* FT = lds_at<u64>(lb + L_FT);
    int pub = 0;
    unsigned total = 0x7FFFFFFFu;
    auto publish = [&](int n) { pub = n > pub ? n : pub; *FT = (u64)(unsigned)pub | ((u64)total << 32); };
    auto wait_free = [&](int i) {
      const int p = i - NSLOT;
      volatile LAS u64* dn = lds_at<u64>(lb + L_DONE + 8 * (p & 1));
      const unsigned need = (unsigned)(p >> 1) + 1u;
      auto freed = [&]() { const u64 d = *dn; return (unsigned)d >= need && (unsigned)(d >> 32) >= need; };
      if (!freed()) {
        VM0(); publish(i);
        for (unsigned sp = 0; !freed(); ++sp) { __builtin_amdgcn_s_sleep(1); if ((sp & 63u) == 63u && rt() - t0 > TLIMIT) break; }
      }
      MEMFENCE();
    };
    const int sb = x * STAT;
    for (unsigned sp = 0; NOORDER == 0 && *lds_at<unsigned>(lb + L_ISSUED) < 5u && sp < 4096u; ++sp) {}
    STAMP(26);
    unsigned ko = toff(sb), tn = 1;
    int next = 0, left = 0;
    int i = 0;
    for (;; ++i) {
      fill(ko, USEPQ && i >= BURST && i < NSLOT);
      if (i >= BURST) publish(i - 1);
      if (i + 1 < 28) FSTAMP(i + 1);
      if (i == 2) STAMP(19);
      if (i == NSLOT - 1) STAMP(24);
      if (i == NSLOT) STAMP(27);
      if (i == 24) STAMP(13); if (i == 56) STAMP(14); if (i == 88) STAMP(15); if (i == 120) STAMP(16); if (i == 152) STAMP(23); if (i == 184) STAMP(25);
      if (i + 1 < STAT) {
        ko = toff(sb + i + 1);
        if (i == STAT - 5) { *lds_at<unsigned>(lb + L_TKC) = 1u; STAMP(17); }
      } else {
        if (left == 0) {
          u64 tv;
          for (unsigned sp = 0; (unsigned)((tv = *lds_at<u64>(lb + L_TK)) >> 32) < tn; ++sp) if (EXPIRED(sp)) { tv = NTICK; break; }
          const unsigned t = __builtin_amdgcn_readfirstlane((unsigned)tv);
          if (t >= (unsigned)NTICK) break;
          *lds_at<unsigned>(lb + L_TKC) = ++tn;
          left = t < (unsigned)T4 ? 4 : 2;
          next = t < (unsigned)T4 ? DYN0 + 4 * (int)t : DYN2 + 2 * ((int)t - T4);
        }
        ko = toff(next); ++next; --left;
      }
      if (i + 1 >= NSLOT) wait_free(i + 1);
      if (i + 1 >= BURST) vmwait<16>();
    }
    total = (unsigned)i + 3u;
    wait_free(i + 1); fill(OOB, 0);
    wait_free(i + 2); fill(OOB, 0);
    vmwait<48>(); publish(i);
    vmwait<32>(); publish(i + 1);
    vmwait<16>(); publish(i + 2);
    VM0(); publish(i + 3);
    STAMP(18);
    STAMP_FLUSH();
    return;
  }

  asm volatile("" : "+s"(w_b));
  if (w_b == 5) {
    __builtin_amdgcn_s_setprio(3);
    const rsrc_t rwm = mkrsrc(a.wm, DM * 3072 * 2), rc = mkrsrc(a.c, DM * 2);
    const int n0 = 512 * (gq & 1) + 64 * x + 16 * (j & 3);
    const int ar = 128 * kq + 4 * (lane >> 1);
    v4u w1[4];
#pragma unroll
    for (int i = 0; i < 4; ++i) w1[i] = ld128<NT>(rwm, (unsigned)((ar + i) * 3072 + n0 + 8 * (lane & 1)) * 2);
    const v2u c4 = ld64<0>(rc, ar * 2);
    const v2u ep = ld64<SC1>(ws, WS_EPOCH);
    const int mr = 64 * gq + 8 * (lane >> 3), mc = 64 * (x + 8 * (j & 3));
    const v4u cv = ld128<0>(rc, mr * 2);
    MEMFENCE();
    if (lane == 0) __hip_atomic_fetch_add(lds_pl<unsigned>(lb + L_ISSUED), 1u, __ATOMIC_RELAXED, __HIP_MEMORY_SCOPE_WORKGROUP);
    STAMP(22);
    const u64 e1 = (((u64)ep[1] << 32) | ep[0]) + 1ull;
    {
      float s8[8] = {0.f, 0.f, 0.f, 0.f, 0.f, 0.f, 0.f, 0.f};
      fma8(s8, silu_bf(lo16(c4[0])), w1[0]); fma8(s8, silu_bf(hi16(c4[0])), w1[1]);
      fma8(s8, silu_bf(lo16(c4[1])), w1[2]); fma8(s8, silu_bf(hi16(c4[1])), w1[3]);
      float u0, u1;
      rows_reduce8(s8, u0, u1);
      u0 += dpp<ROR8>(u0); u1 += dpp<ROR8>(u1); u0 += dpp<ROR4>(u0); u1 += dpp<ROR4>(u1); u0 += dpp<XOR2>(u0); u1 += dpp<XOR2>(u1);
      const unsigned tlo = (unsigned)e1 & 0xFu, thi = (unsigned)(e1 >> 4);
      const int col = n0 + 8 * (lane & 1) + 2 * (lane >> 4);
      st128(ws, (lane & 15) < 2 ? WS_P1 + (kq * 1024 + col) * 8 : OOB, v4u{gr_lo(u0, tlo), thi, gr_lo(u1, tlo), thi});
      *lds_at<unsigned>(lb + L_AUXPUB) = 1u;
    }
    STAMP(20);
    __builtin_amdgcn_s_setprio(0);
    auto stream_at = [&](unsigned fillno) {
      for (unsigned sp = 0; *lds_at<unsigned>(lb + L_FT) < fillno; ++sp) { __builtin_amdgcn_s_sleep(8); if ((sp & 63u) == 63u && rt() - t0 > TLIMIT) break; }
      MEMFENCE();
    };
    stream_at(NSLOT + 4);
    {
      const v4u e4 = {EMPTY, EMPTY, EMPTY, EMPTY};
      unsigned o = OOB;
      if (lane < 33) o = WS_PO + b * REC + lane * 16;
      else if (lane == 33) o = WS_M2 + b * 16;
      else if (lane < 42) o = mlp ? OOB : WS_YP + (og * 512 + oc0) * 4 + (lane - 34) * 16;
      else if (lane >= 48) o = WS_M2P + (gq * 2048 + mc) * 4 + (lane - 48) * 16;
      st128(ws, o, e4);
      const unsigned d = has_gu ? WS_DP + up * 2048 + lane * 16 : OOB;
      st128(ws, d, e4);
      st128(ws, d + 1024, e4);
      st32(ws, (lane == 0 && x == 0) ? WS_CTR + j * 256 : OOB, 0u);
      VM0();
      st64(ws, lane == 0 ? WS_CLR + b * 8 : OOB, v2u{(unsigned)e1, (unsigned)(e1 >> 32)});
    }
    {
      v4u w2[8];
#pragma unroll
      for (int i = 0; i < 8; ++i) w2[i] = ld128<NT>(rwm, (unsigned)((mr + i) * 3072 + 1024 + mc + 8 * (lane & 7)) * 2);
      float s8[8] = {0.f, 0.f, 0.f, 0.f, 0.f, 0.f, 0.f, 0.f};
#pragma unroll
      for (int e = 0; e < 4; ++e) { fma8(s8, silu_bf(lo16(cv[e])), w2[2 * e]); fma8(s8, silu_bf(hi16(cv[e])), w2[2 * e + 1]); }
      float u0, u1;
      rows_reduce8(s8, u0, u1);
      u0 += dpp<ROR8>(u0); u1 += dpp<ROR8>(u1);
      st64(ws, (lane & 8) == 0 ? WS_M2P + (gq * 2048 + mc + 8 * (lane & 7) + 2 * (lane >> 4)) * 4 : OOB, v2u{noempty(asu(u0)), noempty(asu(u1))});
    }
    stream_at(NSLOT + 16);
    for (;;) {
      MEMFENCE();
      const v4u f0 = ld128<SC1>(ws, WS_CLR + lane * 16), f1 = ld128<SC1>(ws, WS_CLR + 1024 + lane * 16);
      const unsigned el = (unsigned)e1, eh = (unsigned)(e1 >> 32);
      if (__all(((f0[0] ^ el) | (f0[1] ^ eh) | (f0[2] ^ el) | (f0[3] ^ eh) | (f1[0] ^ el) | (f1[1] ^ eh) | (f1[2] ^ el) | (f1[3] ^ eh)) == 0u) || rt() - t0 > TLIMIT) break;
      __builtin_amdgcn_s_sleep(32);
    }
    {
      v4u m;
      for (;;) {
        MEMFENCE();
        m = ld128<SC1>(ws, lane < 16 ? WS_M2P + ((lane >> 1) * 2048 + 8 * b + 4 * (lane & 1)) * 4 : OOB);
        if (__all(mx4(0u, m) != EMPTY) || rt() - t0 > TLIMIT) break;
        __builtin_amdgcn_s_sleep(32);
      }
      float t[4];
#pragma unroll
      for (int e = 0; e < 4; ++e) { t[e] = asf(m[e]); t[e] += dpp<ROR8>(t[e]); t[e] += dpp<ROR4>(t[e]); t[e] += dpp<XOR2>(t[e]); }
      st64(ws, lane < 2 ? WS_M2 + b * 16 + lane * 8 : OOB, v2u{noempty(cvtpk(t[0], t[1])), noempty(cvtpk(t[2], t[3]))});
    }
    stream_at(NSLOT + 28);
    if (mlp) {
      v4u m[4];
      for (;;) {
        MEMFENCE();
#pragma unroll
        for (int u = 0; u < 4; ++u) m[u] = ld128<SC1>(ws, WS_M2 + u * 1024 + lane * 16);
        if (__all(mx4(mx4(mx4(mx4(0u, m[0]), m[1]), m[2]), m[3]) != EMPTY) || rt() - t0 > TLIMIT) break;
        __builtin_amdgcn_s_sleep(32);
      }
#pragma unroll
      for (int u = 0; u < 4; ++u) *lds_pl<v4u>(lb + L_M2 + u * 1024 + lane * 16) = m[u];
      LGKM0();
      *lds_at<unsigned>(lb + L_M2RDY) = 1u;
    }
    STAMP(21);
    {
      GAS unsigned* ctr = (GAS unsigned*)(a.ws + WS_CTR + j * 256);
      for (unsigned n = 1;; ++n) {
        for (unsigned sp = 0; *lds_at<unsigned>(lb + L_TKC) < n; ++sp) { __builtin_amdgcn_s_sleep(2); if ((sp & 63u) == 63u && rt() - t0 > TLIMIT) break; }
        unsigned v = 0;
        if (lane == 0) v = __hip_atomic_fetch_add(ctr, 1u, __ATOMIC_RELAXED, __HIP_MEMORY_SCOPE_AGENT);
        v = __builtin_amdgcn_readfirstlane(v);
        *lds_at<u64>(lb + L_TK) = ((u64)n << 32) | v;
        if (v >= (unsigned)NTICK || rt() - t0 > TLIMIT) break;
      }
    }
    STAMP_FLUSH();
    return;
  }

  const int g = lane >> 4, c = lane & 15;
  const rsrc_t rx = mkrsrc(a.x, DM * 2), rwq = mkrsrc(a.wq, DM * 4096 * 2);
  const rsrc_t rwd = mkrsrc(a.wd, DF * DM * 2);
  const int r0 = 128 * kq + 32 * wave;
  v4u wqv[4];
#pragma unroll
  for (int i = 0; i < 4; ++i) wqv[i] = ld128<NT>(rwq, (unsigned)((r0 + 4 * (lane >> 3) + i) * 4096 + 64 * cb + 8 * (lane & 7)) * 2);
  const v2u ep = ld64<SC1>(ws, WS_EPOCH);
  const v4u x8 = ld128<0>(rx, lane * 16);
  const unsigned x2 = ld32<0>(rx, tid * 4);
  const unsigned xh = ld32<0>(rx, (r0 + 2 * c) * 2);
  MEMFENCE();
  if (lane == 0) __hip_atomic_fetch_add(lds_pl<unsigned>(lb + L_ISSUED), 1u, __ATOMIC_RELAXED, __HIP_MEMORY_SCOPE_WORKGROUP);
  float mb, sd;
  {
    float xs[8];
#pragma unroll
    for (int e = 0; e < 4; ++e) { xs[2 * e] = lo16(x8[e]); xs[2 * e + 1] = hi16(x8[e]); }
    float s = 0.f;
#pragma unroll
    for (int e = 0; e < 8; ++e) s += xs[e];
    const float mean = wave_sum(s) * (1.f / 512.f);
    float v = 0.f;
#pragma unroll
    for (int e = 0; e < 8; ++e) { const float d = xs[e] - mean; v = fmaf(d, d, v); }
    ln_round(mean, wave_sum(v) * (1.f / 512.f), mb, sd);
  }
  float nrow = rbf((lane >= 32 ? hi16(xh) : lo16(xh)) - mb) / sd;
  kpair(wqv[0], wqv[1]); kpair(wqv[2], wqv[3]);
  PIN4(wqv); asm volatile("" : "+v"(nrow));
  const u64 epoch1 = (((u64)ep[1] << 32) | ep[0]) + 1ull;
  const unsigned tlo = (unsigned)epoch1 & 0xFu, thi = (unsigned)(epoch1 >> 4);
  lds_wait_ge(lb + L_AUXPUB, 1u, t0);
  {
    const unsigned o1 = WS_P1 + (g * 1024 + r0 + 2 * c) * 8;
    v4u gs = {0u, 0u, 0u, 0u}, gc = gs;
    bool bad = true;
    if (P1WAIT) __builtin_amdgcn_s_sleep(P1WAIT);
    for (;;) {
      MEMFENCE();
      const v4u ns_ = ld128<SC1>(ws, bad ? o1 : OOB), nc_ = ld128<SC1>(ws, bad ? o1 + 4096 : OOB);
      const bool tk = bad & ((gr_bad(ns_, tlo, thi) | gr_bad(nc_, tlo, thi)) == 0u);
      gs = sel4(tk, ns_, gs); gc = sel4(tk, nc_, gc); bad = bad & !tk;
      if (!__any(bad) || rt() - t0 > TLIMIT) break;
      if (P1SLP) __builtin_amdgcn_s_sleep(P1SLP);
    }
    STAMP(1);
    const unsigned hp = pack_halves(mod_round(nrow, rbf(gr_sum(gc)), rbf(gr_sum(gs))));
    float acc[8] = {0.f, 0.f, 0.f, 0.f, 0.f, 0.f, 0.f, 0.f};
    dot8(acc, __builtin_amdgcn_ds_bpermute((lane >> 3) * 8, hp), wqv[0], wqv[1]);
    dot8(acc, __builtin_amdgcn_ds_bpermute((lane >> 3) * 8 + 4, hp), wqv[2], wqv[3]);
    float u0, u1;
    rows_reduce8(acc, u0, u1);
    u0 += dpp<ROR8>(u0); u1 += dpp<ROR8>(u1);
    if ((lane & 8) == 0) *lds_pl<f32x2>(lb + L_RED + wave * 256 + (8 * (lane & 7) + 2 * g) * 4) = f32x2{u0, u1};
    LGKM0();
    unsigned old = 0;
    if (lane == 0) old = __hip_atomic_fetch_add(lds_pl<unsigned>(lb + L_QCNT), 1u, __ATOMIC_RELAXED, __HIP_MEMORY_SCOPE_WORKGROUP);
    old = __builtin_amdgcn_readfirstlane(old);
    MEMFENCE();
    if (old == 3u) {
      const unsigned ra = lb + L_RED + lane * 4;
      const float qp = (*lds_pl<float>(ra) + *lds_pl<float>(ra + 256)) + (*lds_pl<float>(ra + 512) + *lds_pl<float>(ra + 768));
      st64(ws, WS_QS + (kq * 4096 + 64 * cb + lane) * 8, v2u{gr_lo(qp, tlo), thi});
      STAMP(2);
    }
  }
  const int r8 = 8 * (tid >> 2);
  v4u wv[8];
  unsigned wdv[16];
#pragma unroll
  for (int i = 0; i < 8; ++i) { wv[i] = v4u{0u, 0u, 0u, 0u}; wdv[2 * i] = 0u; wdv[2 * i + 1] = 0u; }
  const GAS v4u* wp = (const GAS v4u*)(mlp ? ((tid & 2) ? a.wu : a.wg) + (r8 * DF + 16 * (has_gu ? up : 0) + 8 * (tid & 1)) : a.wo + ((512 * og + r8) * DM + oc0 + 8 * (tid & 3)));
  const int wstride = mlp ? DF / 8 : DM / 8;
  const unsigned wdo = has_gu ? (unsigned)(16 * up * DM + 2 * tid) * 2 : OOB;

  const unsigned key = 4 * (c & 3) + (c >> 2);
  unsigned ka[4], msk[4];
#pragma unroll
  for (int st = 0; st < 4; ++st) {
    ka[st] = lb + L_RING + (wave & 1) * 4096 + key * 256 + (((st + 4 * g) ^ key) << 4);
    msk[st] = ((c & 7) >> 1) == st ? ((c & 1) ? 0xffff0000u : 0x0000ffffu) : 0u;
  }
  const unsigned va = lb + L_RING + 8192 + (wave & 1) * 4096 + lane * 16;
  const bool is_hi = c < 8;
  volatile LAS u64* FT = lds_at<u64>(lb + L_FT);
  volatile LAS unsigned* DN = lds_at<unsigned>(lb + L_DONE + 4 * wave);
  unsigned so = (wave >> 1) * SLOT;
  v4u sv[8], sw[8], sx[8];
#pragma unroll
  for (int i = 0; i < 8; ++i) { sv[i] = v4u{0u, 0u, 0u, 0u}; sw[i] = v4u{0u, 0u, 0u, 0u}; sx[i] = v4u{0u, 0u, 0u, 0u}; }
  int ns = 0;
  auto read_unit = [&](v4u (&d)[8], int i) {
#pragma unroll
    for (int u = 0; u < 4; ++u) d[u] = *lds_pl<v4u>(va + so + u * 1024);
#pragma unroll
    for (int st = 0; st < 4; ++st) d[4 + st] = *lds_pl<v4u>(ka[st] + so);
    LGKM0();
    *DN = (unsigned)(i >> 1) + 1u;
    asm volatile("" : "+v"(d[0]), "+v"(d[1]), "+v"(d[2]), "+v"(d[3]), "+v"(d[4]), "+v"(d[5]), "+v"(d[6]), "+v"(d[7]) :: "memory");
    so += 2 * SLOT; so = so >= NSLOT * SLOT ? so - NSLOT * SLOT : so;
  };
  float m2 = -INFINITY, lsum = 0.f;
  f32x4 acc = {0.f, 0.f, 0.f, 0.f};
  int total = 0x7FFFFFFF;
  v4u pa[4] = {v4u{0u, 0u, 0u, 0u}, v4u{0u, 0u, 0u, 0u}, v4u{0u, 0u, 0u, 0u}, v4u{0u, 0u, 0u, 0u}}, qf[4], ud[8];
  auto values = [&](const v4u (&d)[8]) {
#pragma unroll
    for (int u = 0; u < 4; ++u) acc = mfma(pa[u], d[u], acc);
  };
  auto scores = [&](const v4u (&d)[8]) {
    f32x4 sa = mfma(d[4], qf[0], f32x4{0.f, 0.f, 0.f, 0.f}), sb4 = mfma(d[5], qf[1], f32x4{0.f, 0.f, 0.f, 0.f});
    sa = mfma(d[6], qf[2], sa); sb4 = mfma(d[7], qf[3], sb4);
    sa += sb4;
    float s[4];
#pragma unroll
    for (int u = 0; u < 2; ++u) {
      const unsigned p1 = cvtpk(sa[2 * u], sa[2 * u + 1]);
      const unsigned p2 = cvtpk(lo16(p1) * SCALE, hi16(p1) * SCALE);
      s[2 * u] = lo16(p2); s[2 * u + 1] = hi16(p2);
    }
    const float mx = xrow_max(fmaxf(fmaxf(s[0], s[1]), fmaxf(s[2], s[3])));
    const float mn = fmaxf(m2, mx * LOG2E);
    const float alpha = __builtin_amdgcn_exp2f(m2 - mn);
    m2 = mn;
    acc *= alpha;
    float ps = 0.f;
#pragma unroll
    for (int u = 0; u < 4; ++u) {
      const float p = __builtin_amdgcn_exp2f(fmaf(s[u], LOG2E, -mn));
      ps += p;
      const float ph = hi16(asu(p));
      const float xv = is_hi ? ph : p - ph;
      const unsigned pk = cvtpk(xv, xv);
      pa[u] = v4u{pk & msk[0], pk & msk[1], pk & msk[2], pk & msk[3]};
    }
    lsum = fmaf(lsum, alpha, ps);
  };
  auto stash = [&]() {
    const int full = __builtin_amdgcn_readfirstlane((int)*lds_at<unsigned>(lb + L_FT)), i0 = wave >> 1;
    MEMFENCE();
    if (NSTASH >= 1 && ns == 0 && full > i0) { read_unit(wv, i0); ns = 1; }
    if (NSTASH >= 2 && ns == 1 && full > i0 + 2) { read_unit(sv, i0 + 2); ns = 2; }
    if (NSTASH >= 3 && ns == 2 && full > i0 + 4) { read_unit(sw, i0 + 4); ns = 3; }
    if (NSTASH == 4 && ns == 3 && full > i0 + 6) { read_unit(ud, i0 + 6); ns = 4; }
    if (NSTASH == 5 && ns == 3 && full > i0 + 6) { read_unit(sx, i0 + 6); ns = 4; }
    if (NSTASH == 5 && ns == 4 && full > i0 + 8) { read_unit(ud, i0 + 8); ns = 5; }
  };
#pragma unroll
  for (int i = 0; i < 8; ++i) ud[i] = v4u{0u, 0u, 0u, 0u};
  {
    const unsigned o2 = WS_QS + (g * 4096 + 128 * j + 32 * wave + 2 * c) * 8;
    v4u gq4 = {0u, 0u, 0u, 0u};
    bool bad = true;
    stash();
    if (QWAIT) __builtin_amdgcn_s_sleep(QWAIT);
    {
      const v4u nq = ld128<SC1>(ws, o2);
      bad = gr_bad(nq, tlo, thi) != 0u;
      gq4 = nq;
    }
    asm volatile("" : "+v"(gq4) :: "memory");
    stash();
    for (unsigned it = 0; __any(bad) && !EXPIRED(it); ++it) {
      MEMFENCE();
      const v4u nq = ld128<SC1>(ws, bad ? o2 : OOB);
      const bool tk = bad & (gr_bad(nq, tlo, thi) == 0u);
      gq4 = sel4(tk, nq, gq4); bad = bad & !tk;
    }
    *lds_pl<unsigned>(lb + L_QSC + (16 * wave + c) * 4) = pack_halves(rbf(gr_sum(gq4)));
    LGKM0();
    if (lane == 0) __hip_atomic_fetch_add(lds_pl<unsigned>(lb + L_QIN), 1u, __ATOMIC_RELAXED, __HIP_MEMORY_SCOPE_WORKGROUP);
    lds_wait_ge(lb + L_QIN, 4u, t0);
#pragma unroll
    for (int st = 0; st < 4; ++st) qf[st] = *lds_pl<v4u>(lb + L_QSC + (st + 4 * g) * 16);
  }
  STAMP(3); STAMPC(28);

  if (ns >= 1) scores(wv);
  if (ns >= 2) { values(sv); scores(sv); }
  if (ns >= 3) { values(sw); scores(sw); }
  if (NSTASH == 5 && ns >= 4) { values(sx); scores(sx); }
  {
    int wk = (mlp && !has_gu) ? 11 : 0;
    bool done = false;
    constexpr int NPRE = NSTASH == 5 ? 4 : 3;
    bool have = ns > NPRE;
    for (int i = (wave >> 1) + 2 * (ns < NPRE ? ns : NPRE);; i += 2) {
      if (!done) {
        if (!have) {
          for (unsigned sp = 0;; ++sp) {
            const u64 ft = *FT;
            const int full = __builtin_amdgcn_readfirstlane((int)(unsigned)ft);
            total = __builtin_amdgcn_readfirstlane((int)(unsigned)(ft >> 32));
            if (EXPIRED(sp)) total = 0;
            if (full > i || total == 0) break;
            if (total == 0x7FFFFFFF) __builtin_amdgcn_s_sleep(CSLEEP);
          }
          MEMFENCE();
          read_unit(ud, i);
        }
        have = false;
        if (i >= 2) values(ud);
        done = i + 2 >= total;
        if (!done) scores(ud);
      }
      MEMFENCE();
      if (wk <= 10 && (done || rt() - t0 >= (u64)(TW + TD * wk + (wk == 10 ? 400 : 0)))) {
#pragma unroll
        for (int u = 0; u < 8; ++u) if (wk == u) wv[u] = __builtin_nontemporal_load(wp + (XP_NOW ? 0 : u * wstride));
#pragma unroll
        for (int v = 0; v < 2; ++v) if (wk == 8 + v) {
#pragma unroll
          for (int u = 8 * v; u < 8 * v + 8; ++u) wdv[u] = ld32<NT>(rwd, wdo + u * (DM * 2));
        }
        if (wk == 10) {
#pragma unroll
          for (int u = 0; u < 8; u += 2) kpair(wv[u], wv[u + 1]);
#pragma unroll
          for (int u = 0; u < 16; u += 2) {
            const unsigned lo = __builtin_amdgcn_perm(wdv[u + 1], wdv[u], 0x05040100u), hi = __builtin_amdgcn_perm(wdv[u + 1], wdv[u], 0x07060302u);
            wdv[u] = lo; wdv[u + 1] = hi;
          }
        }
        ++wk;
      }
      MEMFENCE();
      if (done && wk > 10) break;
    }
  }
  STAMP(4); STAMPC(29);
  {
    f32x4 o;
#pragma unroll
    for (int r = 0; r < 4; ++r) o[r] = sw32_add(acc[r], acc[r]);
    float l = sw16_add(lsum, lsum); l = sw32_add(l, l);
    *lds_pl<f32x4>(lb + L_MRG + wave * 512 + (8 * c + 4 * (g & 1)) * 4) = o;
    *lds_pl<f32x2>(lb + L_ML + wave * 8) = f32x2{m2, l};
    wg_barrier();
    if (wave == 0) {
      const unsigned ra = lb + L_MRG + (lane & 31) * 16;
      f32x4 mA = *lds_pl<f32x4>(lb + L_ML), mB = *lds_pl<f32x4>(lb + L_ML + 16);
      f32x4 o0 = *lds_pl<f32x4>(ra), o1 = *lds_pl<f32x4>(ra + 512), o2 = *lds_pl<f32x4>(ra + 1024), o3 = *lds_pl<f32x4>(ra + 1536);
      asm volatile("" : "+v"(mA), "+v"(mB), "+v"(o0), "+v"(o1), "+v"(o2), "+v"(o3));
      const float M = fmaxf(fmaxf(mA[0], mA[2]), fmaxf(mB[0], mB[2]));
      const float w0 = __builtin_amdgcn_exp2f(mA[0] - M), w1 = __builtin_amdgcn_exp2f(mA[2] - M), w2 = __builtin_amdgcn_exp2f(mB[0] - M), w3 = __builtin_amdgcn_exp2f(mB[2] - M);
      const float L = (w0 * mA[1] + w1 * mA[3]) + (w2 * mB[1] + w3 * mB[3]);
      f32x4 O = (w0 * o0 + w1 * o1) + (w2 * o2 + w3 * o3);
      if (lane >= 32) O = f32x4{M, L, 0.f, 0.f};
      st128(ws, lane < 33 ? WS_PO + b * REC + lane * 16 : OOB, noempty4(O));
      STAMP(5);
    }
  }
  if (!mlp) {
    const unsigned hb = WS_PO + (4 * og + wave) * 8 * REC;
    v2u po[8], ml = {EMPTY, EMPTY};
#pragma unroll
    for (int s = 0; s < 8; ++s) po[s] = v2u{0u, 0u};
    unsigned miss = 0x1FFu;
    for (unsigned it = 0;; ++it) {
      MEMFENCE();
      if (miss & 0x100u) ml = ld64<SC1>(ws, lane < 8 ? hb + lane * REC + 512 : OOB);
#pragma unroll
      for (int s = 0; s < 8; ++s) if (miss & (1u << s)) po[s] = ld64<SC1>(ws, hb + s * REC + lane * 8);
      if ((miss & 0x100u) && __all(mx2(0u, ml) != EMPTY)) miss &= ~0x100u;
#pragma unroll
      for (int s = 0; s < 8; ++s) if ((miss & (1u << s)) && __all(mx2(0u, po[s]) != EMPTY)) miss &= ~(1u << s);
      if (miss == 0u || EXPIRED(it)) break;
    }
    STAMP(6);
    const float ms = lane < 8 ? asf(ml[0]) : -INFINITY;
    const float wt = __builtin_amdgcn_exp2f(ms - row_max(ms));
    const float wn = wt * __builtin_amdgcn_rcpf(row_sum(wt * asf(ml[1])));
    float oa = 0.f, ob = 0.f;
#pragma unroll
    for (int s = 0; s < 8; ++s) {
      const float w = asf(__builtin_amdgcn_readlane(asu(wn), s));
      oa = fmaf(w, asf(po[s][0]), oa); ob = fmaf(w, asf(po[s][1]), ob);
    }
    const unsigned op = cvtpk(oa, ob);
    float y[8] = {0.f, 0.f, 0.f, 0.f, 0.f, 0.f, 0.f, 0.f};
    dot8(y, dppu<0x00>(op), wv[0], wv[1]); dot8(y, dppu<0x55>(op), wv[2], wv[3]);
    dot8(y, dppu<0xAA>(op), wv[4], wv[5]); dot8(y, dppu<0xFF>(op), wv[6], wv[7]);
    float u0, u1;
    rows_reduce8(y, u0, u1);
    u0 += dpp<ROR8>(u0); u1 += dpp<ROR8>(u1); u0 += dpp<ROR4>(u0); u1 += dpp<ROR4>(u1);
    *lds_pl<f32x2>(lb + L_RED3 + wave * 128 + (8 * (c & 3) + 2 * g) * 4) = f32x2{u0, u1};
    wg_barrier();
    if (wave == 0) {
      const unsigned ra = lb + L_RED3 + (lane & 7) * 16;
      const f32x4 t = (*lds_pl<f32x4>(ra) + *lds_pl<f32x4>(ra + 128)) + (*lds_pl<f32x4>(ra + 256) + *lds_pl<f32x4>(ra + 384));
      st128(ws, lane < 8 ? WS_YP + (og * 512 + oc0) * 4 + lane * 16 : OOB, noempty4(t));
      STAMP(7);
    }
    STAMP_FLUSH();
    return;
  }
  lds_wait_ge(lb + L_M2RDY, 1u, t0);
  float gbf = lo16(*lds_pl<u16>(lb + L_M2 + 3072 + (fc0 + g) * 2));
  {
    const unsigned gaw = *lds_pl<unsigned>(lb + L_M2 + tid * 4), sbw = *lds_pl<unsigned>(lb + L_M2 + 1024 + tid * 4), cbw = *lds_pl<unsigned>(lb + L_M2 + 2048 + tid * 4);
    float ga0 = lo16(gaw), ga1 = hi16(gaw), sb0 = lo16(sbw), sb1 = hi16(sbw), t0c = rbf(1.f + lo16(cbw)), t1c = rbf(1.f + hi16(cbw)), xa = lo16(x2), xb = hi16(x2);
    asm volatile("" : "+v"(ga0), "+v"(ga1), "+v"(sb0), "+v"(sb1), "+v"(t0c), "+v"(t1c), "+v"(xa), "+v"(xb), "+v"(gbf));
    v2u yp[8];
#pragma unroll
    for (int s = 0; s < 8; ++s) yp[s] = v2u{0u, 0u};
    unsigned miss = 0xFFu;
    if (BWAIT) __builtin_amdgcn_s_sleep(BWAIT);
    for (unsigned it = 0;; ++it) {
      MEMFENCE();
#pragma unroll
      for (int s = 0; s < 8; ++s) if (miss & (1u << s)) yp[s] = ld64<SC1>(ws, WS_YP + s * 2048 + tid * 8);
#pragma unroll
      for (int s = 0; s < 8; ++s) if ((miss & (1u << s)) && __all(mx2(0u, yp[s]) != EMPTY)) miss &= ~(1u << s);
      if (miss == 0u || EXPIRED(it)) break;
    }
    STAMP(8);
    const unsigned atp = cvtpk(((asf(yp[0][0]) + asf(yp[1][0])) + (asf(yp[2][0]) + asf(yp[3][0]))) + ((asf(yp[4][0]) + asf(yp[5][0])) + (asf(yp[6][0]) + asf(yp[7][0]))),
                               ((asf(yp[0][1]) + asf(yp[1][1])) + (asf(yp[2][1]) + asf(yp[3][1]))) + ((asf(yp[4][1]) + asf(yp[5][1])) + (asf(yp[6][1]) + asf(yp[7][1]))));
    const unsigned gap = cvtpk(ga0 * lo16(atp), ga1 * hi16(atp));
    const unsigned x1p = cvtpk(xa + lo16(gap), xb + hi16(gap));
    const float x1a = lo16(x1p), x1b = hi16(x1p);
    const float sh = asf(__builtin_amdgcn_readfirstlane(asu(x1a)));
    const float da = x1a - sh, db = x1b - sh;
    const float s1 = wave_sum(da + db), s2 = wave_sum(fmaf(da, da, db * db));
    *lds_pl<f32x2>(lb + L_ST + wave * 8) = f32x2{fmaf(s1, 1.f / 128.f, sh), fmaf(-s1 * (1.f / 128.f), s1, s2)};
    *lds_pl<f32x2>(lb + L_X1 + tid * 8) = f32x2{x1a, x1b};
    wg_barrier();
    STAMP(12);
    if (has_gu) {
      const f32x4 sA = *lds_pl<f32x4>(lb + L_ST), sB = *lds_pl<f32x4>(lb + L_ST + 16);
      const float mean = ((sA[0] + sA[2]) + (sB[0] + sB[2])) * 0.25f;
      const float d0 = sA[0] - mean, d1 = sA[2] - mean, d2 = sB[0] - mean, d3 = sB[2] - mean;
      const float var = fmaxf(((sA[1] + sA[3]) + (sB[1] + sB[3])) + 128.f * ((d0 * d0 + d1 * d1) + (d2 * d2 + d3 * d3)), 0.f) * (1.f / 512.f);
      float mb2, sd2;
      ln_round(mean, var, mb2, sd2);
      const float rs = __builtin_amdgcn_rcpf(sd2);
      const unsigned e0 = cvtpk(x1a - mb2, x1b - mb2), e1 = cvtpk(lo16(e0) * rs, hi16(e0) * rs), e2 = cvtpk(lo16(e1) * t0c, hi16(e1) * t1c);
      const unsigned hq = cvtpk(lo16(e2) + sb0, hi16(e2) + sb1);
      float y[8] = {0.f, 0.f, 0.f, 0.f, 0.f, 0.f, 0.f, 0.f};
      dot8(y, dppu<0x00>(hq), wv[0], wv[1]); dot8(y, dppu<0x55>(hq), wv[2], wv[3]);
      dot8(y, dppu<0xAA>(hq), wv[4], wv[5]); dot8(y, dppu<0xFF>(hq), wv[6], wv[7]);
      float u0, u1;
      rows_reduce8(y, u0, u1);
      u0 += dpp<ROR8>(u0); u1 += dpp<ROR8>(u1); u0 += dpp<ROR4>(u0); u1 += dpp<ROR4>(u1);
      *lds_pl<f32x2>(lb + L_RED3 + wave * 128 + (8 * (c & 3) + 2 * g) * 4) = f32x2{u0, u1};
      wg_barrier();
      const unsigned ra = lb + L_RED3 + c * 4;
      const float gs = rbf((*lds_pl<float>(ra) + *lds_pl<float>(ra + 128)) + (*lds_pl<float>(ra + 256) + *lds_pl<float>(ra + 384)));
      const float us = rbf((*lds_pl<float>(ra + 64) + *lds_pl<float>(ra + 192)) + (*lds_pl<float>(ra + 320) + *lds_pl<float>(ra + 448)));
      const float z = 0.7978845608028654f * fmaf(0.044715f * gs * gs, gs, gs);
      const float th = fmaf(-2.f, __builtin_amdgcn_rcpf(1.f + __expf(2.f * z)), 1.f);
      const float h2 = rbf(rbf(0.5f * gs * (1.f + th)) * us);
      const unsigned hp = __builtin_amdgcn_perm(asu(dpp<XOR1>(h2)), asu(h2), 0x07060302u);
      float d0a = 0.f, d1a = 0.f;
#pragma unroll
      for (int e = 0; e < 16; e += 2) {
        const unsigned he = __builtin_amdgcn_readlane(hp, e);
        d0a = dot2(wdv[e], he, d0a); d1a = dot2(wdv[e + 1], he, d1a);
      }
      st64(ws, WS_DP + up * 2048 + tid * 8, v2u{noempty(asu(d0a)), noempty(asu(d1a))});
      st64(ws, (b | tid) == 0 ? WS_EPOCH : OOB, v2u{(unsigned)epoch1, (unsigned)(epoch1 >> 32)});
      STAMP(9);
    }
  }
  if (wave != 0) { STAMP_FLUSH(); return; }
  {
    v4u da = {EMPTY, 0u, 0u, 0u}, db = {lane < NGU - 64 ? EMPTY : 0u, 0u, 0u, 0u};
    const float x1o = *lds_pl<float>(lb + L_X1 + (fc0 + g) * 4);
    for (unsigned it = 0;; ++it) {
      MEMFENCE();
      const bool ma = mx4(0u, da) == EMPTY, mb_ = mx4(0u, db) == EMPTY;
      if (!__any(ma | mb_) || EXPIRED(it)) break;
      const v4u na = ld128<SC1>(ws, ma ? WS_DP + lane * 2048 + fc0 * 4 : OOB), nb = ld128<SC1>(ws, mb_ ? WS_DP + (lane + 64) * 2048 + fc0 * 4 : OOB);
      da = ma ? na : da; db = mb_ ? nb : db;
    }
    STAMP(10);
    float t4[4];
#pragma unroll
    for (int r = 0; r < 4; ++r) t4[r] = asf(da[r]) + asf(db[r]);
    const float r = row_sum(sw16_add(sw32_add(t4[0], t4[2]), sw32_add(t4[1], t4[3])));
    const float res = x1o + rbf(gbf * rbf(r));
    if (c == 0) a.out[fc0 + g] = (u16)(cvtpk(res, res) & 0xffffu);
    STAMP(11);
    STAMP_FLUSH();
  }
}
}

extern "C" void kernel_launch(void* const* d_in, const int* in_sizes, int n_in, void* d_out, int out_size, void* d_ws, size_t ws_size,
                              hipStream_t stream) {
  Args a;
  a.x = (const u16*)d_in[0]; a.K = (const u16*)d_in[1]; a.V = (const u16*)d_in[2]; a.wq = (const u16*)d_in[3]; a.wo = (const u16*)d_in[4];
  a.wg = (const u16*)d_in[6]; a.wu = (const u16*)d_in[7]; a.wd = (const u16*)d_in[8]; a.c = (const u16*)d_in[9]; a.wm = (const u16*)d_in[10];
  a.out = (u16*)d_out; a.ws = (unsigned char*)d_ws;
  (void)hipFuncSetAttribute((const void*)block_decode, hipFuncAttributeMaxDynamicSharedMemorySize, (int)LDS_BYTES);
  hipLaunchKernelGGL(block_decode, dim3(NWG), dim3(NTHR), LDS_BYTES, stream, a);
}
```

```cpp
#include <hip/hip_runtime.h>
#include <hip/hip_bf16.h>

#define GAS __attribute__((address_space(1)))
#define LAS __attribute__((address_space(3)))
typedef unsigned short u16;
typedef unsigned long long u64;
typedef unsigned v4u __attribute__((ext_vector_type(4)));
typedef unsigned v2u __attribute__((ext_vector_type(2)));
typedef int v4i __attribute__((ext_vector_type(4)));
typedef float f32x4 __attribute__((ext_vector_type(4)));
typedef __bf16 bf16x8 __attribute__((ext_vector_type(8)));
typedef __bf16 bf16x2 __attribute__((ext_vector_type(2)));
typedef float f32x2 __attribute__((ext_vector_type(2)));

namespace {
constexpr int DM = 512, SK = 65536, DH = 128, DF = 1792;
constexpr int NWG = 256, NTHR = 384;
constexpr int SLOT = 16384, NSLOT = 8, FH = 1024;
constexpr int SO = 112;
constexpr int NP = 8;
constexpr int POOL0 = 8 * SO, KD0 = POOL0 + 8 * NP, NKT = (FH - KD0) / 4, NPT = 8 * NP / 2;
constexpr int CAP = 144;
static_assert((FH - KD0) % 4 == 0 && SO % 4 == 0 && NP % 2 == 0 && 8 * NP + 16 <= SO && NP <= 8 && 8 * (CAP - 7 - SO) >= FH - KD0, "fill plan");
constexpr int BURST = 3;
constexpr unsigned TW = 10800, TD = 110;
constexpr int INF = 0x7FFFFFFF;
constexpr unsigned HEAD_BYTES = (unsigned)SK * DH * 2;
constexpr unsigned EMPTY = 0xFFFFFFFFu;
constexpr unsigned OOB = 0x7FFF0000u;
constexpr u64 TLIMIT = 5000000ull;
constexpr float LOG2E = 1.4426950408889634f, SCALE = 0.08838834764831845f;
constexpr int NGU = DF / 16, REC = 640;
constexpr unsigned WS_EPOCH = 0, WS_P1 = 4096, WS_QS = WS_P1 + 32768, WS_M2P = WS_QS + 131072, WS_M2 = WS_M2P + 131072, WS_CTR = WS_M2 + 16384,
                   WS_MX = WS_CTR + 8192, WS_YP = WS_MX + 2048, WS_PO = WS_YP + 16384, WS_DP = WS_PO + NWG * REC,
                   WS_SG = WS_DP + NGU * 2048, WS_END = WS_SG + NWG * NP * 128;
constexpr unsigned L_RING = 0, L_CTL = NSLOT * SLOT, L_FT = L_CTL, L_SCR = L_CTL + 8, L_NK = L_CTL + 12, L_DONE = L_CTL + 16, L_QCNT = L_CTL + 32,
                   L_AUXPUB = L_CTL + 36, L_QIN = L_CTL + 40, L_ISSUED = L_CTL + 44, L_M2RDY = L_CTL + 48, L_TKC = L_CTL + 52, L_TK = L_CTL + 56,
                   L_KCNT = L_CTL + 64, L_MH = L_CTL + 68, L_MXW = L_CTL + 80, L_LW = L_CTL + 96, L_LIST = L_CTL + 128, L_RED = L_LIST + 128, L_QSC = L_RED + 1024, L_RED3 = L_QSC + 256,
                   L_ST = L_RED3 + 512, L_M2 = L_ST + 64, L_X1 = L_M2 + 4096, L_MRG = L_X1 + 2048, L_SH = L_X1, L_SC = L_MRG + 2048, L_END = L_SC + CAP * 128;
constexpr unsigned LDS_BYTES = L_END;
static_assert(LDS_BYTES <= 160 * 1024 && LDS_BYTES > 80 * 1024, "one workgroup per CU");

struct Args {
  const u16 *x, *K, *V, *wq, *wo, *wg, *wu, *wd, *c, *wm;
  u16* out;
  unsigned char* ws;
};

__device__ __forceinline__ u64 rt() { return __builtin_amdgcn_s_memrealtime(); }
__device__ __forceinline__ float asf(unsigned u) { return __builtin_bit_cast(float, u); }
__device__ __forceinline__ unsigned asu(float f) { return __builtin_bit_cast(unsigned, f); }
__device__ __forceinline__ float lo16(unsigned u) { return asf(u << 16); }
__device__ __forceinline__ float hi16(unsigned u) { return asf(u & 0xffff0000u); }
__device__ __forceinline__ unsigned cvtpk(float a, float b) {
  const f32x2 v = {a, b};
  return __builtin_bit_cast(unsigned, __builtin_convertvector(v, bf16x2));
}
__device__ __forceinline__ float rbf(float f) { return lo16(cvtpk(f, f)); }
__device__ __forceinline__ unsigned noempty(unsigned v) { return v < EMPTY - 1u ? v : EMPTY - 1u; }
__device__ __forceinline__ float dot2(unsigned a, unsigned b, float c) {
  return __builtin_amdgcn_fdot2_f32_bf16(__builtin_bit_cast(bf16x2, a), __builtin_bit_cast(bf16x2, b), c, false);
}
template <int CTRL> __device__ __forceinline__ float dpp(float v) {
  return asf(__builtin_amdgcn_mov_dpp(asu(v), CTRL, 0xf, 0xf, true));
}
constexpr int ROR8 = 0x128, ROR4 = 0x124, ROR2 = 0x122, ROR1 = 0x121, XOR2 = 0x4E, XOR1 = 0xB1;
template <int CTRL> __device__ __forceinline__ unsigned dppu(unsigned v) { return __builtin_amdgcn_mov_dpp(v, CTRL, 0xf, 0xf, true); }
__device__ __forceinline__ float sw32_add(float a, float b) {
  const auto r = __builtin_amdgcn_permlane32_swap(asu(a), asu(b), false, false);
  return asf(r[0]) + asf(r[1]);
}
__device__ __forceinline__ float sw16_add(float a, float b) {
  const auto r = __builtin_amdgcn_permlane16_swap(asu(a), asu(b), false, false);
  return asf(r[0]) + asf(r[1]);
}
__device__ __forceinline__ float xrow_max(float v) {
  const auto r = __builtin_amdgcn_permlane16_swap(asu(v), asu(v), false, false);
  v = fmaxf(asf(r[0]), asf(r[1]));
  const auto s = __builtin_amdgcn_permlane32_swap(asu(v), asu(v), false, false);
  return fmaxf(asf(s[0]), asf(s[1]));
}
__device__ __forceinline__ unsigned pack_halves(float v) {
  const auto r = __builtin_amdgcn_permlane32_swap(asu(v), asu(v), false, false);
  return __builtin_amdgcn_perm(r[1], r[0], 0x07060302u);
}
__device__ __forceinline__ float row_sum(float v) { v += dpp<ROR8>(v); v += dpp<ROR4>(v); v += dpp<ROR2>(v); v += dpp<ROR1>(v); return v; }
__device__ __forceinline__ float row_max(float v) { v = fmaxf(v, dpp<ROR8>(v)); v = fmaxf(v, dpp<ROR4>(v)); v = fmaxf(v, dpp<ROR2>(v)); return fmaxf(v, dpp<ROR1>(v)); }
__device__ __forceinline__ float wave_sum(float v) { v = row_sum(v); v = sw16_add(v, v); return sw32_add(v, v); }
__device__ __forceinline__ void rows_reduce8(const float (&a)[8], float& u0, float& u1) {
  const float r0 = sw32_add(a[0], a[4]), r1 = sw32_add(a[1], a[5]), r2 = sw32_add(a[2], a[6]), r3 = sw32_add(a[3], a[7]);
  u0 = sw16_add(r0, r2); u1 = sw16_add(r1, r3);
}
__device__ __forceinline__ void fma8(float (&a)[8], float h, v4u w) {
#pragma unroll
  for (int e = 0; e < 4; ++e) { a[2 * e] = fmaf(h, lo16(w[e]), a[2 * e]); a[2 * e + 1] = fmaf(h, hi16(w[e]), a[2 * e + 1]); }
}
__device__ __forceinline__ void kpair(v4u& a, v4u& b) {
  const v4u r0 = a, r1 = b;
  a = v4u{__builtin_amdgcn_perm(r1[0], r0[0], 0x05040100u), __builtin_amdgcn_perm(r1[0], r0[0], 0x07060302u),
          __builtin_amdgcn_perm(r1[1], r0[1], 0x05040100u), __builtin_amdgcn_perm(r1[1], r0[1], 0x07060302u)};
  b = v4u{__builtin_amdgcn_perm(r1[2], r0[2], 0x05040100u), __builtin_amdgcn_perm(r1[2], r0[2], 0x07060302u),
          __builtin_amdgcn_perm(r1[3], r0[3], 0x05040100u), __builtin_amdgcn_perm(r1[3], r0[3], 0x07060302u)};
}
__device__ __forceinline__ void dot8(float (&acc)[8], unsigned hp, v4u a, v4u b) {
#pragma unroll
  for (int e = 0; e < 4; ++e) { acc[e] = dot2(a[e], hp, acc[e]); acc[4 + e] = dot2(b[e], hp, acc[4 + e]); }
}
__device__ __forceinline__ f32x4 mfma(v4u a, v4u b, f32x4 c) {
  return __builtin_amdgcn_mfma_f32_16x16x32_bf16(__builtin_bit_cast(bf16x8, a), __builtin_bit_cast(bf16x8, b), c, 0, 0, 0);
}
typedef __amdgpu_buffer_rsrc_t rsrc_t;
__device__ __forceinline__ rsrc_t mkrsrc(const void* p, unsigned bytes) { return __builtin_amdgcn_make_buffer_rsrc((void*)p, 0, bytes, 0x00020000); }
constexpr int SC1 = 16, NT = 2;
template <int AUX> __device__ __forceinline__ v4u ld128(rsrc_t r, unsigned off) { return __builtin_amdgcn_raw_buffer_load_b128(r, off, 0, AUX); }
template <int AUX> __device__ __forceinline__ v2u ld64(rsrc_t r, unsigned off) { return __builtin_amdgcn_raw_buffer_load_b64(r, off, 0, AUX); }
template <int AUX> __device__ __forceinline__ unsigned ld32(rsrc_t r, unsigned off) { return __builtin_amdgcn_raw_buffer_load_b32(r, off, 0, AUX); }
__device__ __forceinline__ void st128(rsrc_t r, unsigned off, v4u v) { __builtin_amdgcn_raw_buffer_store_b128(v, r, off, 0, SC1); }
__device__ __forceinline__ void st64(rsrc_t r, unsigned off, v2u v) { __builtin_amdgcn_raw_buffer_store_b64(v, r, off, 0, SC1); }
__device__ __forceinline__ void st32(rsrc_t r, unsigned off, unsigned v) { __builtin_amdgcn_raw_buffer_store_b32(v, r, off, 0, SC1); }
__device__ __forceinline__ unsigned umax(unsigned a, unsigned b) { return a > b ? a : b; }
__device__ __forceinline__ unsigned mx4(unsigned m, v4u v) { return umax(umax(umax(m, v[0]), v[1]), umax(v[2], v[3])); }
__device__ __forceinline__ unsigned mx2(unsigned m, v2u v) { return umax(umax(m, v[0]), v[1]); }
__device__ __forceinline__ v4u noempty4(f32x4 v) { return v4u{noempty(asu(v[0])), noempty(asu(v[1])), noempty(asu(v[2])), noempty(asu(v[3]))}; }
__device__ __forceinline__ unsigned gr_lo(float v, unsigned tlo) { return ((asu(v) + 8u) & 0xFFFFFFF0u) | tlo; }
__device__ __forceinline__ unsigned gr_bad(v4u g, unsigned tlo, unsigned thi) { return (((g[0] ^ tlo) | (g[2] ^ tlo)) & 0xFu) | (g[1] ^ thi) | (g[3] ^ thi); }
__device__ __forceinline__ float gr_val(unsigned lo) { return asf(lo & 0xFFFFFFF0u); }
__device__ __forceinline__ v4u gr2(float a, float b, unsigned tlo, unsigned thi) { return v4u{gr_lo(a, tlo), thi, gr_lo(b, tlo), thi}; }
__device__ __forceinline__ float gr_sum(v4u g) { const float s = sw32_add(asf(g[0] & 0xFFFFFFF0u), asf(g[2] & 0xFFFFFFF0u)); return sw16_add(s, s); }
__device__ __forceinline__ v4u sel4(bool t, v4u a, v4u b) { return v4u{t ? a[0] : b[0], 0u, t ? a[2] : b[2], 0u}; }
#define MEMFENCE() asm volatile("" ::: "memory")
#define LGKM0() asm volatile("s_waitcnt lgkmcnt(0)" ::: "memory")
#define VM0() asm volatile("s_waitcnt vmcnt(0)" ::: "memory")
#define PIN4(a) asm volatile("" : "+v"((a)[0]), "+v"((a)[1]), "+v"((a)[2]), "+v"((a)[3]))
template <int N> __device__ __forceinline__ void vmwait() { asm volatile("s_waitcnt vmcnt(%0)" ::"n"(N) : "memory"); }
__device__ __forceinline__ void wg_barrier() { LGKM0(); __builtin_amdgcn_s_barrier(); MEMFENCE(); }
template <typename T> __device__ __forceinline__ volatile LAS T* lds_at(unsigned off) { return (volatile LAS T*)(size_t)off; }
template <typename T> __device__ __forceinline__ LAS T* lds_pl(unsigned off) { return (LAS T*)(size_t)off; }
__device__ __forceinline__ void lds_wait_ge(unsigned addr, unsigned need, u64 t0) {
  for (unsigned sp = 0; *lds_at<unsigned>(addr) < need; ++sp) { __builtin_amdgcn_s_sleep(1); if ((sp & 63u) == 63u && rt() - t0 > TLIMIT) break; }
  MEMFENCE();
}

#define EXPIRED(it) (((it) & 63u) == 63u && rt() - t0 > TLIMIT)

__device__ __forceinline__ void dma4(v4i srd, unsigned soff, unsigned ldsb, unsigned v0, unsigned v1, unsigned v2, unsigned v3) {
  asm volatile("s_mov_b32 m0, %0\n\ts_nop 0\n\t"
               "buffer_load_dwordx4 %1, %5, %6 offen nt lds\n\t"
               "buffer_load_dwordx4 %2, %5, %6 offen offset:1024 nt lds\n\t"
               "buffer_load_dwordx4 %3, %5, %6 offen offset:2048 nt lds\n\t"
               "buffer_load_dwordx4 %4, %5, %6 offen offset:3072 nt lds"
               ::"s"(ldsb), "v"(v0), "v"(v1), "v"(v2), "v"(v3), "s"(srd), "s"(soff) : "memory");
}

__device__ __forceinline__ float silu_bf(float v) { return rbf(v / (1.f + __expf(-v))); }
__device__ __forceinline__ void ln_round(float mean, float var, float& mb, float& sd) {
  mb = rbf(mean);
  sd = rbf(__builtin_amdgcn_sqrtf(rbf(rbf(var) + 1e-5f)));
}
__device__ __forceinline__ float mod_round(float n, float sc, float sh) { return rbf(rbf(rbf(n) * rbf(1.f + sc)) + sh); }

__global__ void __launch_bounds__(NTHR, 2) block_decode(Args a) {
  extern __shared__ __attribute__((aligned(16))) unsigned char smem[];
  const int tid = threadIdx.x, lane = tid & 63, wave = __builtin_amdgcn_readfirstlane(tid >> 6);
  const int b = blockIdx.x, x = b & 7, j = b >> 3;
  const u64 t0 = rt();
  const unsigned lb = (unsigned)(size_t)(LAS unsigned char*)smem;
  const rsrc_t ws = mkrsrc(a.ws, WS_END);
  const int kq = j >> 3, cb = x + 8 * (j & 7);
  const int gq = j >> 2, rk = 4 * (gq >> 1) + (j & 3);
  const bool mlp = ((x ^ gq) & 1) == 0;
  const int og = rk >> 1, oc0 = 64 * x + 32 * (rk & 1);
  const bool has_gu = mlp && 4 * gq + (x >> 1) < 28;
  const int up = 4 * (4 * gq + (x >> 1)) + (j & 3);
  const int fc0 = 64 * x + 4 * rk;

  if (tid < 20) *lds_pl<unsigned>(lb + L_CTL + 4 * tid) = (tid == 1 || tid == 3) ? (unsigned)INF : 0u;
  asm volatile("" :: "s"(a.K), "s"(a.ws));
  wg_barrier();
  int w_a = wave, w_b = wave;
  asm volatile("" : "+s"(w_a));
  if (w_a == 4) {
    const u64 kb = (u64)(a.K + (size_t)j * SK * DH), vb = (u64)(a.V + (size_t)j * SK * DH);
    const v4i ksrd = {(int)kb, (int)(kb >> 32), (int)HEAD_BYTES, 0x00020000}, vsrd = {(int)vb, (int)(vb >> 32), (int)HEAD_BYTES, 0x00020000};
    const unsigned g = lane >> 4, c = lane & 15;
    const unsigned k0 = g * 256 + ((c ^ g) << 4), k1 = g * 256 + ((c ^ (4 + g)) << 4), k2 = g * 256 + ((c ^ (8 + g)) << 4), k3 = g * 256 + ((c ^ (12 + g)) << 4);
    const unsigned vl = lane * 16;
    unsigned sl = lb + L_RING;
    auto fill = [&](v4i srd, int n, unsigned v0, unsigned v1, unsigned v2, unsigned v3) {
      const unsigned o = ((unsigned)n >> 2) * 65536u + ((unsigned)n & 3u) * 8192u;
      dma4(srd, o, sl, v0, v1, v2, v3);
      dma4(srd, o + 4096, sl + 4096, v0, v1, v2, v3);
      dma4(srd, o + 32768, sl + 8192, v0, v1, v2, v3);
      dma4(srd, o + 36864, sl + 12288, v0, v1, v2, v3);
      sl = sl + SLOT == lb + L_RING + NSLOT * SLOT ? lb + L_RING : sl + SLOT;
    };
    volatile LAS u64* FT = lds_at<u64>(lb + L_FT);
    int pub = 0;
    unsigned total = (unsigned)INF;
    auto publish = [&](int n) { pub = n > pub ? n : pub; *FT = (u64)(unsigned)pub | ((u64)total << 32); };
    auto wait_free = [&](int i) {
      const int p = i - NSLOT;
      volatile LAS u64* dn = lds_at<u64>(lb + L_DONE + 8 * (p & 1));
      const unsigned need = (unsigned)(p >> 1) + 1u;
      auto freed = [&]() { const u64 d = *dn; return (unsigned)d >= need && (unsigned)(d >> 32) >= need; };
      if (!freed()) {
        VM0(); publish(i);
        for (unsigned sp = 0; !freed(); ++sp) { __builtin_amdgcn_s_sleep(1); if (EXPIRED(sp)) break; }
      }
      MEMFENCE();
    };
    for (unsigned sp = 0; *lds_at<unsigned>(lb + L_ISSUED) < 5u && sp < 4096u; ++sp) {}
    unsigned tn = 1;
    auto take = [&]() {
      u64 tv;
      for (unsigned sp = 0; (unsigned)((tv = *lds_at<u64>(lb + L_TK)) >> 32) < tn; ++sp) if (EXPIRED(sp)) { tv = EMPTY; break; }
      ++tn;
      return (unsigned)__builtin_amdgcn_readfirstlane((unsigned)tv);
    };
    int i = 0, nt = 0, next = POOL0 + x * NP, left = NP;
    bool asked = true;
    for (;; ++i) {
      fill(ksrd, next, k0, k1, k2, k3);
      ++next; --left;
      if (i >= BURST) publish(i - 1);
      if (i == NP + SO - 5) *lds_at<unsigned>(lb + L_TKC) = tn;
      if (left == 0) {
        if (i == NP - 1) { next = x * SO; left = SO; }
        else {
          const unsigned t = asked ? take() : EMPTY;
          if (t >= (unsigned)NKT) break;
          left = 4; next = KD0 + 4 * (int)t;
          *lds_at<unsigned>(lb + L_LIST + 4 * nt) = t; ++nt;
          asked = i + 1 - NP + left + 4 <= CAP;
          if (asked) *lds_at<unsigned>(lb + L_TKC) = tn;
        }
      }
      if (i + 1 >= NSLOT) wait_free(i + 1);
      if (i + 1 >= BURST) vmwait<16>();
    }
    const int nk = i + 1, vown = 2 * nk - NP;
    *lds_at<unsigned>(lb + L_NK) = (unsigned)nk;
    wait_free(i + 1);
    vmwait<16>();
    next = x * SO; left = SO;
    int rt_ = 0;
    for (++i;; ++i) {
      fill(vsrd, next, vl, vl, vl, vl);
      ++next; --left;
      publish(i - 1);
      if (i == vown - 3) *lds_at<unsigned>(lb + L_TKC) = tn;
      if (left == 0) {
        if (rt_ < nt) { const unsigned t = __builtin_amdgcn_readfirstlane(*lds_at<unsigned>(lb + L_LIST + 4 * rt_)); ++rt_; left = 4; next = KD0 + 4 * (int)t; }
        else {
          const unsigned t = take();
          if (t >= (unsigned)NPT) break;
          *lds_at<unsigned>(lb + L_TKC) = tn;
          left = 2; next = POOL0 + 2 * (int)t;
        }
      }
      wait_free(i + 1);
      vmwait<16>();
    }
    total = (unsigned)i + 1u;
    publish(i - 1);
    vmwait<16>(); publish(i);
    VM0(); publish(i + 1);
    return;
  }

  asm volatile("" : "+s"(w_b));
  if (w_b == 5) {
    __builtin_amdgcn_s_setprio(3);
    const rsrc_t rwm = mkrsrc(a.wm, DM * 3072 * 2), rc = mkrsrc(a.c, DM * 2);
    const int n0 = 512 * (gq & 1) + 64 * x + 16 * (j & 3);
    const int ar = 128 * kq + 4 * (lane >> 1);
    v4u w1[4];
#pragma unroll
    for (int i = 0; i < 4; ++i) w1[i] = ld128<NT>(rwm, (unsigned)((ar + i) * 3072 + n0 + 8 * (lane & 1)) * 2);
    const v2u c4 = ld64<0>(rc, ar * 2);
    const v2u ep = ld64<SC1>(ws, WS_EPOCH);
    const int mr = 64 * gq + 8 * (lane >> 3), mc = 64 * (x + 8 * (j & 3));
    const v4u cv = ld128<0>(rc, mr * 2);
    MEMFENCE();
    if (lane == 0) __hip_atomic_fetch_add(lds_pl<unsigned>(lb + L_ISSUED), 1u, __ATOMIC_RELAXED, __HIP_MEMORY_SCOPE_WORKGROUP);
    st128(ws, (lane < 8 && !mlp) ? WS_YP + (og * 512 + oc0) * 4 + lane * 16 : OOB, v4u{EMPTY, EMPTY, EMPTY, EMPTY});
    st128(ws, has_gu ? WS_DP + up * 2048 + lane * 16 : OOB, v4u{EMPTY, EMPTY, EMPTY, EMPTY});
    st128(ws, has_gu ? WS_DP + up * 2048 + 1024 + lane * 16 : OOB, v4u{EMPTY, EMPTY, EMPTY, EMPTY});
    st128(ws, lane < 33 ? WS_PO + b * REC + lane * 16 : OOB, v4u{EMPTY, EMPTY, EMPTY, EMPTY});
    st32(ws, (lane < 2 && x == 0) ? WS_CTR + j * 256 + lane * 128 : OOB, 0u);
    const u64 e1 = (((u64)ep[1] << 32) | ep[0]) + 1ull;
    const unsigned tlo = (unsigned)e1 & 0xFu, thi = (unsigned)(e1 >> 4);
    {
      float s8[8] = {0.f, 0.f, 0.f, 0.f, 0.f, 0.f, 0.f, 0.f};
      fma8(s8, silu_bf(lo16(c4[0])), w1[0]); fma8(s8, silu_bf(hi16(c4[0])), w1[1]);
      fma8(s8, silu_bf(lo16(c4[1])), w1[2]); fma8(s8, silu_bf(hi16(c4[1])), w1[3]);
      float u0, u1;
      rows_reduce8(s8, u0, u1);
      u0 += dpp<ROR8>(u0); u1 += dpp<ROR8>(u1); u0 += dpp<ROR4>(u0); u1 += dpp<ROR4>(u1); u0 += dpp<XOR2>(u0); u1 += dpp<XOR2>(u1);
      const int col = n0 + 8 * (lane & 1) + 2 * (lane >> 4);
      st128(ws, (lane & 15) < 2 ? WS_P1 + (kq * 1024 + col) * 8 : OOB, gr2(u0, u1, tlo, thi));
      *lds_at<unsigned>(lb + L_AUXPUB) = 1u;
    }
    __builtin_amdgcn_s_setprio(0);
    auto stream_at = [&](unsigned fillno) {
      for (unsigned sp = 0; *lds_at<unsigned>(lb + L_FT) < fillno; ++sp) { __builtin_amdgcn_s_sleep(8); if ((sp & 63u) == 63u && rt() - t0 > TLIMIT) break; }
      MEMFENCE();
    };
    stream_at(NSLOT + 4);
    {
      v4u w2[8];
#pragma unroll
      for (int i = 0; i < 8; ++i) w2[i] = ld128<NT>(rwm, (unsigned)((mr + i) * 3072 + 1024 + mc + 8 * (lane & 7)) * 2);
      float s8[8] = {0.f, 0.f, 0.f, 0.f, 0.f, 0.f, 0.f, 0.f};
#pragma unroll
      for (int e = 0; e < 4; ++e) { fma8(s8, silu_bf(lo16(cv[e])), w2[2 * e]); fma8(s8, silu_bf(hi16(cv[e])), w2[2 * e + 1]); }
      float u0, u1;
      rows_reduce8(s8, u0, u1);
      u0 += dpp<ROR8>(u0); u1 += dpp<ROR8>(u1);
      st128(ws, (lane & 8) == 0 ? WS_M2P + (gq * 2048 + mc + 8 * (lane & 7) + 2 * (lane >> 4)) * 8 : OOB, gr2(u0, u1, tlo, thi));
    }
    stream_at(NSLOT + 16);
    {
      v4u m;
      for (;;) {
        MEMFENCE();
        m = ld128<SC1>(ws, WS_M2P + (((lane >> 2) & 7) * 2048 + 8 * b + 2 * (lane & 3)) * 8);
        if (__all(gr_bad(m, tlo, thi) == 0u) || rt() - t0 > TLIMIT) break;
        __builtin_amdgcn_s_sleep(32);
      }
      float u0 = gr_val(m[0]), u1 = gr_val(m[2]);
      u0 = sw16_add(u0, u0); u1 = sw16_add(u1, u1);
      u0 += dpp<ROR8>(u0); u1 += dpp<ROR8>(u1); u0 += dpp<ROR4>(u0); u1 += dpp<ROR4>(u1);
      VM0();
      st128(ws, lane < 4 ? WS_M2 + b * 64 + lane * 16 : OOB, gr2(rbf(u0), rbf(u1), tlo, thi));
    }
    stream_at(NSLOT + 28);
#pragma unroll 1
    for (int u = 0; u < 16; ++u) {
      v4u m;
      for (;;) {
        MEMFENCE();
        m = ld128<SC1>(ws, WS_M2 + u * 1024 + lane * 16);
        if (__all(gr_bad(m, tlo, thi) == 0u) || rt() - t0 > TLIMIT) break;
        __builtin_amdgcn_s_sleep(32);
      }
      *lds_pl<unsigned>(lb + L_M2 + u * 256 + lane * 4) = __builtin_amdgcn_perm(m[2], m[0], 0x07060302u);
      __builtin_amdgcn_s_sleep(16);
    }
    LGKM0();
    *lds_at<unsigned>(lb + L_M2RDY) = 1u;
    GAS unsigned* ctr = (GAS unsigned*)(a.ws + WS_CTR + j * 256);
    unsigned n = 1;
    auto draw = [&](int which) {
      unsigned v = 0;
      if (lane == 0) v = __hip_atomic_fetch_add(ctr + 32 * which, 1u, __ATOMIC_RELAXED, __HIP_MEMORY_SCOPE_AGENT);
      v = __builtin_amdgcn_readfirstlane(v);
      *lds_at<u64>(lb + L_TK) = ((u64)n << 32) | v;
      ++n;
      return v;
    };
    for (;;) {
      bool over = false;
      for (unsigned sp = 0; *lds_at<unsigned>(lb + L_TKC) < n; ++sp) {
        if (*lds_at<unsigned>(lb + L_NK) != (unsigned)INF || EXPIRED(sp)) { over = true; break; }
        __builtin_amdgcn_s_sleep(2);
      }
      if (over || *lds_at<unsigned>(lb + L_NK) != (unsigned)INF || draw(0) >= (unsigned)NKT) break;
    }
    lds_wait_ge(lb + L_KCNT, 5u, t0);
    __builtin_amdgcn_s_sleep(127);
    {
      v2u mx;
      for (;;) {
        MEMFENCE();
        mx = ld64<SC1>(ws, WS_MX + j * 64 + (lane & 7) * 8);
        if (__all((((mx[0] ^ tlo) & 0xFu) | (mx[1] ^ thi)) == 0u) || rt() - t0 > TLIMIT) break;
        __builtin_amdgcn_s_sleep(32);
      }
      *lds_at<float>(lb + L_MH) = row_max(gr_val(mx[0]));
      LGKM0();
    }
    {
      unsigned dq = 0, pend = 0;
      v4u sc = {0u, 0u, 0u, 0u};
      auto flush = [&]() {
        if ((unsigned)lane < 8u * pend) *lds_pl<v4u>(lb + L_SC + dq * 128 + lane * 16) = sc;
        dq += pend; pend = 0;
        LGKM0();
        *lds_at<unsigned>(lb + L_SCR) = dq;
      };
      for (;;) {
        if (__builtin_amdgcn_readfirstlane(*lds_at<unsigned>(lb + L_TKC)) < n) flush();
        for (unsigned sp = 0; *lds_at<unsigned>(lb + L_TKC) < n; ++sp) { __builtin_amdgcn_s_sleep(2); if (EXPIRED(sp)) break; }
        const unsigned v = draw(1);
        flush();
        if (v >= (unsigned)NPT || rt() - t0 > TLIMIT) break;
        pend = 2u;
        sc = ld128<SC1 | NT>(ws, lane < 16 ? WS_SG + (j * 8 * NP + 2 * v) * 128 + lane * 16 : OOB);
        MEMFENCE();
      }
    }
    return;
  }

  const int g = lane >> 4, c = lane & 15;
  const rsrc_t rx = mkrsrc(a.x, DM * 2), rwq = mkrsrc(a.wq, DM * 4096 * 2);
  const rsrc_t rwd = mkrsrc(a.wd, DF * DM * 2);
  const int r0 = 128 * kq + 32 * wave;
  v4u wqv[4];
#pragma unroll
  for (int i = 0; i < 4; ++i) wqv[i] = ld128<NT>(rwq, (unsigned)((r0 + 4 * (lane >> 3) + i) * 4096 + 64 * cb + 8 * (lane & 7)) * 2);
  const v2u ep = ld64<SC1>(ws, WS_EPOCH);
  const v4u x8 = ld128<0>(rx, lane * 16);
  const unsigned x2 = ld32<0>(rx, tid * 4);
  const unsigned xh = ld32<0>(rx, (r0 + 2 * c) * 2);
  MEMFENCE();
  if (lane == 0) __hip_atomic_fetch_add(lds_pl<unsigned>(lb + L_ISSUED), 1u, __ATOMIC_RELAXED, __HIP_MEMORY_SCOPE_WORKGROUP);
  float mb, sd;
  {
    float xs[8];
#pragma unroll
    for (int e = 0; e < 4; ++e) { xs[2 * e] = lo16(x8[e]); xs[2 * e + 1] = hi16(x8[e]); }
    float s = 0.f;
#pragma unroll
    for (int e = 0; e < 8; ++e) s += xs[e];
    const float mean = wave_sum(s) * (1.f / 512.f);
    float v = 0.f;
#pragma unroll
    for (int e = 0; e < 8; ++e) { const float d = xs[e] - mean; v = fmaf(d, d, v); }
    ln_round(mean, wave_sum(v) * (1.f / 512.f), mb, sd);
  }
  float nrow = rbf((lane >= 32 ? hi16(xh) : lo16(xh)) - mb) / sd;
  kpair(wqv[0], wqv[1]); kpair(wqv[2], wqv[3]);
  PIN4(wqv); asm volatile("" : "+v"(nrow));
  const u64 epoch1 = (((u64)ep[1] << 32) | ep[0]) + 1ull;
  const unsigned tlo = (unsigned)epoch1 & 0xFu, thi = (unsigned)(epoch1 >> 4);
  lds_wait_ge(lb + L_AUXPUB, 1u, t0);
  {
    const unsigned o1 = WS_P1 + (g * 1024 + r0 + 2 * c) * 8;
    v4u gs = {0u, 0u, 0u, 0u}, gc = gs;
    bool bad = true;
    __builtin_amdgcn_s_sleep(32);
    for (;;) {
      MEMFENCE();
      const v4u ns_ = ld128<SC1>(ws, bad ? o1 : OOB), nc_ = ld128<SC1>(ws, bad ? o1 + 4096 : OOB);
      const bool tk = bad & ((gr_bad(ns_, tlo, thi) | gr_bad(nc_, tlo, thi)) == 0u);
      gs = sel4(tk, ns_, gs); gc = sel4(tk, nc_, gc); bad = bad & !tk;
      if (!__any(bad) || rt() - t0 > TLIMIT) break;
    }
    const unsigned hp = pack_halves(mod_round(nrow, rbf(gr_sum(gc)), rbf(gr_sum(gs))));
    float acc[8] = {0.f, 0.f, 0.f, 0.f, 0.f, 0.f, 0.f, 0.f};
    dot8(acc, __builtin_amdgcn_ds_bpermute((lane >> 3) * 8, hp), wqv[0], wqv[1]);
    dot8(acc, __builtin_amdgcn_ds_bpermute((lane >> 3) * 8 + 4, hp), wqv[2], wqv[3]);
    float u0, u1;
    rows_reduce8(acc, u0, u1);
    u0 += dpp<ROR8>(u0); u1 += dpp<ROR8>(u1);
    if ((lane & 8) == 0) *lds_pl<f32x2>(lb + L_RED + wave * 256 + (8 * (lane & 7) + 2 * g) * 4) = f32x2{u0, u1};
    LGKM0();
    unsigned old = 0;
    if (lane == 0) old = __hip_atomic_fetch_add(lds_pl<unsigned>(lb + L_QCNT), 1u, __ATOMIC_RELAXED, __HIP_MEMORY_SCOPE_WORKGROUP);
    old = __builtin_amdgcn_readfirstlane(old);
    MEMFENCE();
    if (old == 3u) {
      const unsigned ra = lb + L_RED + lane * 4;
      const float qp = (*lds_pl<float>(ra) + *lds_pl<float>(ra + 256)) + (*lds_pl<float>(ra + 512) + *lds_pl<float>(ra + 768));
      st64(ws, WS_QS + (kq * 4096 + 64 * cb + lane) * 8, v2u{gr_lo(qp, tlo), thi});
    }
  }
  const int r8 = 8 * (tid >> 2);
  v4u wv[8];
  unsigned wdv[16];
#pragma unroll
  for (int i = 0; i < 8; ++i) { wv[i] = v4u{0u, 0u, 0u, 0u}; wdv[2 * i] = 0u; wdv[2 * i + 1] = 0u; }
  const GAS v4u* wp = (const GAS v4u*)(mlp ? ((tid & 2) ? a.wu : a.wg) + (r8 * DF + 16 * (has_gu ? up : 0) + 8 * (tid & 1)) : a.wo + ((512 * og + r8) * DM + oc0 + 8 * (tid & 3)));
  const int wstride = mlp ? DF / 8 : DM / 8;
  const unsigned wdo = has_gu ? (unsigned)(16 * up * DM + 2 * tid) * 2 : OOB;

  const unsigned key = 4 * (c & 3) + (c >> 2);
  unsigned ka[4], msk[4];
#pragma unroll
  for (int st = 0; st < 4; ++st) {
    ka[st] = lb + L_RING + (wave & 1) * 8192 + key * 256 + (((st + 4 * g) ^ key) << 4);
    msk[st] = ((c & 7) >> 1) == st ? ((c & 1) ? 0xffff0000u : 0x0000ffffu) : 0u;
  }
  const unsigned va = lb + L_RING + (wave & 1) * 8192 + lane * 16;
  const bool is_hi = c < 8;
  volatile LAS unsigned* DN = lds_at<unsigned>(lb + L_DONE + 4 * wave);
  unsigned so = (wave >> 1) * SLOT;
  v4u sv[8], sw[8], ud[8], qf[4];
#pragma unroll
  for (int i = 0; i < 8; ++i) { sv[i] = v4u{0u, 0u, 0u, 0u}; sw[i] = sv[i]; ud[i] = sv[i]; }
  int ns = 0;
  auto release = [&](v4u (&d)[8], int i) {
    LGKM0();
    *DN = (unsigned)(i >> 1) + 1u;
    asm volatile("" : "+v"(d[0]), "+v"(d[1]), "+v"(d[2]), "+v"(d[3]), "+v"(d[4]), "+v"(d[5]), "+v"(d[6]), "+v"(d[7]) :: "memory");
    so = (so + 2 * SLOT) & (NSLOT * SLOT - 1);
  };
  auto read_k = [&](v4u (&d)[8], int i) {
#pragma unroll
    for (int st = 0; st < 4; ++st) { d[st] = *lds_pl<v4u>(ka[st] + so); d[4 + st] = *lds_pl<v4u>(ka[st] + so + 4096); }
    release(d, i);
  };
  int total = INF, nk = INF;
  auto wait_full = [&](int i, bool in_k) {
    for (unsigned sp = 0;; ++sp) {
      const v4u ct = *(volatile LAS v4u*)(size_t)(lb + L_FT);
      total = __builtin_amdgcn_readfirstlane((int)ct[1]);
      nk = __builtin_amdgcn_readfirstlane((int)ct[3]);
      if (EXPIRED(sp)) { total = 0; nk = nk < i ? nk : i; break; }
      if (__builtin_amdgcn_readfirstlane((int)ct[0]) > i || (in_k && nk <= i)) break;
      if (total == INF) __builtin_amdgcn_s_sleep(1);
    }
    MEMFENCE();
  };
  float mraw = -INFINITY;
  auto scores = [&](const v4u (&d)[8], int i) {
    f32x4 sa = {0.f, 0.f, 0.f, 0.f}, sb = {0.f, 0.f, 0.f, 0.f};
#pragma unroll
    for (int st = 0; st < 4; ++st) { sa = mfma(d[st], qf[st], sa); sb = mfma(d[4 + st], qf[st], sb); }
    mraw = fmaxf(fmaxf(fmaxf(mraw, sa[0]), fmaxf(sa[1], sa[2])), fmaxf(fmaxf(sa[3], sb[0]), fmaxf(fmaxf(sb[1], sb[2]), sb[3])));
    v4u pk = {cvtpk(sa[0], sa[1]), cvtpk(sa[2], sa[3]), cvtpk(sb[0], sb[1]), cvtpk(sb[2], sb[3])};
#pragma unroll
    for (int u = 0; u < 4; ++u) pk[u] = cvtpk(lo16(pk[u]) * SCALE, hi16(pk[u]) * SCALE);
    const unsigned so_ = (wave & 1) * 64 + g * 16;
    const unsigned dst = i < NP ? L_SH + i * 128 : L_SC + (i - NP) * 128;
    if (c == 0) *lds_pl<v4u>(lb + dst + so_) = pk;
  };
  auto stash = [&]() {
    const int full = __builtin_amdgcn_readfirstlane((int)*lds_at<unsigned>(lb + L_FT)), i0 = wave >> 1;
    MEMFENCE();
    if (ns == 0 && full > i0) { read_k(wv, i0); ns = 1; }
    if (ns == 1 && full > i0 + 2) { read_k(sv, i0 + 2); ns = 2; }
    if (ns == 2 && full > i0 + 4) { read_k(sw, i0 + 4); ns = 3; }
    if (ns == 3 && full > i0 + 6) { read_k(ud, i0 + 6); ns = 4; }
  };
  {
    const unsigned o2 = WS_QS + (g * 4096 + 128 * j + 32 * wave + 2 * c) * 8;
    v4u gq4 = {0u, 0u, 0u, 0u};
    bool bad = true;
    stash();
    {
      const v4u nq = ld128<SC1>(ws, o2);
      bad = gr_bad(nq, tlo, thi) != 0u;
      gq4 = nq;
    }
    asm volatile("" : "+v"(gq4) :: "memory");
    stash();
    for (unsigned it = 0; __any(bad) && !EXPIRED(it); ++it) {
      MEMFENCE();
      const v4u nq = ld128<SC1>(ws, bad ? o2 : OOB);
      const bool tk = bad & (gr_bad(nq, tlo, thi) == 0u);
      gq4 = sel4(tk, nq, gq4); bad = bad & !tk;
    }
    *lds_pl<unsigned>(lb + L_QSC + (16 * wave + c) * 4) = pack_halves(rbf(gr_sum(gq4)));
    LGKM0();
    if (lane == 0) __hip_atomic_fetch_add(lds_pl<unsigned>(lb + L_QIN), 1u, __ATOMIC_RELAXED, __HIP_MEMORY_SCOPE_WORKGROUP);
    lds_wait_ge(lb + L_QIN, 4u, t0);
#pragma unroll
    for (int st = 0; st < 4; ++st) qf[st] = *lds_pl<v4u>(lb + L_QSC + (st + 4 * g) * 16);
  }
  if (ns >= 1) scores(wv, wave >> 1);
  if (ns >= 2) scores(sv, (wave >> 1) + 2);
  if (ns >= 3) scores(sw, (wave >> 1) + 4);
  if (ns >= 4) scores(ud, (wave >> 1) + 6);
  int i = (wave >> 1) + 2 * ns;
  for (;; i += 2) {
    wait_full(i, true);
    if (nk <= i) break;
    read_k(ud, i);
    scores(ud, i);
  }
  float m2;
  {
    *lds_pl<float>(lb + L_MXW + wave * 4) = rbf(rbf(xrow_max(mraw)) * SCALE);
    LGKM0();
    unsigned old = 0;
    if (lane == 0) old = __hip_atomic_fetch_add(lds_pl<unsigned>(lb + L_KCNT), 1u, __ATOMIC_RELAXED, __HIP_MEMORY_SCOPE_WORKGROUP);
    old = __builtin_amdgcn_readfirstlane(old);
    lds_wait_ge(lb + L_KCNT, 4u, t0);
    const f32x4 mw = *lds_pl<f32x4>(lb + L_MXW);
    const float mg = fmaxf(fmaxf(mw[0], mw[1]), fmaxf(mw[2], mw[3]));
    if (old == 3u) {
#pragma unroll
      for (int u = 0; u < (NP + 7) / 8; ++u)
        __builtin_amdgcn_raw_buffer_store_b128(*lds_pl<v4u>(lb + L_SH + u * 1024 + lane * 16), ws,
                                               u * 1024 + lane * 16 < NP * 128 ? WS_SG + b * NP * 128 + u * 1024 + lane * 16 : OOB, 0, SC1 | NT);
      VM0();
      st64(ws, lane == 0 ? WS_MX + b * 8 : OOB, v2u{gr_lo(mg, tlo), thi});
      *lds_at<unsigned>(lb + L_KCNT) = 5u;
    }
    m2 = mg * LOG2E;
  }
  float lsum = 0.f;
  f32x4 acc = {0.f, 0.f, 0.f, 0.f};
  {
    int wk = (mlp && !has_gu) ? 11 : 0;
    bool done = false, own = true;
    for (;; i += 2) {
      int e = i - nk;
      if (!done && e >= nk - NP) {
        e -= nk - NP;
        for (unsigned sp = 0;; ++sp) {
          const v4u ct = *(volatile LAS v4u*)(size_t)(lb + L_FT);
          if (__builtin_amdgcn_readfirstlane((int)ct[2]) > e) break;
          if (__builtin_amdgcn_readfirstlane((int)ct[1]) <= i || EXPIRED(sp)) { done = true; break; }
          __builtin_amdgcn_s_sleep(1);
        }
        MEMFENCE();
        if (own) {
          const float mh = *lds_at<float>(lb + L_MH) * LOG2E, al = __builtin_amdgcn_exp2f(m2 - mh);
          acc *= al; lsum *= al; m2 = mh; own = false;
        }
      }
      if (!done) {
        const v4u sc = *lds_pl<v4u>(lb + L_SC + e * 128 + (wave & 1) * 64 + g * 16);
        v4u pa[8];
#pragma unroll
        for (int u = 0; u < 8; ++u) {
          const float p = __builtin_amdgcn_exp2f(fmaf((u & 1) ? hi16(sc[u >> 1]) : lo16(sc[u >> 1]), LOG2E, -m2));
          lsum += p;
          const float ph = hi16(asu(p));
          const float xv = is_hi ? ph : p - ph;
          const unsigned pk = cvtpk(xv, xv);
          pa[u] = v4u{pk & msk[0], pk & msk[1], pk & msk[2], pk & msk[3]};
        }
        asm volatile("" : "+v"(pa[0]), "+v"(pa[1]), "+v"(pa[2]), "+v"(pa[3]), "+v"(pa[4]), "+v"(pa[5]), "+v"(pa[6]), "+v"(pa[7]));
        wait_full(i, false);
#pragma unroll
        for (int u = 0; u < 8; ++u) ud[u] = *lds_pl<v4u>(va + so + u * 1024);
        release(ud, i);
#pragma unroll
        for (int u = 0; u < 8; ++u) acc = mfma(pa[u], ud[u], acc);
        done = i + 2 >= total && !own;
      }
      MEMFENCE();
      if (wk <= 10 && (done || rt() - t0 >= (u64)(TW + TD * wk + (wk == 10 ? 400 : 0)))) {
#pragma unroll
        for (int u = 0; u < 8; ++u) if (wk == u) wv[u] = __builtin_nontemporal_load(wp + u * wstride);
#pragma unroll
        for (int v = 0; v < 2; ++v) if (wk == 8 + v) {
#pragma unroll
          for (int u = 8 * v; u < 8 * v + 8; ++u) wdv[u] = ld32<NT>(rwd, wdo + u * (DM * 2));
        }
        if (wk == 10) {
#pragma unroll
          for (int u = 0; u < 8; u += 2) kpair(wv[u], wv[u + 1]);
#pragma unroll
          for (int u = 0; u < 16; u += 2) {
            const unsigned lo = __builtin_amdgcn_perm(wdv[u + 1], wdv[u], 0x05040100u), hi = __builtin_amdgcn_perm(wdv[u + 1], wdv[u], 0x07060302u);
            wdv[u] = lo; wdv[u + 1] = hi;
          }
        }
        ++wk;
      }
      MEMFENCE();
      if (done && wk > 10) break;
    }
  }
  {
    f32x4 o;
#pragma unroll
    for (int r = 0; r < 4; ++r) o[r] = sw32_add(acc[r], acc[r]);
    float l = sw16_add(lsum, lsum); l = sw32_add(l, l);
    *lds_pl<f32x4>(lb + L_MRG + wave * 512 + (8 * c + 4 * (g & 1)) * 4) = o;
    *lds_pl<float>(lb + L_LW + wave * 4) = l;
    wg_barrier();
    if (wave == 0) {
      const unsigned ra = lb + L_MRG + (lane & 31) * 16;
      const f32x4 lw = *lds_pl<f32x4>(lb + L_LW);
      f32x4 O = (*lds_pl<f32x4>(ra) + *lds_pl<f32x4>(ra + 512)) + (*lds_pl<f32x4>(ra + 1024) + *lds_pl<f32x4>(ra + 1536));
      if (lane >= 32) O = f32x4{(lw[0] + lw[1]) + (lw[2] + lw[3]), 0.f, 0.f, 0.f};
      st128(ws, lane < 33 ? WS_PO + b * REC + lane * 16 : OOB, noempty4(O));
    }
  }
  if (!mlp) {
    const unsigned hb = WS_PO + (4 * og + wave) * 8 * REC;
    v2u po[8];
    unsigned ls = EMPTY;
#pragma unroll
    for (int s = 0; s < 8; ++s) po[s] = v2u{0u, 0u};
    unsigned miss = 0x1FFu;
    for (unsigned it = 0;; ++it) {
      MEMFENCE();
      if (miss & 0x100u) ls = ld32<SC1>(ws, (lane & 15) < 8 ? hb + (lane & 15) * REC + 512 : OOB);
#pragma unroll
      for (int s = 0; s < 8; ++s) if (miss & (1u << s)) po[s] = ld64<SC1>(ws, hb + s * REC + lane * 8);
      if ((miss & 0x100u) && __all(ls != EMPTY)) miss &= ~0x100u;
#pragma unroll
      for (int s = 0; s < 8; ++s) if ((miss & (1u << s)) && __all(mx2(0u, po[s]) != EMPTY)) miss &= ~(1u << s);
      if (miss == 0u || EXPIRED(it)) break;
    }
    const float inv = __builtin_amdgcn_rcpf(row_sum(asf(ls)));
    const unsigned op = cvtpk((((asf(po[0][0]) + asf(po[1][0])) + (asf(po[2][0]) + asf(po[3][0]))) + ((asf(po[4][0]) + asf(po[5][0])) + (asf(po[6][0]) + asf(po[7][0])))) * inv,
                              (((asf(po[0][1]) + asf(po[1][1])) + (asf(po[2][1]) + asf(po[3][1]))) + ((asf(po[4][1]) + asf(po[5][1])) + (asf(po[6][1]) + asf(po[7][1])))) * inv);
    float y[8] = {0.f, 0.f, 0.f, 0.f, 0.f, 0.f, 0.f, 0.f};
    dot8(y, dppu<0x00>(op), wv[0], wv[1]); dot8(y, dppu<0x55>(op), wv[2], wv[3]);
    dot8(y, dppu<0xAA>(op), wv[4], wv[5]); dot8(y, dppu<0xFF>(op), wv[6], wv[7]);
    float u0, u1;
    rows_reduce8(y, u0, u1);
    u0 += dpp<ROR8>(u0); u1 += dpp<ROR8>(u1); u0 += dpp<ROR4>(u0); u1 += dpp<ROR4>(u1);
    *lds_pl<f32x2>(lb + L_RED3 + wave * 128 + (8 * (c & 3) + 2 * g) * 4) = f32x2{u0, u1};
    wg_barrier();
    if (wave == 0) {
      const unsigned ra = lb + L_RED3 + (lane & 7) * 16;
      const f32x4 t = (*lds_pl<f32x4>(ra) + *lds_pl<f32x4>(ra + 128)) + (*lds_pl<f32x4>(ra + 256) + *lds_pl<f32x4>(ra + 384));
      st128(ws, lane < 8 ? WS_YP + (og * 512 + oc0) * 4 + lane * 16 : OOB, noempty4(t));
    }
    return;
  }
  lds_wait_ge(lb + L_M2RDY, 1u, t0);
  float gbf = lo16(*lds_pl<u16>(lb + L_M2 + 3072 + (fc0 + g) * 2));
  {
    const unsigned gaw = *lds_pl<unsigned>(lb + L_M2 + tid * 4), sbw = *lds_pl<unsigned>(lb + L_M2 + 1024 + tid * 4), cbw = *lds_pl<unsigned>(lb + L_M2 + 2048 + tid * 4);
    float ga0 = lo16(gaw), ga1 = hi16(gaw), sb0 = lo16(sbw), sb1 = hi16(sbw), t0c = rbf(1.f + lo16(cbw)), t1c = rbf(1.f + hi16(cbw)), xa = lo16(x2), xb = hi16(x2);
    asm volatile("" : "+v"(ga0), "+v"(ga1), "+v"(sb0), "+v"(sb1), "+v"(t0c), "+v"(t1c), "+v"(xa), "+v"(xb), "+v"(gbf));
    v2u yp[8];
#pragma unroll
    for (int s = 0; s < 8; ++s) yp[s] = v2u{0u, 0u};
    unsigned miss = 0xFFu;
    for (unsigned it = 0;; ++it) {
      MEMFENCE();
#pragma unroll
      for (int s = 0; s < 8; ++s) if (miss & (1u << s)) yp[s] = ld64<SC1>(ws, WS_YP + s * 2048 + tid * 8);
#pragma unroll
      for (int s = 0; s < 8; ++s) if ((miss & (1u << s)) && __all(mx2(0u, yp[s]) != EMPTY)) miss &= ~(1u << s);
      if (miss == 0u || EXPIRED(it)) break;
    }
    const unsigned atp = cvtpk(((asf(yp[0][0]) + asf(yp[1][0])) + (asf(yp[2][0]) + asf(yp[3][0]))) + ((asf(yp[4][0]) + asf(yp[5][0])) + (asf(yp[6][0]) + asf(yp[7][0]))),
                               ((asf(yp[0][1]) + asf(yp[1][1])) + (asf(yp[2][1]) + asf(yp[3][1]))) + ((asf(yp[4][1]) + asf(yp[5][1])) + (asf(yp[6][1]) + asf(yp[7][1]))));
    const unsigned gap = cvtpk(ga0 * lo16(atp), ga1 * hi16(atp));
    const unsigned x1p = cvtpk(xa + lo16(gap), xb + hi16(gap));
    const float x1a = lo16(x1p), x1b = hi16(x1p);
    const float sh = asf(__builtin_amdgcn_readfirstlane(asu(x1a)));
    const float da = x1a - sh, db = x1b - sh;
    const float s1 = wave_sum(da + db), s2 = wave_sum(fmaf(da, da, db * db));
    *lds_pl<f32x2>(lb + L_ST + wave * 8) = f32x2{fmaf(s1, 1.f / 128.f, sh), fmaf(-s1 * (1.f / 128.f), s1, s2)};
    *lds_pl<f32x2>(lb + L_X1 + tid * 8) = f32x2{x1a, x1b};
    wg_barrier();
    if (has_gu) {
      const f32x4 sA = *lds_pl<f32x4>(lb + L_ST), sB = *lds_pl<f32x4>(lb + L_ST + 16);
      const float mean = ((sA[0] + sA[2]) + (sB[0] + sB[2])) * 0.25f;
      const float d0 = sA[0] - mean, d1 = sA[2] - mean, d2 = sB[0] - mean, d3 = sB[2] - mean;
      const float var = fmaxf(((sA[1] + sA[3]) + (sB[1] + sB[3])) + 128.f * ((d0 * d0 + d1 * d1) + (d2 * d2 + d3 * d3)), 0.f) * (1.f / 512.f);
      float mb2, sd2;
      ln_round(mean, var, mb2, sd2);
      const float rs = __builtin_amdgcn_rcpf(sd2);
      const unsigned e0 = cvtpk(x1a - mb2, x1b - mb2), e1 = cvtpk(lo16(e0) * rs, hi16(e0) * rs), e2 = cvtpk(lo16(e1) * t0c, hi16(e1) * t1c);
      const unsigned hq = cvtpk(lo16(e2) + sb0, hi16(e2) + sb1);
      float y[8] = {0.f, 0.f, 0.f, 0.f, 0.f, 0.f, 0.f, 0.f};
      dot8(y, dppu<0x00>(hq), wv[0], wv[1]); dot8(y, dppu<0x55>(hq), wv[2], wv[3]);
      dot8(y, dppu<0xAA>(hq), wv[4], wv[5]); dot8(y, dppu<0xFF>(hq), wv[6], wv[7]);
      float u0, u1;
      rows_reduce8(y, u0, u1);
      u0 += dpp<ROR8>(u0); u1 += dpp<ROR8>(u1); u0 += dpp<ROR4>(u0); u1 += dpp<ROR4>(u1);
      *lds_pl<f32x2>(lb + L_RED3 + wave * 128 + (8 * (c & 3) + 2 * g) * 4) = f32x2{u0, u1};
      wg_barrier();
      const unsigned ra = lb + L_RED3 + c * 4;
      const float gs = rbf((*lds_pl<float>(ra) + *lds_pl<float>(ra + 128)) + (*lds_pl<float>(ra + 256) + *lds_pl<float>(ra + 384)));
      const float us = rbf((*lds_pl<float>(ra + 64) + *lds_pl<float>(ra + 192)) + (*lds_pl<float>(ra + 320) + *lds_pl<float>(ra + 448)));
      const float z = 0.7978845608028654f * fmaf(0.044715f * gs * gs, gs, gs);
      const float th = fmaf(-2.f, __builtin_amdgcn_rcpf(1.f + __expf(2.f * z)), 1.f);
      const float h2 = rbf(rbf(0.5f * gs * (1.f + th)) * us);
      const unsigned hp = __builtin_amdgcn_perm(asu(dpp<XOR1>(h2)), asu(h2), 0x07060302u);
      float d0a = 0.f, d1a = 0.f;
#pragma unroll
      for (int e = 0; e < 16; e += 2) {
        const unsigned he = __builtin_amdgcn_readlane(hp, e);
        d0a = dot2(wdv[e], he, d0a); d1a = dot2(wdv[e + 1], he, d1a);
      }
      st64(ws, WS_DP + up * 2048 + tid * 8, v2u{noempty(asu(d0a)), noempty(asu(d1a))});
      st64(ws, (b | tid) == 0 ? WS_EPOCH : OOB, v2u{(unsigned)epoch1, (unsigned)(epoch1 >> 32)});
    }
  }
  if (wave != 0) return;
  {
    v4u da = {EMPTY, 0u, 0u, 0u}, db = {lane < NGU - 64 ? EMPTY : 0u, 0u, 0u, 0u};
    const float x1o = *lds_pl<float>(lb + L_X1 + (fc0 + g) * 4);
    for (unsigned it = 0;; ++it) {
      MEMFENCE();
      const bool ma = mx4(0u, da) == EMPTY, mb_ = mx4(0u, db) == EMPTY;
      if (!__any(ma | mb_) || EXPIRED(it)) break;
      const v4u na = ld128<SC1>(ws, ma ? WS_DP + lane * 2048 + fc0 * 4 : OOB), nb = ld128<SC1>(ws, mb_ ? WS_DP + (lane + 64) * 2048 + fc0 * 4 : OOB);
      da = ma ? na : da; db = mb_ ? nb : db;
    }
    float t4[4];
#pragma unroll
    for (int r = 0; r < 4; ++r) t4[r] = asf(da[r]) + asf(db[r]);
    const float r = row_sum(sw16_add(sw32_add(t4[0], t4[2]), sw32_add(t4[1], t4[3])));
    const float res = x1o + rbf(gbf * rbf(r));
    if (c == 0) a.out[fc0 + g] = (u16)(cvtpk(res, res) & 0xffffu);
  }
}
}

extern "C" void kernel_launch(void* const* d_in, const int* in_sizes, int n_in, void* d_out, int out_size, void* d_ws, size_t ws_size,
                              hipStream_t stream) {
  Args a;
  a.x = (const u16*)d_in[0]; a.K = (const u16*)d_in[1]; a.V = (const u16*)d_in[2]; a.wq = (const u16*)d_in[3]; a.wo = (const u16*)d_in[4];
  a.wg = (const u16*)d_in[6]; a.wu = (const u16*)d_in[7]; a.wd = (const u16*)d_in[8]; a.c = (const u16*)d_in[9]; a.wm = (const u16*)d_in[10];
  a.out = (u16*)d_out; a.ws = (unsigned char*)d_ws;
  (void)hipFuncSetAttribute((const void*)block_decode, hipFuncAttributeMaxDynamicSharedMemorySize, (int)LDS_BYTES);
  hipLaunchKernelGGL(block_decode, dim3(NWG), dim3(NTHR), LDS_BYTES, stream, a);
}
```

```cpp
#include <hip/hip_runtime.h>
#include <hip/hip_bf16.h>

#define GAS __attribute__((address_space(1)))
#define LAS __attribute__((address_space(3)))
typedef unsigned short u16;
typedef unsigned long long u64;
typedef unsigned v4u __attribute__((ext_vector_type(4)));
typedef unsigned v2u __attribute__((ext_vector_type(2)));
typedef int v4i __attribute__((ext_vector_type(4)));
typedef float f32x4 __attribute__((ext_vector_type(4)));
typedef __bf16 bf16x8 __attribute__((ext_vector_type(8)));
typedef __bf16 bf16x2 __attribute__((ext_vector_type(2)));
typedef float f32x2 __attribute__((ext_vector_type(2)));

namespace {
constexpr int DM = 512, SK = 65536, DH = 128, DF = 1792;
constexpr int NWG = 256, NTHR = 384;
constexpr int SLOT = 16384, NSLOT = 8, FH = 1024;
constexpr int SO = 112;
constexpr int NP = 8;
constexpr int POOL0 = 8 * SO, KD0 = POOL0 + 8 * NP, NKT = (FH - KD0) / 4, NPT = 8 * NP / 2;
constexpr int CAP = 144;
static_assert((FH - KD0) % 4 == 0 && SO % 4 == 0 && NP % 2 == 0 && 8 * NP + 16 <= SO && NP <= 8 && 8 * (CAP - 7 - SO) >= FH - KD0, "fill plan");
constexpr int BURST = 3;
constexpr unsigned TW = 10800, TD = 110;
constexpr int INF = 0x7FFFFFFF;
constexpr unsigned HEAD_BYTES = (unsigned)SK * DH * 2;
constexpr unsigned EMPTY = 0xFFFFFFFFu;
constexpr unsigned OOB = 0x7FFF0000u;
constexpr u64 TLIMIT = 5000000ull;
constexpr float LOG2E = 1.4426950408889634f, SCALE = 0.08838834764831845f;
constexpr int NGU = DF / 16, REC = 640;
constexpr unsigned WS_EPOCH = 0, WS_P1 = 4096, WS_QS = WS_P1 + 32768, WS_M2P = WS_QS + 131072, WS_M2 = WS_M2P + 131072, WS_CTR = WS_M2 + 16384,
                   WS_MX = WS_CTR + 8192, WS_YP = WS_MX + 2048, WS_PO = WS_YP + 16384, WS_DP = WS_PO + NWG * REC,
                   WS_SG = WS_DP + NGU * 2048, WS_END = WS_SG + NWG * NP * 128;
constexpr unsigned L_RING = 0, L_CTL = NSLOT * SLOT, L_FT = L_CTL, L_SCR = L_CTL + 8, L_NK = L_CTL + 12, L_DONE = L_CTL + 16, L_QCNT = L_CTL + 32,
                   L_AUXPUB = L_CTL + 36, L_QIN = L_CTL + 40, L_ISSUED = L_CTL + 44, L_M2RDY = L_CTL + 48, L_TKC = L_CTL + 52, L_TK = L_CTL + 56,
                   L_KCNT = L_CTL + 64, L_MH = L_CTL + 68, L_MXW = L_CTL + 80, L_LW = L_CTL + 96, L_LIST = L_CTL + 128, L_RED = L_LIST + 128, L_QSC = L_RED + 1024, L_RED3 = L_QSC + 256,
                   L_ST = L_RED3 + 512, L_M2 = L_ST + 64, L_X1 = L_M2 + 4096, L_MRG = L_X1 + 2048, L_SH = L_X1, L_SC = L_MRG + 2048, L_END = L_SC + CAP * 128;
constexpr unsigned LDS_BYTES = L_END;
static_assert(LDS_BYTES <= 160 * 1024 && LDS_BYTES > 80 * 1024, "one workgroup per CU");

struct Args {
  const u16 *x, *K, *V, *wq, *wo, *wg, *wu, *wd, *c, *wm;
  u16* out;
  unsigned char* ws;
};

__device__ __forceinline__ u64 rt() { return __builtin_amdgcn_s_memrealtime(); }
__device__ __forceinline__ float asf(unsigned u) { return __builtin_bit_cast(float, u); }
__device__ __forceinline__ unsigned asu(float f) { return __builtin_bit_cast(unsigned, f); }
__device__ __forceinline__ float lo16(unsigned u) { return asf(u << 16); }
__device__ __forceinline__ float hi16(unsigned u) { return asf(u & 0xffff0000u); }
__device__ __forceinline__ unsigned cvtpk(float a, float b) {
  const f32x2 v = {a, b};
  return __builtin_bit_cast(unsigned, __builtin_convertvector(v, bf16x2));
}
__device__ __forceinline__ float rbf(float f) { return lo16(cvtpk(f, f)); }
__device__ __forceinline__ unsigned noempty(unsigned v) { return v < EMPTY - 1u ? v : EMPTY - 1u; }
__device__ __forceinline__ float dot2(unsigned a, unsigned b, float c) {
  return __builtin_amdgcn_fdot2_f32_bf16(__builtin_bit_cast(bf16x2, a), __builtin_bit_cast(bf16x2, b), c, false);
}
template <int CTRL> __device__ __forceinline__ float dpp(float v) {
  return asf(__builtin_amdgcn_mov_dpp(asu(v), CTRL, 0xf, 0xf, true));
}
constexpr int ROR8 = 0x128, ROR4 = 0x124, ROR2 = 0x122, ROR1 = 0x121, XOR2 = 0x4E, XOR1 = 0xB1;
template <int CTRL> __device__ __forceinline__ unsigned dppu(unsigned v) { return __builtin_amdgcn_mov_dpp(v, CTRL, 0xf, 0xf, true); }
__device__ __forceinline__ float sw32_add(float a, float b) {
  const auto r = __builtin_amdgcn_permlane32_swap(asu(a), asu(b), false, false);
  return asf(r[0]) + asf(r[1]);
}
__device__ __forceinline__ float sw16_add(float a, float b) {
  const auto r = __builtin_amdgcn_permlane16_swap(asu(a), asu(b), false, false);
  return asf(r[0]) + asf(r[1]);
}
__device__ __forceinline__ float xrow_max(float v) {
  const auto r = __builtin_amdgcn_permlane16_swap(asu(v), asu(v), false, false);
  v = fmaxf(asf(r[0]), asf(r[1]));
  const auto s = __builtin_amdgcn_permlane32_swap(asu(v), asu(v), false, false);
  return fmaxf(asf(s[0]), asf(s[1]));
}
__device__ __forceinline__ unsigned pack_halves(float v) {
  const auto r = __builtin_amdgcn_permlane32_swap(asu(v), asu(v), false, false);
  return __builtin_amdgcn_perm(r[1], r[0], 0x07060302u);
}
__device__ __forceinline__ float row_sum(float v) { v += dpp<ROR8>(v); v += dpp<ROR4>(v); v += dpp<ROR2>(v); v += dpp<ROR1>(v); return v; }
__device__ __forceinline__ float row_max(float v) { v = fmaxf(v, dpp<ROR8>(v)); v = fmaxf(v, dpp<ROR4>(v)); v = fmaxf(v, dpp<ROR2>(v)); return fmaxf(v, dpp<ROR1>(v)); }
__device__ __forceinline__ float wave_sum(float v) { v = row_sum(v); v = sw16_add(v, v); return sw32_add(v, v); }
__device__ __forceinline__ void rows_reduce8(const float (&a)[8], float& u0, float& u1) {
  const float r0 = sw32_add(a[0], a[4]), r1 = sw32_add(a[1], a[5]), r2 = sw32_add(a[2], a[6]), r3 = sw32_add(a[3], a[7]);
  u0 = sw16_add(r0, r2); u1 = sw16_add(r1, r3);
}
__device__ __forceinline__ void fma8(float (&a)[8], float h, v4u w) {
#pragma unroll
  for (int e = 0; e < 4; ++e) { a[2 * e] = fmaf(h, lo16(w[e]), a[2 * e]); a[2 * e + 1] = fmaf(h, hi16(w[e]), a[2 * e + 1]); }
}
__device__ __forceinline__ void kpair(v4u& a, v4u& b) {
  const v4u r0 = a, r1 = b;
  a = v4u{__builtin_amdgcn_perm(r1[0], r0[0], 0x05040100u), __builtin_amdgcn_perm(r1[0], r0[0], 0x07060302u),
          __builtin_amdgcn_perm(r1[1], r0[1], 0x05040100u), __builtin_amdgcn_perm(r1[1], r0[1], 0x07060302u)};
  b = v4u{__builtin_amdgcn_perm(r1[2], r0[2], 0x05040100u), __builtin_amdgcn_perm(r1[2], r0[2], 0x07060302u),
          __builtin_amdgcn_perm(r1[3], r0[3], 0x05040100u), __builtin_amdgcn_perm(r1[3], r0[3], 0x07060302u)};
}
__device__ __forceinline__ void dot8(float (&acc)[8], unsigned hp, v4u a, v4u b) {
#pragma unroll
  for (int e = 0; e < 4; ++e) { acc[e] = dot2(a[e], hp, acc[e]); acc[4 + e] = dot2(b[e], hp, acc[4 + e]); }
}
__device__ __forceinline__ f32x4 mfma(v4u a, v4u b, f32x4 c) {
  return __builtin_amdgcn_mfma_f32_16x16x32_bf16(__builtin_bit_cast(bf16x8, a), __builtin_bit_cast(bf16x8, b), c, 0, 0, 0);
}
typedef __amdgpu_buffer_rsrc_t rsrc_t;
__device__ __forceinline__ rsrc_t mkrsrc(const void* p, unsigned bytes) { return __builtin_amdgcn_make_buffer_rsrc((void*)p, 0, bytes, 0x00020000); }
constexpr int SC1 = 16, NT = 2;
template <int AUX> __device__ __forceinline__ v4u ld128(rsrc_t r, unsigned off) { return __builtin_amdgcn_raw_buffer_load_b128(r, off, 0, AUX); }
template <int AUX> __device__ __forceinline__ v2u ld64(rsrc_t r, unsigned off) { return __builtin_amdgcn_raw_buffer_load_b64(r, off, 0, AUX); }
template <int AUX> __device__ __forceinline__ unsigned ld32(rsrc_t r, unsigned off) { return __builtin_amdgcn_raw_buffer_load_b32(r, off, 0, AUX); }
__device__ __forceinline__ void st128(rsrc_t r, unsigned off, v4u v) { __builtin_amdgcn_raw_buffer_store_b128(v, r, off, 0, SC1); }
__device__ __forceinline__ void st64(rsrc_t r, unsigned off, v2u v) { __builtin_amdgcn_raw_buffer_store_b64(v, r, off, 0, SC1); }
__device__ __forceinline__ void st32(rsrc_t r, unsigned off, unsigned v) { __builtin_amdgcn_raw_buffer_store_b32(v, r, off, 0, SC1); }
__device__ __forceinline__ unsigned umax(unsigned a, unsigned b) { return a > b ? a : b; }
__device__ __forceinline__ unsigned mx4(unsigned m, v4u v) { return umax(umax(umax(m, v[0]), v[1]), umax(v[2], v[3])); }
__device__ __forceinline__ unsigned mx2(unsigned m, v2u v) { return umax(umax(m, v[0]), v[1]); }
__device__ __forceinline__ v4u noempty4(f32x4 v) { return v4u{noempty(asu(v[0])), noempty(asu(v[1])), noempty(asu(v[2])), noempty(asu(v[3]))}; }
__device__ __forceinline__ unsigned gr_lo(float v, unsigned tlo) { return ((asu(v) + 8u) & 0xFFFFFFF0u) | tlo; }
__device__ __forceinline__ unsigned gr_bad(v4u g, unsigned tlo, unsigned thi) { return (((g[0] ^ tlo) | (g[2] ^ tlo)) & 0xFu) | (g[1] ^ thi) | (g[3] ^ thi); }
__device__ __forceinline__ float gr_val(unsigned lo) { return asf(lo & 0xFFFFFFF0u); }
__device__ __forceinline__ v4u gr2(float a, float b, unsigned tlo, unsigned thi) { return v4u{gr_lo(a, tlo), thi, gr_lo(b, tlo), thi}; }
__device__ __forceinline__ float gr_sum(v4u g) { const float s = sw32_add(asf(g[0] & 0xFFFFFFF0u), asf(g[2] & 0xFFFFFFF0u)); return sw16_add(s, s); }
__device__ __forceinline__ v4u sel4(bool t, v4u a, v4u b) { return v4u{t ? a[0] : b[0], 0u, t ? a[2] : b[2], 0u}; }
#define MEMFENCE() asm volatile("" ::: "memory")
#define LGKM0() asm volatile("s_waitcnt lgkmcnt(0)" ::: "memory")
#define VM0() asm volatile("s_waitcnt vmcnt(0)" ::: "memory")
#define PIN4(a) asm volatile("" : "+v"((a)[0]), "+v"((a)[1]), "+v"((a)[2]), "+v"((a)[3]))
template <int N> __device__ __forceinline__ void vmwait() { asm volatile("s_waitcnt vmcnt(%0)" ::"n"(N) : "memory"); }
__device__ __forceinline__ void wg_barrier() { LGKM0(); __builtin_amdgcn_s_barrier(); MEMFENCE(); }
template <typename T> __device__ __forceinline__ volatile LAS T* lds_at(unsigned off) { return (volatile LAS T*)(size_t)off; }
template <typename T> __device__ __forceinline__ LAS T* lds_pl(unsigned off) { return (LAS T*)(size_t)off; }
__device__ __forceinline__ void lds_wait_ge(unsigned addr, unsigned need, u64 t0) {
  for (unsigned sp = 0; *lds_at<unsigned>(addr) < need; ++sp) { __builtin_amdgcn_s_sleep(1); if ((sp & 63u) == 63u && rt() - t0 > TLIMIT) break; }
  MEMFENCE();
}

#define EXPIRED(it) (((it) & 63u) == 63u && rt() - t0 > TLIMIT)

__device__ __forceinline__ void dma4(v4i srd, unsigned soff, unsigned ldsb, unsigned v0, unsigned v1, unsigned v2, unsigned v3) {
  asm volatile("s_mov_b32 m0, %0\n\ts_nop 0\n\t"
               "buffer_load_dwordx4 %1, %5, %6 offen nt lds\n\t"
               "buffer_load_dwordx4 %2, %5, %6 offen offset:1024 nt lds\n\t"
               "buffer_load_dwordx4 %3, %5, %6 offen offset:2048 nt lds\n\t"
               "buffer_load_dwordx4 %4, %5, %6 offen offset:3072 nt lds"
               ::"s"(ldsb), "v"(v0), "v"(v1), "v"(v2), "v"(v3), "s"(srd), "s"(soff) : "memory");
}

__device__ __forceinline__ float silu_bf(float v) { return rbf(v / (1.f + __expf(-v))); }
__device__ __forceinline__ void ln_round(float mean, float var, float& mb, float& sd) {
  mb = rbf(mean);
  sd = rbf(__builtin_amdgcn_sqrtf(rbf(rbf(var) + 1e-5f)));
}
__device__ __forceinline__ float mod_round(float n, float sc, float sh) { return rbf(rbf(rbf(n) * rbf(1.f + sc)) + sh); }

__global__ void __launch_bounds__(NTHR, 2) block_decode(Args a) {
  extern __shared__ __attribute__((aligned(16))) unsigned char smem[];
  const int tid = threadIdx.x, lane = tid & 63, wave = __builtin_amdgcn_readfirstlane(tid >> 6);
  const int b = blockIdx.x, x = b & 7, j = b >> 3;
  const u64 t0 = rt();
  const unsigned lb = (unsigned)(size_t)(LAS unsigned char*)smem;
  const rsrc_t ws = mkrsrc(a.ws, WS_END);
  const int kq = j >> 3, cb = x + 8 * (j & 7);
  const int gq = j >> 2;
  const bool mlp = (x & 1) == 0;
  const int og = (x & 6) + (j >> 4), oc0 = 32 * (j & 15);
  const bool has_gu = mlp && 4 * gq + (x >> 1) < 28;
  const int up = 4 * (4 * gq + (x >> 1)) + (j & 3);
  const int fc0 = 64 * x + 4 * j;

  if (tid < 20) *lds_pl<unsigned>(lb + L_CTL + 4 * tid) = (tid == 1 || tid == 3) ? (unsigned)INF : 0u;
  asm volatile("" :: "s"(a.K), "s"(a.ws));
  wg_barrier();
  int w_a = wave, w_b = wave;
  asm volatile("" : "+s"(w_a));
  if (w_a == 4) {
    const u64 kb = (u64)(a.K + (size_t)j * SK * DH), vb = (u64)(a.V + (size_t)j * SK * DH);
    const v4i ksrd = {(int)kb, (int)(kb >> 32), (int)HEAD_BYTES, 0x00020000}, vsrd = {(int)vb, (int)(vb >> 32), (int)HEAD_BYTES, 0x00020000};
    const unsigned g = lane >> 4, c = lane & 15;
    const unsigned k0 = g * 256 + ((c ^ g) << 4), k1 = g * 256 + ((c ^ (4 + g)) << 4), k2 = g * 256 + ((c ^ (8 + g)) << 4), k3 = g * 256 + ((c ^ (12 + g)) << 4);
    const unsigned vl = lane * 16;
    unsigned sl = lb + L_RING;
    auto fill = [&](v4i srd, int n, unsigned v0, unsigned v1, unsigned v2, unsigned v3) {
      const unsigned o = ((unsigned)n >> 2) * 65536u + ((unsigned)n & 3u) * 8192u;
      dma4(srd, o, sl, v0, v1, v2, v3);
      dma4(srd, o + 4096, sl + 4096, v0, v1, v2, v3);
      dma4(srd, o + 32768, sl + 8192, v0, v1, v2, v3);
      dma4(srd, o + 36864, sl + 12288, v0, v1, v2, v3);
      sl = sl + SLOT == lb + L_RING + NSLOT * SLOT ? lb + L_RING : sl + SLOT;
    };
    volatile LAS u64* FT = lds_at<u64>(lb + L_FT);
    int pub = 0;
    unsigned total = (unsigned)INF;
    auto publish = [&](int n) { pub = n > pub ? n : pub; *FT = (u64)(unsigned)pub | ((u64)total << 32); };
    auto wait_free = [&](int i) {
      const int p = i - NSLOT;
      volatile LAS u64* dn = lds_at<u64>(lb + L_DONE + 8 * (p & 1));
      const unsigned need = (unsigned)(p >> 1) + 1u;
      auto freed = [&]() { const u64 d = *dn; return (unsigned)d >= need && (unsigned)(d >> 32) >= need; };
      if (!freed()) {
        VM0(); publish(i);
        for (unsigned sp = 0; !freed(); ++sp) { __builtin_amdgcn_s_sleep(1); if (EXPIRED(sp)) break; }
      }
      MEMFENCE();
    };
    for (unsigned sp = 0; *lds_at<unsigned>(lb + L_ISSUED) < 5u && sp < 4096u; ++sp) {}
    unsigned tn = 1;
    auto take = [&]() {
      u64 tv;
      for (unsigned sp = 0; (unsigned)((tv = *lds_at<u64>(lb + L_TK)) >> 32) < tn; ++sp) if (EXPIRED(sp)) { tv = EMPTY; break; }
      ++tn;
      return (unsigned)__builtin_amdgcn_readfirstlane((unsigned)tv);
    };
    int i = 0, nt = 0, next = POOL0 + x * NP, left = NP;
    bool asked = true;
    for (;; ++i) {
      fill(ksrd, next, k0, k1, k2, k3);
      ++next; --left;
      if (i >= BURST) publish(i - 1);
      if (i == NP + SO - 5) *lds_at<unsigned>(lb + L_TKC) = tn;
      if (left == 0) {
        if (i == NP - 1) { next = x * SO; left = SO; }
        else {
          const unsigned t = asked ? take() : EMPTY;
          if (t >= (unsigned)NKT) break;
          left = 4; next = KD0 + 4 * (int)t;
          *lds_at<unsigned>(lb + L_LIST + 4 * nt) = t; ++nt;
          asked = i + 1 - NP + left + 4 <= CAP;
          if (asked) *lds_at<unsigned>(lb + L_TKC) = tn;
        }
      }
      if (i + 1 >= NSLOT) wait_free(i + 1);
      if (i + 1 >= BURST) vmwait<16>();
    }
    const int nk = i + 1, vown = 2 * nk - NP;
    *lds_at<unsigned>(lb + L_NK) = (unsigned)nk;
    wait_free(i + 1);
    vmwait<16>();
    next = x * SO; left = SO;
    int rt_ = 0;
    for (++i;; ++i) {
      fill(vsrd, next, vl, vl, vl, vl);
      ++next; --left;
      publish(i - 1);
      if (i == vown - 3) *lds_at<unsigned>(lb + L_TKC) = tn;
      if (left == 0) {
        if (rt_ < nt) { const unsigned t = __builtin_amdgcn_readfirstlane(*lds_at<unsigned>(lb + L_LIST + 4 * rt_)); ++rt_; left = 4; next = KD0 + 4 * (int)t; }
        else {
          const unsigned t = take();
          if (t >= (unsigned)NPT) break;
          *lds_at<unsigned>(lb + L_TKC) = tn;
          left = 2; next = POOL0 + 2 * (int)t;
        }
      }
      wait_free(i + 1);
      vmwait<16>();
    }
    total = (unsigned)i + 1u;
    publish(i - 1);
    vmwait<16>(); publish(i);
    VM0(); publish(i + 1);
    return;
  }

  asm volatile("" : "+s"(w_b));
  if (w_b == 5) {
    __builtin_amdgcn_s_setprio(3);
    const rsrc_t rwm = mkrsrc(a.wm, DM * 3072 * 2), rc = mkrsrc(a.c, DM * 2);
    const int n0 = 512 * (gq & 1) + 64 * x + 16 * (j & 3);
    const int ar = 128 * kq + 4 * (lane >> 1);
    v4u w1[4];
#pragma unroll
    for (int i = 0; i < 4; ++i) w1[i] = ld128<NT>(rwm, (unsigned)((ar + i) * 3072 + n0 + 8 * (lane & 1)) * 2);
    const v2u c4 = ld64<0>(rc, ar * 2);
    const v2u ep = ld64<SC1>(ws, WS_EPOCH);
    const int mr = 64 * gq + 8 * (lane >> 3), mc = 64 * (x + 8 * (j & 3));
    const v4u cv = ld128<0>(rc, mr * 2);
    MEMFENCE();
    if (lane == 0) __hip_atomic_fetch_add(lds_pl<unsigned>(lb + L_ISSUED), 1u, __ATOMIC_RELAXED, __HIP_MEMORY_SCOPE_WORKGROUP);
    st128(ws, (lane < 8 && !mlp) ? WS_YP + (og * 512 + oc0) * 4 + lane * 16 : OOB, v4u{EMPTY, EMPTY, EMPTY, EMPTY});
    st128(ws, has_gu ? WS_DP + up * 2048 + lane * 16 : OOB, v4u{EMPTY, EMPTY, EMPTY, EMPTY});
    st128(ws, has_gu ? WS_DP + up * 2048 + 1024 + lane * 16 : OOB, v4u{EMPTY, EMPTY, EMPTY, EMPTY});
    st128(ws, lane < 33 ? WS_PO + b * REC + lane * 16 : OOB, v4u{EMPTY, EMPTY, EMPTY, EMPTY});
    st32(ws, (lane < 2 && x == 0) ? WS_CTR + j * 256 + lane * 128 : OOB, 0u);
    const u64 e1 = (((u64)ep[1] << 32) | ep[0]) + 1ull;
    const unsigned tlo = (unsigned)e1 & 0xFu, thi = (unsigned)(e1 >> 4);
    {
      float s8[8] = {0.f, 0.f, 0.f, 0.f, 0.f, 0.f, 0.f, 0.f};
      fma8(s8, silu_bf(lo16(c4[0])), w1[0]); fma8(s8, silu_bf(hi16(c4[0])), w1[1]);
      fma8(s8, silu_bf(lo16(c4[1])), w1[2]); fma8(s8, silu_bf(hi16(c4[1])), w1[3]);
      float u0, u1;
      rows_reduce8(s8, u0, u1);
      u0 += dpp<ROR8>(u0); u1 += dpp<ROR8>(u1); u0 += dpp<ROR4>(u0); u1 += dpp<ROR4>(u1); u0 += dpp<XOR2>(u0); u1 += dpp<XOR2>(u1);
      const int col = n0 + 8 * (lane & 1) + 2 * (lane >> 4);
      st128(ws, (lane & 15) < 2 ? WS_P1 + (kq * 1024 + col) * 8 : OOB, gr2(u0, u1, tlo, thi));
      *lds_at<unsigned>(lb + L_AUXPUB) = 1u;
    }
    __builtin_amdgcn_s_setprio(0);
    auto stream_at = [&](unsigned fillno) {
      for (unsigned sp = 0; *lds_at<unsigned>(lb + L_FT) < fillno; ++sp) { __builtin_amdgcn_s_sleep(8); if ((sp & 63u) == 63u && rt() - t0 > TLIMIT) break; }
      MEMFENCE();
    };
    stream_at(NSLOT + 4);
    {
      v4u w2[8];
#pragma unroll
      for (int i = 0; i < 8; ++i) w2[i] = ld128<NT>(rwm, (unsigned)((mr + i) * 3072 + 1024 + mc + 8 * (lane & 7)) * 2);
      float s8[8] = {0.f, 0.f, 0.f, 0.f, 0.f, 0.f, 0.f, 0.f};
#pragma unroll
      for (int e = 0; e < 4; ++e) { fma8(s8, silu_bf(lo16(cv[e])), w2[2 * e]); fma8(s8, silu_bf(hi16(cv[e])), w2[2 * e + 1]); }
      float u0, u1;
      rows_reduce8(s8, u0, u1);
      u0 += dpp<ROR8>(u0); u1 += dpp<ROR8>(u1);
      st128(ws, (lane & 8) == 0 ? WS_M2P + (gq * 2048 + mc + 8 * (lane & 7) + 2 * (lane >> 4)) * 8 : OOB, gr2(u0, u1, tlo, thi));
    }
    stream_at(NSLOT + 16);
    {
      v4u m;
      for (;;) {
        MEMFENCE();
        m = ld128<SC1>(ws, WS_M2P + (((lane >> 2) & 7) * 2048 + 8 * b + 2 * (lane & 3)) * 8);
        if (__all(gr_bad(m, tlo, thi) == 0u) || rt() - t0 > TLIMIT) break;
        __builtin_amdgcn_s_sleep(32);
      }
      float u0 = gr_val(m[0]), u1 = gr_val(m[2]);
      u0 = sw16_add(u0, u0); u1 = sw16_add(u1, u1);
      u0 += dpp<ROR8>(u0); u1 += dpp<ROR8>(u1); u0 += dpp<ROR4>(u0); u1 += dpp<ROR4>(u1);
      VM0();
      st128(ws, lane < 4 ? WS_M2 + b * 64 + lane * 16 : OOB, gr2(rbf(u0), rbf(u1), tlo, thi));
    }
    stream_at(NSLOT + 28);
#pragma unroll 1
    for (int u = 0; u < 16; ++u) {
      v4u m;
      for (;;) {
        MEMFENCE();
        m = ld128<SC1>(ws, WS_M2 + u * 1024 + lane * 16);
        if (__all(gr_bad(m, tlo, thi) == 0u) || rt() - t0 > TLIMIT) break;
        __builtin_amdgcn_s_sleep(32);
      }
      *lds_pl<unsigned>(lb + L_M2 + u * 256 + lane * 4) = __builtin_amdgcn_perm(m[2], m[0], 0x07060302u);
      __builtin_amdgcn_s_sleep(16);
    }
    LGKM0();
    *lds_at<unsigned>(lb + L_M2RDY) = 1u;
    GAS unsigned* ctr = (GAS unsigned*)(a.ws + WS_CTR + j * 256);
    unsigned n = 1;
    auto draw = [&](int which) {
      unsigned v = 0;
      if (lane == 0) v = __hip_atomic_fetch_add(ctr + 32 * which, 1u, __ATOMIC_RELAXED, __HIP_MEMORY_SCOPE_AGENT);
      v = __builtin_amdgcn_readfirstlane(v);
      *lds_at<u64>(lb + L_TK) = ((u64)n << 32) | v;
      ++n;
      return v;
    };
    for (;;) {
      bool over = false;
      for (unsigned sp = 0; *lds_at<unsigned>(lb + L_TKC) < n; ++sp) {
        if (*lds_at<unsigned>(lb + L_NK) != (unsigned)INF || EXPIRED(sp)) { over = true; break; }
        __builtin_amdgcn_s_sleep(2);
      }
      if (over || *lds_at<unsigned>(lb + L_NK) != (unsigned)INF || draw(0) >= (unsigned)NKT) break;
    }
    lds_wait_ge(lb + L_KCNT, 5u, t0);
    __builtin_amdgcn_s_sleep(127);
    {
      v2u mx;
      for (;;) {
        MEMFENCE();
        mx = ld64<SC1>(ws, WS_MX + j * 64 + (lane & 7) * 8);
        if (__all((((mx[0] ^ tlo) & 0xFu) | (mx[1] ^ thi)) == 0u) || rt() - t0 > TLIMIT) break;
        __builtin_amdgcn_s_sleep(32);
      }
      *lds_at<float>(lb + L_MH) = row_max(gr_val(mx[0]));
      LGKM0();
    }
    {
      unsigned dq = 0, pend = 0;
      v4u sc = {0u, 0u, 0u, 0u};
      auto flush = [&]() {
        if ((unsigned)lane < 8u * pend) *lds_pl<v4u>(lb + L_SC + dq * 128 + lane * 16) = sc;
        dq += pend; pend = 0;
        LGKM0();
        *lds_at<unsigned>(lb + L_SCR) = dq;
      };
      for (;;) {
        if (__builtin_amdgcn_readfirstlane(*lds_at<unsigned>(lb + L_TKC)) < n) flush();
        for (unsigned sp = 0; *lds_at<unsigned>(lb + L_TKC) < n; ++sp) { __builtin_amdgcn_s_sleep(2); if (EXPIRED(sp)) break; }
        const unsigned v = draw(1);
        flush();
        if (v >= (unsigned)NPT || rt() - t0 > TLIMIT) break;
        pend = 2u;
        sc = ld128<SC1 | NT>(ws, lane < 16 ? WS_SG + (j * 8 * NP + 2 * v) * 128 + lane * 16 : OOB);
        MEMFENCE();
      }
    }
    return;
  }

  const int g = lane >> 4, c = lane & 15;
  const rsrc_t rx = mkrsrc(a.x, DM * 2), rwq = mkrsrc(a.wq, DM * 4096 * 2);
  const rsrc_t rwd = mkrsrc(a.wd, DF * DM * 2);
  const int r0 = 128 * kq + 32 * wave;
  v4u wqv[4];
#pragma unroll
  for (int i = 0; i < 4; ++i) wqv[i] = ld128<NT>(rwq, (unsigned)((r0 + 4 * (lane >> 3) + i) * 4096 + 64 * cb + 8 * (lane & 7)) * 2);
  const v2u ep = ld64<SC1>(ws, WS_EPOCH);
  const v4u x8 = ld128<0>(rx, lane * 16);
  const unsigned x2 = ld32<0>(rx, tid * 4);
  const unsigned xh = ld32<0>(rx, (r0 + 2 * c) * 2);
  MEMFENCE();
  if (lane == 0) __hip_atomic_fetch_add(lds_pl<unsigned>(lb + L_ISSUED), 1u, __ATOMIC_RELAXED, __HIP_MEMORY_SCOPE_WORKGROUP);
  float mb, sd;
  {
    float xs[8];
#pragma unroll
    for (int e = 0; e < 4; ++e) { xs[2 * e] = lo16(x8[e]); xs[2 * e + 1] = hi16(x8[e]); }
    float s = 0.f;
#pragma unroll
    for (int e = 0; e < 8; ++e) s += xs[e];
    const float mean = wave_sum(s) * (1.f / 512.f);
    float v = 0.f;
#pragma unroll
    for (int e = 0; e < 8; ++e) { const float d = xs[e] - mean; v = fmaf(d, d, v); }
    ln_round(mean, wave_sum(v) * (1.f / 512.f), mb, sd);
  }
  float nrow = rbf((lane >= 32 ? hi16(xh) : lo16(xh)) - mb) / sd;
  kpair(wqv[0], wqv[1]); kpair(wqv[2], wqv[3]);
  PIN4(wqv); asm volatile("" : "+v"(nrow));
  const u64 epoch1 = (((u64)ep[1] << 32) | ep[0]) + 1ull;
  const unsigned tlo = (unsigned)epoch1 & 0xFu, thi = (unsigned)(epoch1 >> 4);
  lds_wait_ge(lb + L_AUXPUB, 1u, t0);
  {
    const unsigned o1 = WS_P1 + (g * 1024 + r0 + 2 * c) * 8;
    v4u gs = {0u, 0u, 0u, 0u}, gc = gs;
    bool bad = true;
    __builtin_amdgcn_s_sleep(32);
    for (;;) {
      MEMFENCE();
      const v4u ns_ = ld128<SC1>(ws, bad ? o1 : OOB), nc_ = ld128<SC1>(ws, bad ? o1 + 4096 : OOB);
      const bool tk = bad & ((gr_bad(ns_, tlo, thi) | gr_bad(nc_, tlo, thi)) == 0u);
      gs = sel4(tk, ns_, gs); gc = sel4(tk, nc_, gc); bad = bad & !tk;
      if (!__any(bad) || rt() - t0 > TLIMIT) break;
    }
    const unsigned hp = pack_halves(mod_round(nrow, rbf(gr_sum(gc)), rbf(gr_sum(gs))));
    float acc[8] = {0.f, 0.f, 0.f, 0.f, 0.f, 0.f, 0.f, 0.f};
    dot8(acc, __builtin_amdgcn_ds_bpermute((lane >> 3) * 8, hp), wqv[0], wqv[1]);
    dot8(acc, __builtin_amdgcn_ds_bpermute((lane >> 3) * 8 + 4, hp), wqv[2], wqv[3]);
    float u0, u1;
    rows_reduce8(acc, u0, u1);
    u0 += dpp<ROR8>(u0); u1 += dpp<ROR8>(u1);
    if ((lane & 8) == 0) *lds_pl<f32x2>(lb + L_RED + wave * 256 + (8 * (lane & 7) + 2 * g) * 4) = f32x2{u0, u1};
    LGKM0();
    unsigned old = 0;
    if (lane == 0) old = __hip_atomic_fetch_add(lds_pl<unsigned>(lb + L_QCNT), 1u, __ATOMIC_RELAXED, __HIP_MEMORY_SCOPE_WORKGROUP);
    old = __builtin_amdgcn_readfirstlane(old);
    MEMFENCE();
    if (old == 3u) {
      const unsigned ra = lb + L_RED + lane * 4;
      const float qp = (*lds_pl<float>(ra) + *lds_pl<float>(ra + 256)) + (*lds_pl<float>(ra + 512) + *lds_pl<float>(ra + 768));
      st64(ws, WS_QS + (kq * 4096 + 64 * cb + lane) * 8, v2u{gr_lo(qp, tlo), thi});
    }
  }
  const int r8 = 8 * (tid >> 2);
  v4u wv[8];
  unsigned wdv[16];
#pragma unroll
  for (int i = 0; i < 8; ++i) { wv[i] = v4u{0u, 0u, 0u, 0u}; wdv[2 * i] = 0u; wdv[2 * i + 1] = 0u; }
  const GAS v4u* wp = (const GAS v4u*)(mlp ? ((tid & 2) ? a.wu : a.wg) + (r8 * DF + 16 * (has_gu ? up : 0) + 8 * (tid & 1)) : a.wo + ((512 * og + r8) * DM + oc0 + 8 * (tid & 3)));
  const int wstride = mlp ? DF / 8 : DM / 8;
  const unsigned wdo = has_gu ? (unsigned)(16 * up * DM + 2 * tid) * 2 : OOB;

  const unsigned key = 4 * (c & 3) + (c >> 2);
  unsigned ka[4], msk[4];
#pragma unroll
  for (int st = 0; st < 4; ++st) {
    ka[st] = lb + L_RING + (wave & 1) * 8192 + key * 256 + (((st + 4 * g) ^ key) << 4);
    msk[st] = ((c & 7) >> 1) == st ? ((c & 1) ? 0xffff0000u : 0x0000ffffu) : 0u;
  }
  const unsigned va = lb + L_RING + (wave & 1) * 8192 + lane * 16;
  const bool is_hi = c < 8;
  volatile LAS unsigned* DN = lds_at<unsigned>(lb + L_DONE + 4 * wave);
  unsigned so = (wave >> 1) * SLOT;
  v4u sv[8], sw[8], ud[8], qf[4];
#pragma unroll
  for (int i = 0; i < 8; ++i) { sv[i] = v4u{0u, 0u, 0u, 0u}; sw[i] = sv[i]; ud[i] = sv[i]; }
  int ns = 0;
  auto release = [&](v4u (&d)[8], int i) {
    LGKM0();
    *DN = (unsigned)(i >> 1) + 1u;
    asm volatile("" : "+v"(d[0]), "+v"(d[1]), "+v"(d[2]), "+v"(d[3]), "+v"(d[4]), "+v"(d[5]), "+v"(d[6]), "+v"(d[7]) :: "memory");
    so = (so + 2 * SLOT) & (NSLOT * SLOT - 1);
  };
  auto read_k = [&](v4u (&d)[8], int i) {
#pragma unroll
    for (int st = 0; st < 4; ++st) { d[st] = *lds_pl<v4u>(ka[st] + so); d[4 + st] = *lds_pl<v4u>(ka[st] + so + 4096); }
    release(d, i);
  };
  int total = INF, nk = INF;
  auto wait_full = [&](int i, bool in_k) {
    for (unsigned sp = 0;; ++sp) {
      const v4u ct = *(volatile LAS v4u*)(size_t)(lb + L_FT);
      total = __builtin_amdgcn_readfirstlane((int)ct[1]);
      nk = __builtin_amdgcn_readfirstlane((int)ct[3]);
      if (EXPIRED(sp)) { total = 0; nk = nk < i ? nk : i; break; }
      if (__builtin_amdgcn_readfirstlane((int)ct[0]) > i || (in_k && nk <= i)) break;
      if (total == INF) __builtin_amdgcn_s_sleep(1);
    }
    MEMFENCE();
  };
  float mraw = -INFINITY;
  auto scores = [&](const v4u (&d)[8], int i) {
    f32x4 sa = {0.f, 0.f, 0.f, 0.f}, sb = {0.f, 0.f, 0.f, 0.f};
#pragma unroll
    for (int st = 0; st < 4; ++st) { sa = mfma(d[st], qf[st], sa); sb = mfma(d[4 + st], qf[st], sb); }
    mraw = fmaxf(fmaxf(fmaxf(mraw, sa[0]), fmaxf(sa[1], sa[2])), fmaxf(fmaxf(sa[3], sb[0]), fmaxf(fmaxf(sb[1], sb[2]), sb[3])));
    v4u pk = {cvtpk(sa[0], sa[1]), cvtpk(sa[2], sa[3]), cvtpk(sb[0], sb[1]), cvtpk(sb[2], sb[3])};
#pragma unroll
    for (int u = 0; u < 4; ++u) pk[u] = cvtpk(lo16(pk[u]) * SCALE, hi16(pk[u]) * SCALE);
    const unsigned so_ = (wave & 1) * 64 + g * 16;
    const unsigned dst = i < NP ? L_SH + i * 128 : L_SC + (i - NP) * 128;
    if (c == 0) *lds_pl<v4u>(lb + dst + so_) = pk;
  };
  auto stash = [&]() {
    const int full = __builtin_amdgcn_readfirstlane((int)*lds_at<unsigned>(lb + L_FT)), i0 = wave >> 1;
    MEMFENCE();
    if (ns == 0 && full > i0) { read_k(wv, i0); ns = 1; }
    if (ns == 1 && full > i0 + 2) { read_k(sv, i0 + 2); ns = 2; }
    if (ns == 2 && full > i0 + 4) { read_k(sw, i0 + 4); ns = 3; }
    if (ns == 3 && full > i0 + 6) { read_k(ud, i0 + 6); ns = 4; }
  };
  {
    const unsigned o2 = WS_QS + (g * 4096 + 128 * j + 32 * wave + 2 * c) * 8;
    v4u gq4 = {0u, 0u, 0u, 0u};
    bool bad = true;
    stash();
    {
      const v4u nq = ld128<SC1>(ws, o2);
      bad = gr_bad(nq, tlo, thi) != 0u;
      gq4 = nq;
    }
    asm volatile("" : "+v"(gq4) :: "memory");
    stash();
    for (unsigned it = 0; __any(bad) && !EXPIRED(it); ++it) {
      MEMFENCE();
      const v4u nq = ld128<SC1>(ws, bad ? o2 : OOB);
      const bool tk = bad & (gr_bad(nq, tlo, thi) == 0u);
      gq4 = sel4(tk, nq, gq4); bad = bad & !tk;
    }
    *lds_pl<unsigned>(lb + L_QSC + (16 * wave + c) * 4) = pack_halves(rbf(gr_sum(gq4)));
    LGKM0();
    if (lane == 0) __hip_atomic_fetch_add(lds_pl<unsigned>(lb + L_QIN), 1u, __ATOMIC_RELAXED, __HIP_MEMORY_SCOPE_WORKGROUP);
    lds_wait_ge(lb + L_QIN, 4u, t0);
#pragma unroll
    for (int st = 0; st < 4; ++st) qf[st] = *lds_pl<v4u>(lb + L_QSC + (st + 4 * g) * 16);
  }
  if (ns >= 1) scores(wv, wave >> 1);
  if (ns >= 2) scores(sv, (wave >> 1) + 2);
  if (ns >= 3) scores(sw, (wave >> 1) + 4);
  if (ns >= 4) scores(ud, (wave >> 1) + 6);
  int i = (wave >> 1) + 2 * ns;
  for (;; i += 2) {
    wait_full(i, true);
    if (nk <= i) break;
    read_k(ud, i);
    scores(ud, i);
  }
  float m2;
  {
    *lds_pl<float>(lb + L_MXW + wave * 4) = rbf(rbf(xrow_max(mraw)) * SCALE);
    LGKM0();
    unsigned old = 0;
    if (lane == 0) old = __hip_atomic_fetch_add(lds_pl<unsigned>(lb + L_KCNT), 1u, __ATOMIC_RELAXED, __HIP_MEMORY_SCOPE_WORKGROUP);
    old = __builtin_amdgcn_readfirstlane(old);
    lds_wait_ge(lb + L_KCNT, 4u, t0);
    const f32x4 mw = *lds_pl<f32x4>(lb + L_MXW);
    const float mg = fmaxf(fmaxf(mw[0], mw[1]), fmaxf(mw[2], mw[3]));
    if (old == 3u) {
#pragma unroll
      for (int u = 0; u < (NP + 7) / 8; ++u)
        __builtin_amdgcn_raw_buffer_store_b128(*lds_pl<v4u>(lb + L_SH + u * 1024 + lane * 16), ws,
                                               u * 1024 + lane * 16 < NP * 128 ? WS_SG + b * NP * 128 + u * 1024 + lane * 16 : OOB, 0, SC1 | NT);
      VM0();
      st64(ws, lane == 0 ? WS_MX + b * 8 : OOB, v2u{gr_lo(mg, tlo), thi});
      *lds_at<unsigned>(lb + L_KCNT) = 5u;
    }
    m2 = mg * LOG2E;
  }
  float lsum = 0.f;
  f32x4 acc = {0.f, 0.f, 0.f, 0.f};
  {
    int wk = (mlp && !has_gu) ? 11 : 0;
    bool done = false, own = true;
    for (;; i += 2) {
      int e = i - nk;
      if (!done && e >= nk - NP) {
        e -= nk - NP;
        for (unsigned sp = 0;; ++sp) {
          const v4u ct = *(volatile LAS v4u*)(size_t)(lb + L_FT);
          if (__builtin_amdgcn_readfirstlane((int)ct[2]) > e) break;
          if (__builtin_amdgcn_readfirstlane((int)ct[1]) <= i || EXPIRED(sp)) { done = true; break; }
          __builtin_amdgcn_s_sleep(1);
        }
        MEMFENCE();
        if (own) {
          const float mh = *lds_at<float>(lb + L_MH) * LOG2E, al = __builtin_amdgcn_exp2f(m2 - mh);
          acc *= al; lsum *= al; m2 = mh; own = false;
        }
      }
      if (!done) {
        const v4u sc = *lds_pl<v4u>(lb + L_SC + e * 128 + (wave & 1) * 64 + g * 16);
        v4u pa[8];
#pragma unroll
        for (int u = 0; u < 8; ++u) {
          const float p = __builtin_amdgcn_exp2f(fmaf((u & 1) ? hi16(sc[u >> 1]) : lo16(sc[u >> 1]), LOG2E, -m2));
          lsum += p;
          const float ph = hi16(asu(p));
          const float xv = is_hi ? ph : p - ph;
          const unsigned pk = cvtpk(xv, xv);
          pa[u] = v4u{pk & msk[0], pk & msk[1], pk & msk[2], pk & msk[3]};
        }
        asm volatile("" : "+v"(pa[0]), "+v"(pa[1]), "+v"(pa[2]), "+v"(pa[3]), "+v"(pa[4]), "+v"(pa[5]), "+v"(pa[6]), "+v"(pa[7]));
        wait_full(i, false);
#pragma unroll
        for (int u = 0; u < 8; ++u) ud[u] = *lds_pl<v4u>(va + so + u * 1024);
        release(ud, i);
#pragma unroll
        for (int u = 0; u < 8; ++u) acc = mfma(pa[u], ud[u], acc);
        done = i + 2 >= total && !own;
      }
      MEMFENCE();
      if (wk <= 10 && (done || rt() - t0 >= (u64)(TW + TD * wk + (wk == 10 ? 400 : 0)))) {
#pragma unroll
        for (int u = 0; u < 8; ++u) if (wk == u) wv[u] = __builtin_nontemporal_load(wp + u * wstride);
#pragma unroll
        for (int v = 0; v < 2; ++v) if (wk == 8 + v) {
#pragma unroll
          for (int u = 8 * v; u < 8 * v + 8; ++u) wdv[u] = ld32<NT>(rwd, wdo + u * (DM * 2));
        }
        if (wk == 10) {
#pragma unroll
          for (int u = 0; u < 8; u += 2) kpair(wv[u], wv[u + 1]);
#pragma unroll
          for (int u = 0; u < 16; u += 2) {
            const unsigned lo = __builtin_amdgcn_perm(wdv[u + 1], wdv[u], 0x05040100u), hi = __builtin_amdgcn_perm(wdv[u + 1], wdv[u], 0x07060302u);
            wdv[u] = lo; wdv[u + 1] = hi;
          }
        }
        ++wk;
      }
      MEMFENCE();
      if (done && wk > 10) break;
    }
  }
  {
    f32x4 o;
#pragma unroll
    for (int r = 0; r < 4; ++r) o[r] = sw32_add(acc[r], acc[r]);
    float l = sw16_add(lsum, lsum); l = sw32_add(l, l);
    *lds_pl<f32x4>(lb + L_MRG + wave * 512 + (8 * c + 4 * (g & 1)) * 4) = o;
    *lds_pl<float>(lb + L_LW + wave * 4) = l;
    wg_barrier();
    if (wave == 0) {
      const unsigned ra = lb + L_MRG + (lane & 31) * 16;
      const f32x4 lw = *lds_pl<f32x4>(lb + L_LW);
      f32x4 O = (*lds_pl<f32x4>(ra) + *lds_pl<f32x4>(ra + 512)) + (*lds_pl<f32x4>(ra + 1024) + *lds_pl<f32x4>(ra + 1536));
      if (lane >= 32) O = f32x4{(lw[0] + lw[1]) + (lw[2] + lw[3]), 0.f, 0.f, 0.f};
      st128(ws, lane < 33 ? WS_PO + b * REC + lane * 16 : OOB, noempty4(O));
    }
  }
  if (!mlp) {
    const unsigned hb = WS_PO + (4 * og + wave) * 8 * REC;
    v2u po[8];
    unsigned ls = EMPTY;
#pragma unroll
    for (int s = 0; s < 8; ++s) po[s] = v2u{0u, 0u};
    unsigned miss = 0x1FFu;
    for (unsigned it = 0;; ++it) {
      MEMFENCE();
      if (miss & 0x100u) ls = ld32<SC1>(ws, (lane & 15) < 8 ? hb + (lane & 15) * REC + 512 : OOB);
#pragma unroll
      for (int s = 0; s < 8; ++s) if (miss & (1u << s)) po[s] = ld64<SC1>(ws, hb + s * REC + lane * 8);
      if ((miss & 0x100u) && __all(ls != EMPTY)) miss &= ~0x100u;
#pragma unroll
      for (int s = 0; s < 8; ++s) if ((miss & (1u << s)) && __all(mx2(0u, po[s]) != EMPTY)) miss &= ~(1u << s);
      if (miss == 0u || EXPIRED(it)) break;
    }
    const float inv = __builtin_amdgcn_rcpf(row_sum(asf(ls)));
    const unsigned op = cvtpk((((asf(po[0][0]) + asf(po[1][0])) + (asf(po[2][0]) + asf(po[3][0]))) + ((asf(po[4][0]) + asf(po[5][0])) + (asf(po[6][0]) + asf(po[7][0])))) * inv,
                              (((asf(po[0][1]) + asf(po[1][1])) + (asf(po[2][1]) + asf(po[3][1]))) + ((asf(po[4][1]) + asf(po[5][1])) + (asf(po[6][1]) + asf(po[7][1])))) * inv);
    float y[8] = {0.f, 0.f, 0.f, 0.f, 0.f, 0.f, 0.f, 0.f};
    dot8(y, dppu<0x00>(op), wv[0], wv[1]); dot8(y, dppu<0x55>(op), wv[2], wv[3]);
    dot8(y, dppu<0xAA>(op), wv[4], wv[5]); dot8(y, dppu<0xFF>(op), wv[6], wv[7]);
    float u0, u1;
    rows_reduce8(y, u0, u1);
    u0 += dpp<ROR8>(u0); u1 += dpp<ROR8>(u1); u0 += dpp<ROR4>(u0); u1 += dpp<ROR4>(u1);
    *lds_pl<f32x2>(lb + L_RED3 + wave * 128 + (8 * (c & 3) + 2 * g) * 4) = f32x2{u0, u1};
    wg_barrier();
    if (wave == 0) {
      const unsigned ra = lb + L_RED3 + (lane & 7) * 16;
      const f32x4 t = (*lds_pl<f32x4>(ra) + *lds_pl<f32x4>(ra + 128)) + (*lds_pl<f32x4>(ra + 256) + *lds_pl<f32x4>(ra + 384));
      st128(ws, lane < 8 ? WS_YP + (og * 512 + oc0) * 4 + lane * 16 : OOB, noempty4(t));
    }
    return;
  }
  lds_wait_ge(lb + L_M2RDY, 1u, t0);
  float gbf = lo16(*lds_pl<u16>(lb + L_M2 + 3072 + (fc0 + g) * 2));
  {
    const unsigned gaw = *lds_pl<unsigned>(lb + L_M2 + tid * 4), sbw = *lds_pl<unsigned>(lb + L_M2 + 1024 + tid * 4), cbw = *lds_pl<unsigned>(lb + L_M2 + 2048 + tid * 4);
    float ga0 = lo16(gaw), ga1 = hi16(gaw), sb0 = lo16(sbw), sb1 = hi16(sbw), t0c = rbf(1.f + lo16(cbw)), t1c = rbf(1.f + hi16(cbw)), xa = lo16(x2), xb = hi16(x2);
    asm volatile("" : "+v"(ga0), "+v"(ga1), "+v"(sb0), "+v"(sb1), "+v"(t0c), "+v"(t1c), "+v"(xa), "+v"(xb), "+v"(gbf));
    v2u yp[8];
#pragma unroll
    for (int s = 0; s < 8; ++s) yp[s] = v2u{0u, 0u};
    unsigned miss = 0xFFu;
    for (unsigned it = 0;; ++it) {
      MEMFENCE();
#pragma unroll
      for (int s = 0; s < 8; ++s) if (miss & (1u << s)) yp[s] = ld64<SC1>(ws, WS_YP + s * 2048 + tid * 8);
#pragma unroll
      for (int s = 0; s < 8; ++s) if ((miss & (1u << s)) && __all(mx2(0u, yp[s]) != EMPTY)) miss &= ~(1u << s);
      if (miss == 0u || EXPIRED(it)) break;
    }
    const unsigned atp = cvtpk(((asf(yp[0][0]) + asf(yp[1][0])) + (asf(yp[2][0]) + asf(yp[3][0]))) + ((asf(yp[4][0]) + asf(yp[5][0])) + (asf(yp[6][0]) + asf(yp[7][0]))),
                               ((asf(yp[0][1]) + asf(yp[1][1])) + (asf(yp[2][1]) + asf(yp[3][1]))) + ((asf(yp[4][1]) + asf(yp[5][1])) + (asf(yp[6][1]) + asf(yp[7][1]))));
    const unsigned gap = cvtpk(ga0 * lo16(atp), ga1 * hi16(atp));
    const unsigned x1p = cvtpk(xa + lo16(gap), xb + hi16(gap));
    const float x1a = lo16(x1p), x1b = hi16(x1p);
    const float sh = asf(__builtin_amdgcn_readfirstlane(asu(x1a)));
    const float da = x1a - sh, db = x1b - sh;
    const float s1 = wave_sum(da + db), s2 = wave_sum(fmaf(da, da, db * db));
    *lds_pl<f32x2>(lb + L_ST + wave * 8) = f32x2{fmaf(s1, 1.f / 128.f, sh), fmaf(-s1 * (1.f / 128.f), s1, s2)};
    *lds_pl<f32x2>(lb + L_X1 + tid * 8) = f32x2{x1a, x1b};
    wg_barrier();
    if (has_gu) {
      const f32x4 sA = *lds_pl<f32x4>(lb + L_ST), sB = *lds_pl<f32x4>(lb + L_ST + 16);
      const float mean = ((sA[0] + sA[2]) + (sB[0] + sB[2])) * 0.25f;
      const float d0 = sA[0] - mean, d1 = sA[2] - mean, d2 = sB[0] - mean, d3 = sB[2] - mean;
      const float var = fmaxf(((sA[1] + sA[3]) + (sB[1] + sB[3])) + 128.f * ((d0 * d0 + d1 * d1) + (d2 * d2 + d3 * d3)), 0.f) * (1.f / 512.f);
      float mb2, sd2;
      ln_round(mean, var, mb2, sd2);
      const float rs = __builtin_amdgcn_rcpf(sd2);
      const unsigned e0 = cvtpk(x1a - mb2, x1b - mb2), e1 = cvtpk(lo16(e0) * rs, hi16(e0) * rs), e2 = cvtpk(lo16(e1) * t0c, hi16(e1) * t1c);
      const unsigned hq = cvtpk(lo16(e2) + sb0, hi16(e2) + sb1);
      float y[8] = {0.f, 0.f, 0.f, 0.f, 0.f, 0.f, 0.f, 0.f};
      dot8(y, dppu<0x00>(hq), wv[0], wv[1]); dot8(y, dppu<0x55>(hq), wv[2], wv[3]);
      dot8(y, dppu<0xAA>(hq), wv[4], wv[5]); dot8(y, dppu<0xFF>(hq), wv[6], wv[7]);
      float u0, u1;
      rows_reduce8(y, u0, u1);
      u0 += dpp<ROR8>(u0); u1 += dpp<ROR8>(u1); u0 += dpp<ROR4>(u0); u1 += dpp<ROR4>(u1);
      *lds_pl<f32x2>(lb + L_RED3 + wave * 128 + (8 * (c & 3) + 2 * g) * 4) = f32x2{u0, u1};
      wg_barrier();
      const unsigned ra = lb + L_RED3 + c * 4;
      const float gs = rbf((*lds_pl<float>(ra) + *lds_pl<float>(ra + 128)) + (*lds_pl<float>(ra + 256) + *lds_pl<float>(ra + 384)));
      const float us = rbf((*lds_pl<float>(ra + 64) + *lds_pl<float>(ra + 192)) + (*lds_pl<float>(ra + 320) + *lds_pl<float>(ra + 448)));
      const float z = 0.7978845608028654f * fmaf(0.044715f * gs * gs, gs, gs);
      const float th = fmaf(-2.f, __builtin_amdgcn_rcpf(1.f + __expf(2.f * z)), 1.f);
      const float h2 = rbf(rbf(0.5f * gs * (1.f + th)) * us);
      const unsigned hp = __builtin_amdgcn_perm(asu(dpp<XOR1>(h2)), asu(h2), 0x07060302u);
      float d0a = 0.f, d1a = 0.f;
#pragma unroll
      for (int e = 0; e < 16; e += 2) {
        const unsigned he = __builtin_amdgcn_readlane(hp, e);
        d0a = dot2(wdv[e], he, d0a); d1a = dot2(wdv[e + 1], he, d1a);
      }
      st64(ws, WS_DP + up * 2048 + tid * 8, v2u{noempty(asu(d0a)), noempty(asu(d1a))});
      st64(ws, (b | tid) == 0 ? WS_EPOCH : OOB, v2u{(unsigned)epoch1, (unsigned)(epoch1 >> 32)});
    }
  }
  if (wave != 0) return;
  {
    v4u da = {EMPTY, 0u, 0u, 0u}, db = {lane < NGU - 64 ? EMPTY : 0u, 0u, 0u, 0u};
    const float x1o = *lds_pl<float>(lb + L_X1 + (fc0 + g) * 4);
    for (unsigned it = 0;; ++it) {
      MEMFENCE();
      const bool ma = mx4(0u, da) == EMPTY, mb_ = mx4(0u, db) == EMPTY;
      if (!__any(ma | mb_) || EXPIRED(it)) break;
      const v4u na = ld128<SC1>(ws, ma ? WS_DP + lane * 2048 + fc0 * 4 : OOB), nb = ld128<SC1>(ws, mb_ ? WS_DP + (lane + 64) * 2048 + fc0 * 4 : OOB);
      da = ma ? na : da; db = mb_ ? nb : db;
    }
    float t4[4];
#pragma unroll
    for (int r = 0; r < 4; ++r) t4[r] = asf(da[r]) + asf(db[r]);
    const float r = row_sum(sw16_add(sw32_add(t4[0], t4[2]), sw32_add(t4[1], t4[3])));
    const float res = x1o + rbf(gbf * rbf(r));
    if (c == 0) a.out[fc0 + g] = (u16)(cvtpk(res, res) & 0xffffu);
  }
}
}

extern "C" void kernel_launch(void* const* d_in, const int* in_sizes, int n_in, void* d_out, int out_size, void* d_ws, size_t ws_size,
                              hipStream_t stream) {
  Args a;
  a.x = (const u16*)d_in[0]; a.K = (const u16*)d_in[1]; a.V = (const u16*)d_in[2]; a.wq = (const u16*)d_in[3]; a.wo = (const u16*)d_in[4];
  a.wg = (const u16*)d_in[6]; a.wu = (const u16*)d_in[7]; a.wd = (const u16*)d_in[8]; a.c = (const u16*)d_in[9]; a.wm = (const u16*)d_in[10];
  a.out = (u16*)d_out; a.ws = (unsigned char*)d_ws;
  (void)hipFuncSetAttribute((const void*)block_decode, hipFuncAttributeMaxDynamicSharedMemorySize, (int)LDS_BYTES);
  hipLaunchKernelGGL(block_decode, dim3(NWG), dim3(NTHR), LDS_BYTES, stream, a);
}
```

```cpp
#include <hip/hip_runtime.h>
#include <hip/hip_bf16.h>

#define GAS __attribute__((address_space(1)))
#define LAS __attribute__((address_space(3)))
typedef unsigned short u16;
typedef unsigned long long u64;
typedef unsigned v4u __attribute__((ext_vector_type(4)));
typedef unsigned v2u __attribute__((ext_vector_type(2)));
typedef int v4i __attribute__((ext_vector_type(4)));
typedef float f32x4 __attribute__((ext_vector_type(4)));
typedef __bf16 bf16x8 __attribute__((ext_vector_type(8)));
typedef __bf16 bf16x2 __attribute__((ext_vector_type(2)));
typedef float f32x2 __attribute__((ext_vector_type(2)));

namespace {
constexpr int DM = 512, SK = 65536, DH = 128, DF = 1792;
constexpr int NWG = 256, NTHR = 384;
constexpr int SLOT = 16384, NSLOT = 8, FH = 1024;
constexpr int SO = 112;
constexpr int NP = 8;
constexpr int POOL0 = 8 * SO, KD0 = POOL0 + 8 * NP, NKT = (FH - KD0) / 4, NPT = 8 * NP / 2;
constexpr int CAP = 144;
static_assert((FH - KD0) % 4 == 0 && SO % 4 == 0 && NP % 2 == 0 && 8 * NP + 16 <= SO && NP <= 8 && 8 * (CAP - 7 - SO) >= FH - KD0, "fill plan");
constexpr int BURST = 3;
constexpr unsigned TW = 10800, TD = 110;
constexpr int INF = 0x7FFFFFFF;
constexpr unsigned HEAD_BYTES = (unsigned)SK * DH * 2;
constexpr unsigned EMPTY = 0xFFFFFFFFu;
constexpr unsigned OOB = 0x7FFF0000u;
constexpr u64 TLIMIT = 5000000ull;
constexpr float LOG2E = 1.4426950408889634f, SCALE = 0.08838834764831845f;
constexpr int NGU = DF / 16, REC = 640;
constexpr unsigned WS_EPOCH = 0, WS_P1 = 4096, WS_QS = WS_P1 + 32768, WS_M2P = WS_QS + 131072, WS_M2 = WS_M2P + 131072, WS_CTR = WS_M2 + 16384,
                   WS_MX = WS_CTR + 8192, WS_YP = WS_MX + 2048, WS_PO = WS_YP + 16384, WS_DP = WS_PO + NWG * REC,
                   WS_SG = WS_DP + NGU * 2048, WS_END = WS_SG + NWG * NP * 128;
constexpr unsigned L_RING = 0, L_CTL = NSLOT * SLOT, L_FT = L_CTL, L_SCR = L_CTL + 8, L_NK = L_CTL + 12, L_DONE = L_CTL + 16, L_QCNT = L_CTL + 32,
                   L_AUXPUB = L_CTL + 36, L_QIN = L_CTL + 40, L_ISSUED = L_CTL + 44, L_M2RDY = L_CTL + 48, L_TKC = L_CTL + 52, L_TK = L_CTL + 56,
                   L_KCNT = L_CTL + 64, L_MH = L_CTL + 68, L_MXW = L_CTL + 80, L_LW = L_CTL + 96, L_LIST = L_CTL + 128, L_RED = L_LIST + 128, L_QSC = L_RED + 1024, L_RED3 = L_QSC + 256,
                   L_ST = L_RED3 + 512, L_M2 = L_ST + 64, L_X1 = L_M2 + 4096, L_MRG = L_X1 + 2048, L_SH = L_X1, L_SC = L_MRG + 2048, L_END = L_SC + CAP * 128;
constexpr unsigned LDS_BYTES = L_END;
static_assert(LDS_BYTES <= 160 * 1024 && LDS_BYTES > 80 * 1024, "one workgroup per CU");

struct Args {
  const u16 *x, *K, *V, *wq, *wo, *wg, *wu, *wd, *c, *wm;
  u16* out;
  unsigned char* ws;
};

__device__ __forceinline__ u64 rt() { return __builtin_amdgcn_s_memrealtime(); }
__device__ __forceinline__ float asf(unsigned u) { return __builtin_bit_cast(float, u); }
__device__ __forceinline__ unsigned asu(float f) { return __builtin_bit_cast(unsigned, f); }
__device__ __forceinline__ float lo16(unsigned u) { return asf(u << 16); }
__device__ __forceinline__ float hi16(unsigned u) { return asf(u & 0xffff0000u); }
__device__ __forceinline__ unsigned cvtpk(float a, float b) {
  const f32x2 v = {a, b};
  return __builtin_bit_cast(unsigned, __builtin_convertvector(v, bf16x2));
}
__device__ __forceinline__ float rbf(float f) { return lo16(cvtpk(f, f)); }
__device__ __forceinline__ unsigned noempty(unsigned v) { return v < EMPTY - 1u ? v : EMPTY - 1u; }
__device__ __forceinline__ float dot2(unsigned a, unsigned b, float c) {
  return __builtin_amdgcn_fdot2_f32_bf16(__builtin_bit_cast(bf16x2, a), __builtin_bit_cast(bf16x2, b), c, false);
}
template <int CTRL> __device__ __forceinline__ float dpp(float v) {
  return asf(__builtin_amdgcn_mov_dpp(asu(v), CTRL, 0xf, 0xf, true));
}
constexpr int ROR8 = 0x128, ROR4 = 0x124, ROR2 = 0x122, ROR1 = 0x121, XOR2 = 0x4E, XOR1 = 0xB1;
template <int CTRL> __device__ __forceinline__ unsigned dppu(unsigned v) { return __builtin_amdgcn_mov_dpp(v, CTRL, 0xf, 0xf, true); }
__device__ __forceinline__ float sw32_add(float a, float b) {
  const auto r = __builtin_amdgcn_permlane32_swap(asu(a), asu(b), false, false);
  return asf(r[0]) + asf(r[1]);
}
__device__ __forceinline__ float sw16_add(float a, float b) {
  const auto r = __builtin_amdgcn_permlane16_swap(asu(a), asu(b), false, false);
  return asf(r[0]) + asf(r[1]);
}
__device__ __forceinline__ float xrow_max(float v) {
  const auto r = __builtin_amdgcn_permlane16_swap(asu(v), asu(v), false, false);
  v = fmaxf(asf(r[0]), asf(r[1]));
  const auto s = __builtin_amdgcn_permlane32_swap(asu(v), asu(v), false, false);
  return fmaxf(asf(s[0]), asf(s[1]));
}
__device__ __forceinline__ unsigned pack_halves(float v) {
  const auto r = __builtin_amdgcn_permlane32_swap(asu(v), asu(v), false, false);
  return __builtin_amdgcn_perm(r[1], r[0], 0x07060302u);
}
__device__ __forceinline__ float row_sum(float v) { v += dpp<ROR8>(v); v += dpp<ROR4>(v); v += dpp<ROR2>(v); v += dpp<ROR1>(v); return v; }
__device__ __forceinline__ float row_max(float v) { v = fmaxf(v, dpp<ROR8>(v)); v = fmaxf(v, dpp<ROR4>(v)); v = fmaxf(v, dpp<ROR2>(v)); return fmaxf(v, dpp<ROR1>(v)); }
__device__ __forceinline__ float wave_sum(float v) { v = row_sum(v); v = sw16_add(v, v); return sw32_add(v, v); }
__device__ __forceinline__ void rows_reduce8(const float (&a)[8], float& u0, float& u1) {
  const float r0 = sw32_add(a[0], a[4]), r1 = sw32_add(a[1], a[5]), r2 = sw32_add(a[2], a[6]), r3 = sw32_add(a[3], a[7]);
  u0 = sw16_add(r0, r2); u1 = sw16_add(r1, r3);
}
__device__ __forceinline__ void fma8(float (&a)[8], float h, v4u w) {
#pragma unroll
  for (int e = 0; e < 4; ++e) { a[2 * e] = fmaf(h, lo16(w[e]), a[2 * e]); a[2 * e + 1] = fmaf(h, hi16(w[e]), a[2 * e + 1]); }
}
__device__ __forceinline__ void kpair(v4u& a, v4u& b) {
  const v4u r0 = a, r1 = b;
  a = v4u{__builtin_amdgcn_perm(r1[0], r0[0], 0x05040100u), __builtin_amdgcn_perm(r1[0], r0[0], 0x07060302u),
          __builtin_amdgcn_perm(r1[1], r0[1], 0x05040100u), __builtin_amdgcn_perm(r1[1], r0[1], 0x07060302u)};
  b = v4u{__builtin_amdgcn_perm(r1[2], r0[2], 0x05040100u), __builtin_amdgcn_perm(r1[2], r0[2], 0x07060302u),
          __builtin_amdgcn_perm(r1[3], r0[3], 0x05040100u), __builtin_amdgcn_perm(r1[3], r0[3], 0x07060302u)};
}
__device__ __forceinline__ void dot8(float (&acc)[8], unsigned hp, v4u a, v4u b) {
#pragma unroll
  for (int e = 0; e < 4; ++e) { acc[e] = dot2(a[e], hp, acc[e]); acc[4 + e] = dot2(b[e], hp, acc[4 + e]); }
}
__device__ __forceinline__ f32x4 mfma(v4u a, v4u b, f32x4 c) {
  return __builtin_amdgcn_mfma_f32_16x16x32_bf16(__builtin_bit_cast(bf16x8, a), __builtin_bit_cast(bf16x8, b), c, 0, 0, 0);
}
typedef __amdgpu_buffer_rsrc_t rsrc_t;
__device__ __forceinline__ rsrc_t mkrsrc(const void* p, unsigned bytes) { return __builtin_amdgcn_make_buffer_rsrc((void*)p, 0, bytes, 0x00020000); }
constexpr int SC1 = 16, NT = 2;
template <int AUX> __device__ __forceinline__ v4u ld128(rsrc_t r, unsigned off) { return __builtin_amdgcn_raw_buffer_load_b128(r, off, 0, AUX); }
template <int AUX> __device__ __forceinline__ v2u ld64(rsrc_t r, unsigned off) { return __builtin_amdgcn_raw_buffer_load_b64(r, off, 0, AUX); }
template <int AUX> __device__ __forceinline__ unsigned ld32(rsrc_t r, unsigned off) { return __builtin_amdgcn_raw_buffer_load_b32(r, off, 0, AUX); }
__device__ __forceinline__ void st128(rsrc_t r, unsigned off, v4u v) { __builtin_amdgcn_raw_buffer_store_b128(v, r, off, 0, SC1); }
__device__ __forceinline__ void st64(rsrc_t r, unsigned off, v2u v) { __builtin_amdgcn_raw_buffer_store_b64(v, r, off, 0, SC1); }
__device__ __forceinline__ void st32(rsrc_t r, unsigned off, unsigned v) { __builtin_amdgcn_raw_buffer_store_b32(v, r, off, 0, SC1); }
__device__ __forceinline__ unsigned umax(unsigned a, unsigned b) { return a > b ? a : b; }
__device__ __forceinline__ unsigned mx4(unsigned m, v4u v) { return umax(umax(umax(m, v[0]), v[1]), umax(v[2], v[3])); }
__device__ __forceinline__ unsigned mx2(unsigned m, v2u v) { return umax(umax(m, v[0]), v[1]); }
__device__ __forceinline__ v4u noempty4(f32x4 v) { return v4u{noempty(asu(v[0])), noempty(asu(v[1])), noempty(asu(v[2])), noempty(asu(v[3]))}; }
__device__ __forceinline__ unsigned gr_lo(float v, unsigned tlo) { return ((asu(v) + 8u) & 0xFFFFFFF0u) | tlo; }
__device__ __forceinline__ unsigned gr_bad(v4u g, unsigned tlo, unsigned thi) { return (((g[0] ^ tlo) | (g[2] ^ tlo)) & 0xFu) | (g[1] ^ thi) | (g[3] ^ thi); }
__device__ __forceinline__ float gr_val(unsigned lo) { return asf(lo & 0xFFFFFFF0u); }
__device__ __forceinline__ v4u gr2(float a, float b, unsigned tlo, unsigned thi) { return v4u{gr_lo(a, tlo), thi, gr_lo(b, tlo), thi}; }
__device__ __forceinline__ float gr_sum(v4u g) { const float s = sw32_add(asf(g[0] & 0xFFFFFFF0u), asf(g[2] & 0xFFFFFFF0u)); return sw16_add(s, s); }
__device__ __forceinline__ v4u sel4(bool t, v4u a, v4u b) { return v4u{t ? a[0] : b[0], 0u, t ? a[2] : b[2], 0u}; }
#define MEMFENCE() asm volatile("" ::: "memory")
#define LGKM0() asm volatile("s_waitcnt lgkmcnt(0)" ::: "memory")
#define VM0() asm volatile("s_waitcnt vmcnt(0)" ::: "memory")
#define PIN4(a) asm volatile("" : "+v"((a)[0]), "+v"((a)[1]), "+v"((a)[2]), "+v"((a)[3]))
template <int N> __device__ __forceinline__ void vmwait() { asm volatile("s_waitcnt vmcnt(%0)" ::"n"(N) : "memory"); }
__device__ __forceinline__ void wg_barrier() { LGKM0(); __builtin_amdgcn_s_barrier(); MEMFENCE(); }
template <typename T> __device__ __forceinline__ volatile LAS T* lds_at(unsigned off) { return (volatile LAS T*)(size_t)off; }
template <typename T> __device__ __forceinline__ LAS T* lds_pl(unsigned off) { return (LAS T*)(size_t)off; }
__device__ __forceinline__ void lds_wait_ge(unsigned addr, unsigned need, u64 t0) {
  for (unsigned sp = 0; *lds_at<unsigned>(addr) < need; ++sp) { __builtin_amdgcn_s_sleep(1); if ((sp & 63u) == 63u && rt() - t0 > TLIMIT) break; }
  MEMFENCE();
}

#define EXPIRED(it) (((it) & 63u) == 63u && rt() - t0 > TLIMIT)

__device__ __forceinline__ void dma4(v4i srd, unsigned soff, unsigned ldsb, unsigned v0, unsigned v1, unsigned v2, unsigned v3) {
  asm volatile("s_mov_b32 m0, %0\n\ts_nop 0\n\t"
               "buffer_load_dwordx4 %1, %5, %6 offen nt lds\n\t"
               "buffer_load_dwordx4 %2, %5, %6 offen offset:1024 nt lds\n\t"
               "buffer_load_dwordx4 %3, %5, %6 offen offset:2048 nt lds\n\t"
               "buffer_load_dwordx4 %4, %5, %6 offen offset:3072 nt lds"
               ::"s"(ldsb), "v"(v0), "v"(v1), "v"(v2), "v"(v3), "s"(srd), "s"(soff) : "memory");
}

__device__ __forceinline__ float silu_bf(float v) { return rbf(v / (1.f + __expf(-v))); }
__device__ __forceinline__ void ln_round(float mean, float var, float& mb, float& sd) {
  mb = rbf(mean);
  sd = rbf(__builtin_amdgcn_sqrtf(rbf(rbf(var) + 1e-5f)));
}
__device__ __forceinline__ float mod_round(float n, float sc, float sh) { return rbf(rbf(rbf(n) * rbf(1.f + sc)) + sh); }

__global__ void __launch_bounds__(NTHR, 2) block_decode(Args a) {
  extern __shared__ __attribute__((aligned(16))) unsigned char smem[];
  const int tid = threadIdx.x, lane = tid & 63, wave = __builtin_amdgcn_readfirstlane(tid >> 6);
  const int b = blockIdx.x, x = b & 7, j = b >> 3;
  const u64 t0 = rt();
  const unsigned lb = (unsigned)(size_t)(LAS unsigned char*)smem;
  const rsrc_t ws = mkrsrc(a.ws, WS_END);
  const int kq = j >> 3, cb = x + 8 * (j & 7);
  const int gq = j >> 2;
  const bool mlp = (x & 1) == 0;
  const int og = (x & 6) + (j >> 4), oc0 = 32 * (j & 15);
  const bool has_gu = mlp && 4 * gq + (x >> 1) < 28;
  const int up = 4 * (4 * gq + (x >> 1)) + (j & 3);
  const int fc0 = 64 * x + 8 * (j & 15);

  if (tid < 20) *lds_pl<unsigned>(lb + L_CTL + 4 * tid) = (tid == 1 || tid == 3) ? (unsigned)INF : 0u;
  asm volatile("" :: "s"(a.K), "s"(a.ws));
  wg_barrier();
  int w_a = wave, w_b = wave;
  asm volatile("" : "+s"(w_a));
  if (w_a == 4) {
    const u64 kb = (u64)(a.K + (size_t)j * SK * DH), vb = (u64)(a.V + (size_t)j * SK * DH);
    const v4i ksrd = {(int)kb, (int)(kb >> 32), (int)HEAD_BYTES, 0x00020000}, vsrd = {(int)vb, (int)(vb >> 32), (int)HEAD_BYTES, 0x00020000};
    const unsigned g = lane >> 4, c = lane & 15;
    const unsigned k0 = g * 256 + ((c ^ g) << 4), k1 = g * 256 + ((c ^ (4 + g)) << 4), k2 = g * 256 + ((c ^ (8 + g)) << 4), k3 = g * 256 + ((c ^ (12 + g)) << 4);
    const unsigned vl = lane * 16;
    unsigned sl = lb + L_RING;
    auto fill = [&](v4i srd, int n, unsigned v0, unsigned v1, unsigned v2, unsigned v3) {
      const unsigned o = ((unsigned)n >> 2) * 65536u + ((unsigned)n & 3u) * 8192u;
      dma4(srd, o, sl, v0, v1, v2, v3);
      dma4(srd, o + 4096, sl + 4096, v0, v1, v2, v3);
      dma4(srd, o + 32768, sl + 8192, v0, v1, v2, v3);
      dma4(srd, o + 36864, sl + 12288, v0, v1, v2, v3);
      sl = sl + SLOT == lb + L_RING + NSLOT * SLOT ? lb + L_RING : sl + SLOT;
    };
    volatile LAS u64* FT = lds_at<u64>(lb + L_FT);
    int pub = 0;
    unsigned total = (unsigned)INF;
    auto publish = [&](int n) { pub = n > pub ? n : pub; *FT = (u64)(unsigned)pub | ((u64)total << 32); };
    auto wait_free = [&](int i) {
      const int p = i - NSLOT;
      volatile LAS u64* dn = lds_at<u64>(lb + L_DONE + 8 * (p & 1));
      const unsigned need = (unsigned)(p >> 1) + 1u;
      auto freed = [&]() { const u64 d = *dn; return (unsigned)d >= need && (unsigned)(d >> 32) >= need; };
      if (!freed()) {
        VM0(); publish(i);
        for (unsigned sp = 0; !freed(); ++sp) { __builtin_amdgcn_s_sleep(1); if (EXPIRED(sp)) break; }
      }
      MEMFENCE();
    };
    for (unsigned sp = 0; *lds_at<unsigned>(lb + L_ISSUED) < 5u && sp < 4096u; ++sp) {}
    unsigned tn = 1;
    auto take = [&]() {
      u64 tv;
      for (unsigned sp = 0; (unsigned)((tv = *lds_at<u64>(lb + L_TK)) >> 32) < tn; ++sp) if (EXPIRED(sp)) { tv = EMPTY; break; }
      ++tn;
      return (unsigned)__builtin_amdgcn_readfirstlane((unsigned)tv);
    };
    int i = 0, nt = 0, next = POOL0 + x * NP, left = NP;
    bool asked = true;
    for (;; ++i) {
      fill(ksrd, next, k0, k1, k2, k3);
      ++next; --left;
      if (i >= BURST) publish(i - 1);
      if (i == NP + SO - 5) *lds_at<unsigned>(lb + L_TKC) = tn;
      if (left == 0) {
        if (i == NP - 1) { next = x * SO; left = SO; }
        else {
          const unsigned t = asked ? take() : EMPTY;
          if (t >= (unsigned)NKT) break;
          left = 4; next = KD0 + 4 * (int)t;
          *lds_at<unsigned>(lb + L_LIST + 4 * nt) = t; ++nt;
          asked = i + 1 - NP + left + 4 <= CAP;
          if (asked) *lds_at<unsigned>(lb + L_TKC) = tn;
        }
      }
      if (i + 1 >= NSLOT) wait_free(i + 1);
      if (i + 1 >= BURST) vmwait<16>();
    }
    const int nk = i + 1, vown = 2 * nk - NP;
    *lds_at<unsigned>(lb + L_NK) = (unsigned)nk;
    wait_free(i + 1);
    vmwait<16>();
    next = x * SO; left = SO;
    int rt_ = 0;
    for (++i;; ++i) {
      fill(vsrd, next, vl, vl, vl, vl);
      ++next; --left;
      publish(i - 1);
      if (i == vown - 3) *lds_at<unsigned>(lb + L_TKC) = tn;
      if (left == 0) {
        if (rt_ < nt) { const unsigned t = __builtin_amdgcn_readfirstlane(*lds_at<unsigned>(lb + L_LIST + 4 * rt_)); ++rt_; left = 4; next = KD0 + 4 * (int)t; }
        else {
          const unsigned t = take();
          if (t >= (unsigned)NPT) break;
          *lds_at<unsigned>(lb + L_TKC) = tn;
          left = 2; next = POOL0 + 2 * (int)t;
        }
      }
      wait_free(i + 1);
      vmwait<16>();
    }
    total = (unsigned)i + 1u;
    publish(i - 1);
    vmwait<16>(); publish(i);
    VM0(); publish(i + 1);
    return;
  }

  asm volatile("" : "+s"(w_b));
  if (w_b == 5) {
    __builtin_amdgcn_s_setprio(3);
    const rsrc_t rwm = mkrsrc(a.wm, DM * 3072 * 2), rc = mkrsrc(a.c, DM * 2);
    const int n0 = 512 * (gq & 1) + 64 * x + 16 * (j & 3);
    const int ar = 128 * kq + 4 * (lane >> 1);
    v4u w1[4];
#pragma unroll
    for (int i = 0; i < 4; ++i) w1[i] = ld128<NT>(rwm, (unsigned)((ar + i) * 3072 + n0 + 8 * (lane & 1)) * 2);
    const v2u c4 = ld64<0>(rc, ar * 2);
    const v2u ep = ld64<SC1>(ws, WS_EPOCH);
    const int mr = 64 * gq + 8 * (lane >> 3), mc = 64 * (x + 8 * (j & 3));
    const v4u cv = ld128<0>(rc, mr * 2);
    MEMFENCE();
    if (lane == 0) __hip_atomic_fetch_add(lds_pl<unsigned>(lb + L_ISSUED), 1u, __ATOMIC_RELAXED, __HIP_MEMORY_SCOPE_WORKGROUP);
    st128(ws, (lane < 8 && !mlp) ? WS_YP + (og * 512 + oc0) * 4 + lane * 16 : OOB, v4u{EMPTY, EMPTY, EMPTY, EMPTY});
    st128(ws, has_gu ? WS_DP + up * 2048 + lane * 16 : OOB, v4u{EMPTY, EMPTY, EMPTY, EMPTY});
    st128(ws, has_gu ? WS_DP + up * 2048 + 1024 + lane * 16 : OOB, v4u{EMPTY, EMPTY, EMPTY, EMPTY});
    st128(ws, lane < 33 ? WS_PO + b * REC + lane * 16 : OOB, v4u{EMPTY, EMPTY, EMPTY, EMPTY});
    st32(ws, (lane < 2 && x == 0) ? WS_CTR + j * 256 + lane * 128 : OOB, 0u);
    const u64 e1 = (((u64)ep[1] << 32) | ep[0]) + 1ull;
    const unsigned tlo = (unsigned)e1 & 0xFu, thi = (unsigned)(e1 >> 4);
    {
      float s8[8] = {0.f, 0.f, 0.f, 0.f, 0.f, 0.f, 0.f, 0.f};
      fma8(s8, silu_bf(lo16(c4[0])), w1[0]); fma8(s8, silu_bf(hi16(c4[0])), w1[1]);
      fma8(s8, silu_bf(lo16(c4[1])), w1[2]); fma8(s8, silu_bf(hi16(c4[1])), w1[3]);
      float u0, u1;
      rows_reduce8(s8, u0, u1);
      u0 += dpp<ROR8>(u0); u1 += dpp<ROR8>(u1); u0 += dpp<ROR4>(u0); u1 += dpp<ROR4>(u1); u0 += dpp<XOR2>(u0); u1 += dpp<XOR2>(u1);
      const int col = n0 + 8 * (lane & 1) + 2 * (lane >> 4);
      st128(ws, (lane & 15) < 2 ? WS_P1 + (kq * 1024 + col) * 8 : OOB, gr2(u0, u1, tlo, thi));
      *lds_at<unsigned>(lb + L_AUXPUB) = 1u;
    }
    __builtin_amdgcn_s_setprio(0);
    auto stream_at = [&](unsigned fillno) {
      for (unsigned sp = 0; *lds_at<unsigned>(lb + L_FT) < fillno; ++sp) { __builtin_amdgcn_s_sleep(8); if ((sp & 63u) == 63u && rt() - t0 > TLIMIT) break; }
      MEMFENCE();
    };
    stream_at(NSLOT + 4);
    {
      v4u w2[8];
#pragma unroll
      for (int i = 0; i < 8; ++i) w2[i] = ld128<NT>(rwm, (unsigned)((mr + i) * 3072 + 1024 + mc + 8 * (lane & 7)) * 2);
      float s8[8] = {0.f, 0.f, 0.f, 0.f, 0.f, 0.f, 0.f, 0.f};
#pragma unroll
      for (int e = 0; e < 4; ++e) { fma8(s8, silu_bf(lo16(cv[e])), w2[2 * e]); fma8(s8, silu_bf(hi16(cv[e])), w2[2 * e + 1]); }
      float u0, u1;
      rows_reduce8(s8, u0, u1);
      u0 += dpp<ROR8>(u0); u1 += dpp<ROR8>(u1);
      st128(ws, (lane & 8) == 0 ? WS_M2P + (gq * 2048 + mc + 8 * (lane & 7) + 2 * (lane >> 4)) * 8 : OOB, gr2(u0, u1, tlo, thi));
    }
    stream_at(NSLOT + 16);
    {
      v4u m;
      for (;;) {
        MEMFENCE();
        m = ld128<SC1>(ws, WS_M2P + (((lane >> 2) & 7) * 2048 + 8 * b + 2 * (lane & 3)) * 8);
        if (__all(gr_bad(m, tlo, thi) == 0u) || rt() - t0 > TLIMIT) break;
        __builtin_amdgcn_s_sleep(32);
      }
      float u0 = gr_val(m[0]), u1 = gr_val(m[2]);
      u0 = sw16_add(u0, u0); u1 = sw16_add(u1, u1);
      u0 += dpp<ROR8>(u0); u1 += dpp<ROR8>(u1); u0 += dpp<ROR4>(u0); u1 += dpp<ROR4>(u1);
      VM0();
      st128(ws, lane < 4 ? WS_M2 + b * 64 + lane * 16 : OOB, gr2(rbf(u0), rbf(u1), tlo, thi));
    }
    stream_at(NSLOT + 28);
#pragma unroll 1
    for (int u = 0; u < 16; ++u) {
      v4u m;
      for (;;) {
        MEMFENCE();
        m = ld128<SC1>(ws, WS_M2 + u * 1024 + lane * 16);
        if (__all(gr_bad(m, tlo, thi) == 0u) || rt() - t0 > TLIMIT) break;
        __builtin_amdgcn_s_sleep(32);
      }
      *lds_pl<unsigned>(lb + L_M2 + u * 256 + lane * 4) = __builtin_amdgcn_perm(m[2], m[0], 0x07060302u);
      __builtin_amdgcn_s_sleep(16);
    }
    LGKM0();
    *lds_at<unsigned>(lb + L_M2RDY) = 1u;
    GAS unsigned* ctr = (GAS unsigned*)(a.ws + WS_CTR + j * 256);
    unsigned n = 1;
    auto draw = [&](int which) {
      unsigned v = 0;
      if (lane == 0) v = __hip_atomic_fetch_add(ctr + 32 * which, 1u, __ATOMIC_RELAXED, __HIP_MEMORY_SCOPE_AGENT);
      v = __builtin_amdgcn_readfirstlane(v);
      *lds_at<u64>(lb + L_TK) = ((u64)n << 32) | v;
      ++n;
      return v;
    };
    for (;;) {
      bool over = false;
      for (unsigned sp = 0; *lds_at<unsigned>(lb + L_TKC) < n; ++sp) {
        if (*lds_at<unsigned>(lb + L_NK) != (unsigned)INF || EXPIRED(sp)) { over = true; break; }
        __builtin_amdgcn_s_sleep(2);
      }
      if (over || *lds_at<unsigned>(lb + L_NK) != (unsigned)INF || draw(0) >= (unsigned)NKT) break;
    }
    lds_wait_ge(lb + L_KCNT, 5u, t0);
    __builtin_amdgcn_s_sleep(127);
    {
      v2u mx;
      for (;;) {
        MEMFENCE();
        mx = ld64<SC1>(ws, WS_MX + j * 64 + (lane & 7) * 8);
        if (__all((((mx[0] ^ tlo) & 0xFu) | (mx[1] ^ thi)) == 0u) || rt() - t0 > TLIMIT) break;
        __builtin_amdgcn_s_sleep(32);
      }
      *lds_at<float>(lb + L_MH) = row_max(gr_val(mx[0]));
      LGKM0();
    }
    {
      unsigned dq = 0, pend = 0;
      v4u sc = {0u, 0u, 0u, 0u};
      auto flush = [&]() {
        if ((unsigned)lane < 8u * pend) *lds_pl<v4u>(lb + L_SC + dq * 128 + lane * 16) = sc;
        dq += pend; pend = 0;
        LGKM0();
        *lds_at<unsigned>(lb + L_SCR) = dq;
      };
      for (;;) {
        if (__builtin_amdgcn_readfirstlane(*lds_at<unsigned>(lb + L_TKC)) < n) flush();
        for (unsigned sp = 0; *lds_at<unsigned>(lb + L_TKC) < n; ++sp) { __builtin_amdgcn_s_sleep(2); if (EXPIRED(sp)) break; }
        const unsigned v = draw(1);
        flush();
        if (v >= (unsigned)NPT || rt() - t0 > TLIMIT) break;
        pend = 2u;
        sc = ld128<SC1 | NT>(ws, lane < 16 ? WS_SG + (j * 8 * NP + 2 * v) * 128 + lane * 16 : OOB);
        MEMFENCE();
      }
    }
    return;
  }

  const int g = lane >> 4, c = lane & 15;
  const rsrc_t rx = mkrsrc(a.x, DM * 2), rwq = mkrsrc(a.wq, DM * 4096 * 2);
  const rsrc_t rwd = mkrsrc(a.wd, DF * DM * 2);
  const int r0 = 128 * kq + 32 * wave;
  v4u wqv[4];
#pragma unroll
  for (int i = 0; i < 4; ++i) wqv[i] = ld128<NT>(rwq, (unsigned)((r0 + 4 * (lane >> 3) + i) * 4096 + 64 * cb + 8 * (lane & 7)) * 2);
  const v2u ep = ld64<SC1>(ws, WS_EPOCH);
  const v4u x8 = ld128<0>(rx, lane * 16);
  const unsigned x2 = ld32<0>(rx, tid * 4);
  const unsigned xh = ld32<0>(rx, (r0 + 2 * c) * 2);
  MEMFENCE();
  if (lane == 0) __hip_atomic_fetch_add(lds_pl<unsigned>(lb + L_ISSUED), 1u, __ATOMIC_RELAXED, __HIP_MEMORY_SCOPE_WORKGROUP);
  float mb, sd;
  {
    float xs[8];
#pragma unroll
    for (int e = 0; e < 4; ++e) { xs[2 * e] = lo16(x8[e]); xs[2 * e + 1] = hi16(x8[e]); }
    float s = 0.f;
#pragma unroll
    for (int e = 0; e < 8; ++e) s += xs[e];
    const float mean = wave_sum(s) * (1.f / 512.f);
    float v = 0.f;
#pragma unroll
    for (int e = 0; e < 8; ++e) { const float d = xs[e] - mean; v = fmaf(d, d, v); }
    ln_round(mean, wave_sum(v) * (1.f / 512.f), mb, sd);
  }
  float nrow = rbf((lane >= 32 ? hi16(xh) : lo16(xh)) - mb) / sd;
  kpair(wqv[0], wqv[1]); kpair(wqv[2], wqv[3]);
  PIN4(wqv); asm volatile("" : "+v"(nrow));
  const u64 epoch1 = (((u64)ep[1] << 32) | ep[0]) + 1ull;
  const unsigned tlo = (unsigned)epoch1 & 0xFu, thi = (unsigned)(epoch1 >> 4);
  lds_wait_ge(lb + L_AUXPUB, 1u, t0);
  {
    const unsigned o1 = WS_P1 + (g * 1024 + r0 + 2 * c) * 8;
    v4u gs = {0u, 0u, 0u, 0u}, gc = gs;
    bool bad = true;
    __builtin_amdgcn_s_sleep(32);
    for (;;) {
      MEMFENCE();
      const v4u ns_ = ld128<SC1>(ws, bad ? o1 : OOB), nc_ = ld128<SC1>(ws, bad ? o1 + 4096 : OOB);
      const bool tk = bad & ((gr_bad(ns_, tlo, thi) | gr_bad(nc_, tlo, thi)) == 0u);
      gs = sel4(tk, ns_, gs); gc = sel4(tk, nc_, gc); bad = bad & !tk;
      if (!__any(bad) || rt() - t0 > TLIMIT) break;
    }
    const unsigned hp = pack_halves(mod_round(nrow, rbf(gr_sum(gc)), rbf(gr_sum(gs))));
    float acc[8] = {0.f, 0.f, 0.f, 0.f, 0.f, 0.f, 0.f, 0.f};
    dot8(acc, __builtin_amdgcn_ds_bpermute((lane >> 3) * 8, hp), wqv[0], wqv[1]);
    dot8(acc, __builtin_amdgcn_ds_bpermute((lane >> 3) * 8 + 4, hp), wqv[2], wqv[3]);
    float u0, u1;
    rows_reduce8(acc, u0, u1);
    u0 += dpp<ROR8>(u0); u1 += dpp<ROR8>(u1);
    if ((lane & 8) == 0) *lds_pl<f32x2>(lb + L_RED + wave * 256 + (8 * (lane & 7) + 2 * g) * 4) = f32x2{u0, u1};
    LGKM0();
    unsigned old = 0;
    if (lane == 0) old = __hip_atomic_fetch_add(lds_pl<unsigned>(lb + L_QCNT), 1u, __ATOMIC_RELAXED, __HIP_MEMORY_SCOPE_WORKGROUP);
    old = __builtin_amdgcn_readfirstlane(old);
    MEMFENCE();
    if (old == 3u) {
      const unsigned ra = lb + L_RED + lane * 4;
      const float qp = (*lds_pl<float>(ra) + *lds_pl<float>(ra + 256)) + (*lds_pl<float>(ra + 512) + *lds_pl<float>(ra + 768));
      st64(ws, WS_QS + (kq * 4096 + 64 * cb + lane) * 8, v2u{gr_lo(qp, tlo), thi});
    }
  }
  const int r8 = 8 * (tid >> 2);
  v4u wv[8];
  unsigned wdv[16];
#pragma unroll
  for (int i = 0; i < 8; ++i) { wv[i] = v4u{0u, 0u, 0u, 0u}; wdv[2 * i] = 0u; wdv[2 * i + 1] = 0u; }
  const GAS v4u* wp = (const GAS v4u*)(mlp ? ((tid & 2) ? a.wu : a.wg) + (r8 * DF + 16 * (has_gu ? up : 0) + 8 * (tid & 1)) : a.wo + ((512 * og + r8) * DM + oc0 + 8 * (tid & 3)));
  const int wstride = mlp ? DF / 8 : DM / 8;
  const unsigned wdo = has_gu ? (unsigned)(16 * up * DM + 2 * tid) * 2 : OOB;

  const unsigned key = 4 * (c & 3) + (c >> 2);
  unsigned ka[4], msk[4];
#pragma unroll
  for (int st = 0; st < 4; ++st) {
    ka[st] = lb + L_RING + (wave & 1) * 8192 + key * 256 + (((st + 4 * g) ^ key) << 4);
    msk[st] = ((c & 7) >> 1) == st ? ((c & 1) ? 0xffff0000u : 0x0000ffffu) : 0u;
  }
  const unsigned va = lb + L_RING + (wave & 1) * 8192 + lane * 16;
  const bool is_hi = c < 8;
  volatile LAS unsigned* DN = lds_at<unsigned>(lb + L_DONE + 4 * wave);
  unsigned so = (wave >> 1) * SLOT;
  v4u sv[8], sw[8], ud[8], qf[4];
#pragma unroll
  for (int i = 0; i < 8; ++i) { sv[i] = v4u{0u, 0u, 0u, 0u}; sw[i] = sv[i]; ud[i] = sv[i]; }
  int ns = 0;
  auto release = [&](v4u (&d)[8], int i) {
    LGKM0();
    *DN = (unsigned)(i >> 1) + 1u;
    asm volatile("" : "+v"(d[0]), "+v"(d[1]), "+v"(d[2]), "+v"(d[3]), "+v"(d[4]), "+v"(d[5]), "+v"(d[6]), "+v"(d[7]) :: "memory");
    so = (so + 2 * SLOT) & (NSLOT * SLOT - 1);
  };
  auto read_k = [&](v4u (&d)[8], int i) {
#pragma unroll
    for (int st = 0; st < 4; ++st) { d[st] = *lds_pl<v4u>(ka[st] + so); d[4 + st] = *lds_pl<v4u>(ka[st] + so + 4096); }
    release(d, i);
  };
  int total = INF, nk = INF;
  auto wait_full = [&](int i, bool in_k) {
    for (unsigned sp = 0;; ++sp) {
      const v4u ct = *(volatile LAS v4u*)(size_t)(lb + L_FT);
      total = __builtin_amdgcn_readfirstlane((int)ct[1]);
      nk = __builtin_amdgcn_readfirstlane((int)ct[3]);
      if (EXPIRED(sp)) { total = 0; nk = nk < i ? nk : i; break; }
      if (__builtin_amdgcn_readfirstlane((int)ct[0]) > i || (in_k && nk <= i)) break;
      if (total == INF) __builtin_amdgcn_s_sleep(1);
    }
    MEMFENCE();
  };
  float mraw = -INFINITY;
  auto scores = [&](const v4u (&d)[8], int i) {
    f32x4 sa = {0.f, 0.f, 0.f, 0.f}, sb = {0.f, 0.f, 0.f, 0.f};
#pragma unroll
    for (int st = 0; st < 4; ++st) { sa = mfma(d[st], qf[st], sa); sb = mfma(d[4 + st], qf[st], sb); }
    mraw = fmaxf(fmaxf(fmaxf(mraw, sa[0]), fmaxf(sa[1], sa[2])), fmaxf(fmaxf(sa[3], sb[0]), fmaxf(fmaxf(sb[1], sb[2]), sb[3])));
    v4u pk = {cvtpk(sa[0], sa[1]), cvtpk(sa[2], sa[3]), cvtpk(sb[0], sb[1]), cvtpk(sb[2], sb[3])};
#pragma unroll
    for (int u = 0; u < 4; ++u) pk[u] = cvtpk(lo16(pk[u]) * SCALE, hi16(pk[u]) * SCALE);
    const unsigned so_ = (wave & 1) * 64 + g * 16;
    const unsigned dst = i < NP ? L_SH + i * 128 : L_SC + (i - NP) * 128;
    if (c == 0) *lds_pl<v4u>(lb + dst + so_) = pk;
  };
  auto stash = [&]() {
    const int full = __builtin_amdgcn_readfirstlane((int)*lds_at<unsigned>(lb + L_FT)), i0 = wave >> 1;
    MEMFENCE();
    if (ns == 0 && full > i0) { read_k(wv, i0); ns = 1; }
    if (ns == 1 && full > i0 + 2) { read_k(sv, i0 + 2); ns = 2; }
    if (ns == 2 && full > i0 + 4) { read_k(sw, i0 + 4); ns = 3; }
    if (ns == 3 && full > i0 + 6) { read_k(ud, i0 + 6); ns = 4; }
  };
  {
    const unsigned o2 = WS_QS + (g * 4096 + 128 * j + 32 * wave + 2 * c) * 8;
    v4u gq4 = {0u, 0u, 0u, 0u};
    bool bad = true;
    stash();
    {
      const v4u nq = ld128<SC1>(ws, o2);
      bad = gr_bad(nq, tlo, thi) != 0u;
      gq4 = nq;
    }
    asm volatile("" : "+v"(gq4) :: "memory");
    stash();
    for (unsigned it = 0; __any(bad) && !EXPIRED(it); ++it) {
      MEMFENCE();
      const v4u nq = ld128<SC1>(ws, bad ? o2 : OOB);
      const bool tk = bad & (gr_bad(nq, tlo, thi) == 0u);
      gq4 = sel4(tk, nq, gq4); bad = bad & !tk;
    }
    *lds_pl<unsigned>(lb + L_QSC + (16 * wave + c) * 4) = pack_halves(rbf(gr_sum(gq4)));
    LGKM0();
    if (lane == 0) __hip_atomic_fetch_add(lds_pl<unsigned>(lb + L_QIN), 1u, __ATOMIC_RELAXED, __HIP_MEMORY_SCOPE_WORKGROUP);
    lds_wait_ge(lb + L_QIN, 4u, t0);
#pragma unroll
    for (int st = 0; st < 4; ++st) qf[st] = *lds_pl<v4u>(lb + L_QSC + (st + 4 * g) * 16);
  }
  if (ns >= 1) scores(wv, wave >> 1);
  if (ns >= 2) scores(sv, (wave >> 1) + 2);
  if (ns >= 3) scores(sw, (wave >> 1) + 4);
  if (ns >= 4) scores(ud, (wave >> 1) + 6);
  int i = (wave >> 1) + 2 * ns;
  for (;; i += 2) {
    wait_full(i, true);
    if (nk <= i) break;
    read_k(ud, i);
    scores(ud, i);
  }
  float m2;
  {
    *lds_pl<float>(lb + L_MXW + wave * 4) = rbf(rbf(xrow_max(mraw)) * SCALE);
    LGKM0();
    unsigned old = 0;
    if (lane == 0) old = __hip_atomic_fetch_add(lds_pl<unsigned>(lb + L_KCNT), 1u, __ATOMIC_RELAXED, __HIP_MEMORY_SCOPE_WORKGROUP);
    old = __builtin_amdgcn_readfirstlane(old);
    lds_wait_ge(lb + L_KCNT, 4u, t0);
    const f32x4 mw = *lds_pl<f32x4>(lb + L_MXW);
    const float mg = fmaxf(fmaxf(mw[0], mw[1]), fmaxf(mw[2], mw[3]));
    if (old == 3u) {
#pragma unroll
      for (int u = 0; u < (NP + 7) / 8; ++u)
        __builtin_amdgcn_raw_buffer_store_b128(*lds_pl<v4u>(lb + L_SH + u * 1024 + lane * 16), ws,
                                               u * 1024 + lane * 16 < NP * 128 ? WS_SG + b * NP * 128 + u * 1024 + lane * 16 : OOB, 0, SC1 | NT);
      VM0();
      st64(ws, lane == 0 ? WS_MX + b * 8 : OOB, v2u{gr_lo(mg, tlo), thi});
      *lds_at<unsigned>(lb + L_KCNT) = 5u;
    }
    m2 = mg * LOG2E;
  }
  float lsum = 0.f;
  f32x4 acc = {0.f, 0.f, 0.f, 0.f};
  {
    int wk = (mlp && !has_gu) ? 11 : 0;
    bool done = false, own = true;
    for (;; i += 2) {
      int e = i - nk;
      if (!done && e >= nk - NP) {
        e -= nk - NP;
        for (unsigned sp = 0;; ++sp) {
          const v4u ct = *(volatile LAS v4u*)(size_t)(lb + L_FT);
          if (__builtin_amdgcn_readfirstlane((int)ct[2]) > e) break;
          if (__builtin_amdgcn_readfirstlane((int)ct[1]) <= i || EXPIRED(sp)) { done = true; break; }
          __builtin_amdgcn_s_sleep(1);
        }
        MEMFENCE();
        if (own) {
          const float mh = *lds_at<float>(lb + L_MH) * LOG2E, al = __builtin_amdgcn_exp2f(m2 - mh);
          acc *= al; lsum *= al; m2 = mh; own = false;
        }
      }
      if (!done) {
        const v4u sc = *lds_pl<v4u>(lb + L_SC + e * 128 + (wave & 1) * 64 + g * 16);
        v4u pa[8];
#pragma unroll
        for (int u = 0; u < 8; ++u) {
          const float p = __builtin_amdgcn_exp2f(fmaf((u & 1) ? hi16(sc[u >> 1]) : lo16(sc[u >> 1]), LOG2E, -m2));
          lsum += p;
          const float ph = hi16(asu(p));
          const float xv = is_hi ? ph : p - ph;
          const unsigned pk = cvtpk(xv, xv);
          pa[u] = v4u{pk & msk[0], pk & msk[1], pk & msk[2], pk & msk[3]};
        }
        asm volatile("" : "+v"(pa[0]), "+v"(pa[1]), "+v"(pa[2]), "+v"(pa[3]), "+v"(pa[4]), "+v"(pa[5]), "+v"(pa[6]), "+v"(pa[7]));
        wait_full(i, false);
#pragma unroll
        for (int u = 0; u < 8; ++u) ud[u] = *lds_pl<v4u>(va + so + u * 1024);
        release(ud, i);
#pragma unroll
        for (int u = 0; u < 8; ++u) acc = mfma(pa[u], ud[u], acc);
        done = i + 2 >= total && !own;
      }
      MEMFENCE();
      if (wk <= 10 && (done || rt() - t0 >= (u64)(TW + TD * wk + (wk == 10 ? 400 : 0)))) {
#pragma unroll
        for (int u = 0; u < 8; ++u) if (wk == u) wv[u] = __builtin_nontemporal_load(wp + u * wstride);
#pragma unroll
        for (int v = 0; v < 2; ++v) if (wk == 8 + v) {
#pragma unroll
          for (int u = 8 * v; u < 8 * v + 8; ++u) wdv[u] = ld32<NT>(rwd, wdo + u * (DM * 2));
        }
        if (wk == 10) {
#pragma unroll
          for (int u = 0; u < 8; u += 2) kpair(wv[u], wv[u + 1]);
#pragma unroll
          for (int u = 0; u < 16; u += 2) {
            const unsigned lo = __builtin_amdgcn_perm(wdv[u + 1], wdv[u], 0x05040100u), hi = __builtin_amdgcn_perm(wdv[u + 1], wdv[u], 0x07060302u);
            wdv[u] = lo; wdv[u + 1] = hi;
          }
        }
        ++wk;
      }
      MEMFENCE();
      if (done && wk > 10) break;
    }
  }
  {
    f32x4 o;
#pragma unroll
    for (int r = 0; r < 4; ++r) o[r] = sw32_add(acc[r], acc[r]);
    float l = sw16_add(lsum, lsum); l = sw32_add(l, l);
    *lds_pl<f32x4>(lb + L_MRG + wave * 512 + (8 * c + 4 * (g & 1)) * 4) = o;
    *lds_pl<float>(lb + L_LW + wave * 4) = l;
    wg_barrier();
    if (wave == 0) {
      const unsigned ra = lb + L_MRG + (lane & 31) * 16;
      const f32x4 lw = *lds_pl<f32x4>(lb + L_LW);
      f32x4 O = (*lds_pl<f32x4>(ra) + *lds_pl<f32x4>(ra + 512)) + (*lds_pl<f32x4>(ra + 1024) + *lds_pl<f32x4>(ra + 1536));
      if (lane >= 32) O = f32x4{(lw[0] + lw[1]) + (lw[2] + lw[3]), 0.f, 0.f, 0.f};
      st128(ws, lane < 33 ? WS_PO + b * REC + lane * 16 : OOB, noempty4(O));
    }
  }
  if (!mlp) {
    const unsigned hb = WS_PO + (4 * og + wave) * 8 * REC;
    v2u po[8];
    unsigned ls = EMPTY;
#pragma unroll
    for (int s = 0; s < 8; ++s) po[s] = v2u{0u, 0u};
    unsigned miss = 0x1FFu;
    for (unsigned it = 0;; ++it) {
      MEMFENCE();
      if (miss & 0x100u) ls = ld32<SC1>(ws, (lane & 15) < 8 ? hb + (lane & 15) * REC + 512 : OOB);
#pragma unroll
      for (int s = 0; s < 8; ++s) if (miss & (1u << s)) po[s] = ld64<SC1>(ws, hb + s * REC + lane * 8);
      if ((miss & 0x100u) && __all(ls != EMPTY)) miss &= ~0x100u;
#pragma unroll
      for (int s = 0; s < 8; ++s) if ((miss & (1u << s)) && __all(mx2(0u, po[s]) != EMPTY)) miss &= ~(1u << s);
      if (miss == 0u || EXPIRED(it)) break;
    }
    const float inv = __builtin_amdgcn_rcpf(row_sum(asf(ls)));
    const unsigned op = cvtpk((((asf(po[0][0]) + asf(po[1][0])) + (asf(po[2][0]) + asf(po[3][0]))) + ((asf(po[4][0]) + asf(po[5][0])) + (asf(po[6][0]) + asf(po[7][0])))) * inv,
                              (((asf(po[0][1]) + asf(po[1][1])) + (asf(po[2][1]) + asf(po[3][1]))) + ((asf(po[4][1]) + asf(po[5][1])) + (asf(po[6][1]) + asf(po[7][1])))) * inv);
    float y[8] = {0.f, 0.f, 0.f, 0.f, 0.f, 0.f, 0.f, 0.f};
    dot8(y, dppu<0x00>(op), wv[0], wv[1]); dot8(y, dppu<0x55>(op), wv[2], wv[3]);
    dot8(y, dppu<0xAA>(op), wv[4], wv[5]); dot8(y, dppu<0xFF>(op), wv[6], wv[7]);
    float u0, u1;
    rows_reduce8(y, u0, u1);
    u0 += dpp<ROR8>(u0); u1 += dpp<ROR8>(u1); u0 += dpp<ROR4>(u0); u1 += dpp<ROR4>(u1);
    *lds_pl<f32x2>(lb + L_RED3 + wave * 128 + (8 * (c & 3) + 2 * g) * 4) = f32x2{u0, u1};
    wg_barrier();
    if (wave == 0) {
      const unsigned ra = lb + L_RED3 + (lane & 7) * 16;
      const f32x4 t = (*lds_pl<f32x4>(ra) + *lds_pl<f32x4>(ra + 128)) + (*lds_pl<f32x4>(ra + 256) + *lds_pl<f32x4>(ra + 384));
      st128(ws, lane < 8 ? WS_YP + (og * 512 + oc0) * 4 + lane * 16 : OOB, noempty4(t));
    }
    return;
  }
  if (!has_gu) return;
  lds_wait_ge(lb + L_M2RDY, 1u, t0);
  const int fcol = fc0 + 4 * (lane & 1) + g;
  float gbf = lo16(*lds_pl<u16>(lb + L_M2 + 3072 + fcol * 2));
  {
    const unsigned gaw = *lds_pl<unsigned>(lb + L_M2 + tid * 4), sbw = *lds_pl<unsigned>(lb + L_M2 + 1024 + tid * 4), cbw = *lds_pl<unsigned>(lb + L_M2 + 2048 + tid * 4);
    float ga0 = lo16(gaw), ga1 = hi16(gaw), sb0 = lo16(sbw), sb1 = hi16(sbw), t0c = rbf(1.f + lo16(cbw)), t1c = rbf(1.f + hi16(cbw)), xa = lo16(x2), xb = hi16(x2);
    asm volatile("" : "+v"(ga0), "+v"(ga1), "+v"(sb0), "+v"(sb1), "+v"(t0c), "+v"(t1c), "+v"(xa), "+v"(xb), "+v"(gbf));
    v2u yp[8];
#pragma unroll
    for (int s = 0; s < 8; ++s) yp[s] = v2u{0u, 0u};
    unsigned miss = 0xFFu;
    for (unsigned it = 0;; ++it) {
      MEMFENCE();
#pragma unroll
      for (int s = 0; s < 8; ++s) if (miss & (1u << s)) yp[s] = ld64<SC1>(ws, WS_YP + s * 2048 + tid * 8);
#pragma unroll
      for (int s = 0; s < 8; ++s) if ((miss & (1u << s)) && __all(mx2(0u, yp[s]) != EMPTY)) miss &= ~(1u << s);
      if (miss == 0u || EXPIRED(it)) break;
    }
    const unsigned atp = cvtpk(((asf(yp[0][0]) + asf(yp[1][0])) + (asf(yp[2][0]) + asf(yp[3][0]))) + ((asf(yp[4][0]) + asf(yp[5][0])) + (asf(yp[6][0]) + asf(yp[7][0]))),
                               ((asf(yp[0][1]) + asf(yp[1][1])) + (asf(yp[2][1]) + asf(yp[3][1]))) + ((asf(yp[4][1]) + asf(yp[5][1])) + (asf(yp[6][1]) + asf(yp[7][1]))));
    const unsigned gap = cvtpk(ga0 * lo16(atp), ga1 * hi16(atp));
    const unsigned x1p = cvtpk(xa + lo16(gap), xb + hi16(gap));
    const float x1a = lo16(x1p), x1b = hi16(x1p);
    const float sh = asf(__builtin_amdgcn_readfirstlane(asu(x1a)));
    const float da = x1a - sh, db = x1b - sh;
    const float s1 = wave_sum(da + db), s2 = wave_sum(fmaf(da, da, db * db));
    *lds_pl<f32x2>(lb + L_ST + wave * 8) = f32x2{fmaf(s1, 1.f / 128.f, sh), fmaf(-s1 * (1.f / 128.f), s1, s2)};
    *lds_pl<f32x2>(lb + L_X1 + tid * 8) = f32x2{x1a, x1b};
    wg_barrier();
    {
      const f32x4 sA = *lds_pl<f32x4>(lb + L_ST), sB = *lds_pl<f32x4>(lb + L_ST + 16);
      const float mean = ((sA[0] + sA[2]) + (sB[0] + sB[2])) * 0.25f;
      const float d0 = sA[0] - mean, d1 = sA[2] - mean, d2 = sB[0] - mean, d3 = sB[2] - mean;
      const float var = fmaxf(((sA[1] + sA[3]) + (sB[1] + sB[3])) + 128.f * ((d0 * d0 + d1 * d1) + (d2 * d2 + d3 * d3)), 0.f) * (1.f / 512.f);
      float mb2, sd2;
      ln_round(mean, var, mb2, sd2);
      const float rs = __builtin_amdgcn_rcpf(sd2);
      const unsigned e0 = cvtpk(x1a - mb2, x1b - mb2), e1 = cvtpk(lo16(e0) * rs, hi16(e0) * rs), e2 = cvtpk(lo16(e1) * t0c, hi16(e1) * t1c);
      const unsigned hq = cvtpk(lo16(e2) + sb0, hi16(e2) + sb1);
      float y[8] = {0.f, 0.f, 0.f, 0.f, 0.f, 0.f, 0.f, 0.f};
      dot8(y, dppu<0x00>(hq), wv[0], wv[1]); dot8(y, dppu<0x55>(hq), wv[2], wv[3]);
      dot8(y, dppu<0xAA>(hq), wv[4], wv[5]); dot8(y, dppu<0xFF>(hq), wv[6], wv[7]);
      float u0, u1;
      rows_reduce8(y, u0, u1);
      u0 += dpp<ROR8>(u0); u1 += dpp<ROR8>(u1); u0 += dpp<ROR4>(u0); u1 += dpp<ROR4>(u1);
      *lds_pl<f32x2>(lb + L_RED3 + wave * 128 + (8 * (c & 3) + 2 * g) * 4) = f32x2{u0, u1};
      wg_barrier();
      const unsigned ra = lb + L_RED3 + c * 4;
      const float gs = rbf((*lds_pl<float>(ra) + *lds_pl<float>(ra + 128)) + (*lds_pl<float>(ra + 256) + *lds_pl<float>(ra + 384)));
      const float us = rbf((*lds_pl<float>(ra + 64) + *lds_pl<float>(ra + 192)) + (*lds_pl<float>(ra + 320) + *lds_pl<float>(ra + 448)));
      const float z = 0.7978845608028654f * fmaf(0.044715f * gs * gs, gs, gs);
      const float th = fmaf(-2.f, __builtin_amdgcn_rcpf(1.f + __expf(2.f * z)), 1.f);
      const float h2 = rbf(rbf(0.5f * gs * (1.f + th)) * us);
      const unsigned hp = __builtin_amdgcn_perm(asu(dpp<XOR1>(h2)), asu(h2), 0x07060302u);
      float d0a = 0.f, d1a = 0.f;
#pragma unroll
      for (int e = 0; e < 16; e += 2) {
        const unsigned he = __builtin_amdgcn_readlane(hp, e);
        d0a = dot2(wdv[e], he, d0a); d1a = dot2(wdv[e + 1], he, d1a);
      }
      st64(ws, WS_DP + up * 2048 + tid * 8, v2u{noempty(asu(d0a)), noempty(asu(d1a))});
      st64(ws, (b | tid) == 0 ? WS_EPOCH : OOB, v2u{(unsigned)epoch1, (unsigned)(epoch1 >> 32)});
    }
  }
  if (wave != 0 || j >= 16) return;
  {
    const float x1o = *lds_pl<float>(lb + L_X1 + fcol * 4);
    v4u dp[4];
#pragma unroll
    for (int u = 0; u < 4; ++u) dp[u] = v4u{(u < 3 || lane < 2 * (NGU - 96)) ? EMPTY : 0u, 0u, 0u, 0u};
    for (unsigned it = 0;; ++it) {
      MEMFENCE();
      bool ms[4];
      v4u nw[4];
#pragma unroll
      for (int u = 0; u < 4; ++u) ms[u] = mx4(0u, dp[u]) == EMPTY;
      if (!__any(ms[0] | ms[1] | ms[2] | ms[3]) || EXPIRED(it)) break;
#pragma unroll
      for (int u = 0; u < 4; ++u) nw[u] = ld128<SC1>(ws, ms[u] ? WS_DP + (32 * u + (lane >> 1)) * 2048 + fc0 * 4 + (lane & 1) * 16 : OOB);
#pragma unroll
      for (int u = 0; u < 4; ++u) dp[u] = ms[u] ? nw[u] : dp[u];
    }
    float t4[4];
#pragma unroll
    for (int r = 0; r < 4; ++r) t4[r] = (asf(dp[0][r]) + asf(dp[1][r])) + (asf(dp[2][r]) + asf(dp[3][r]));
    float r = sw16_add(sw32_add(t4[0], t4[2]), sw32_add(t4[1], t4[3]));
    r += dpp<ROR8>(r); r += dpp<ROR4>(r); r += dpp<XOR2>(r);
    const float res = x1o + rbf(gbf * rbf(r));
    if (c < 2) a.out[fcol] = (u16)(cvtpk(res, res) & 0xffffu);
  }
}
}

extern "C" void kernel_launch(void* const* d_in, const int* in_sizes, int n_in, void* d_out, int out_size, void* d_ws, size_t ws_size,
                              hipStream_t stream) {
  Args a;
  a.x = (const u16*)d_in[0]; a.K = (const u16*)d_in[1]; a.V = (const u16*)d_in[2]; a.wq = (const u16*)d_in[3]; a.wo = (const u16*)d_in[4];
  a.wg = (const u16*)d_in[6]; a.wu = (const u16*)d_in[7]; a.wd = (const u16*)d_in[8]; a.c = (const u16*)d_in[9]; a.wm = (const u16*)d_in[10];
  a.out = (u16*)d_out; a.ws = (unsigned char*)d_ws;
  (void)hipFuncSetAttribute((const void*)block_decode, hipFuncAttributeMaxDynamicSharedMemorySize, (int)LDS_BYTES);
  hipLaunchKernelGGL(block_decode, dim3(NWG), dim3(NTHR), LDS_BYTES, stream, a);
}
```

```cpp
#include <hip/hip_runtime.h>
#include <hip/hip_bf16.h>

#define GAS __attribute__((address_space(1)))
#define LAS __attribute__((address_space(3)))
typedef unsigned short u16;
typedef unsigned long long u64;
typedef unsigned v4u __attribute__((ext_vector_type(4)));
typedef unsigned v2u __attribute__((ext_vector_type(2)));
typedef int v4i __attribute__((ext_vector_type(4)));
typedef float f32x4 __attribute__((ext_vector_type(4)));
typedef __bf16 bf16x8 __attribute__((ext_vector_type(8)));
typedef __bf16 bf16x2 __attribute__((ext_vector_type(2)));
typedef float f32x2 __attribute__((ext_vector_type(2)));

namespace {
constexpr int DM = 512, SK = 65536, DH = 128, DF = 1792;
constexpr int NWG = 256, NTHR = 384;
constexpr int SLOT = 16384, NSLOT = 8, FH = 1024;
constexpr int SO = 112;
constexpr int NP = 8;
constexpr int POOL0 = 8 * SO, KD0 = POOL0 + 8 * NP, NKT = (FH - KD0) / 4, NPT = 8 * NP / 2;
constexpr int CAP = 144;
static_assert((FH - KD0) % 4 == 0 && SO % 4 == 0 && NP % 2 == 0 && 8 * NP + 16 <= SO && NP <= 8 && 8 * (CAP - 7 - SO) >= FH - KD0, "fill plan");
constexpr int BURST = 3;
constexpr unsigned TW = 10800, TD = 110;
constexpr int INF = 0x7FFFFFFF;
constexpr unsigned HEAD_BYTES = (unsigned)SK * DH * 2;
constexpr unsigned EMPTY = 0xFFFFFFFFu;
constexpr unsigned OOB = 0x7FFF0000u;
constexpr u64 TLIMIT = 5000000ull;
constexpr float LOG2E = 1.4426950408889634f, SCALE = 0.08838834764831845f;
constexpr int NGU = DF / 16, REC = 640;
constexpr unsigned WS_EPOCH = 0, WS_P1 = 4096, WS_QS = WS_P1 + 32768, WS_M2P = WS_QS + 131072, WS_M2 = WS_M2P + 131072, WS_CTR = WS_M2 + 16384,
                   WS_MX = WS_CTR + 8192, WS_YP = WS_MX + 2048, WS_PO = WS_YP + 16384, WS_DP = WS_PO + NWG * REC,
                   WS_SG = WS_DP + NGU * 2048, WS_END = WS_SG + NWG * NP * 128;
constexpr unsigned L_RING = 0, L_CTL = NSLOT * SLOT, L_FT = L_CTL, L_SCR = L_CTL + 8, L_NK = L_CTL + 12, L_DONE = L_CTL + 16, L_QCNT = L_CTL + 32,
                   L_AUXPUB = L_CTL + 36, L_QIN = L_CTL + 40, L_ISSUED = L_CTL + 44, L_M2RDY = L_CTL + 48, L_TKC = L_CTL + 52, L_TK = L_CTL + 56,
                   L_KCNT = L_CTL + 64, L_MH = L_CTL + 68, L_MXW = L_CTL + 80, L_LW = L_CTL + 96, L_LIST = L_CTL + 128, L_RED = L_LIST + 128, L_QSC = L_RED + 1024, L_RED3 = L_QSC + 256,
                   L_ST = L_RED3 + 512, L_M2 = L_ST + 64, L_X1 = L_M2 + 4096, L_MRG = L_X1 + 2048, L_SH = L_X1, L_SC = L_MRG + 2048, L_END = L_SC + CAP * 128;
constexpr unsigned LDS_BYTES = L_END;
static_assert(LDS_BYTES <= 160 * 1024 && LDS_BYTES > 80 * 1024, "one workgroup per CU");

struct Args {
  const u16 *x, *K, *V, *wq, *wo, *wg, *wu, *wd, *c, *wm;
  u16* out;
  unsigned char* ws;
};

__device__ __forceinline__ u64 rt() { return __builtin_amdgcn_s_memrealtime(); }
__device__ __forceinline__ float asf(unsigned u) { return __builtin_bit_cast(float, u); }
__device__ __forceinline__ unsigned asu(float f) { return __builtin_bit_cast(unsigned, f); }
__device__ __forceinline__ float lo16(unsigned u) { return asf(u << 16); }
__device__ __forceinline__ float hi16(unsigned u) { return asf(u & 0xffff0000u); }
__device__ __forceinline__ unsigned cvtpk(float a, float b) {
  const f32x2 v = {a, b};
  return __builtin_bit_cast(unsigned, __builtin_convertvector(v, bf16x2));
}
__device__ __forceinline__ float rbf(float f) { return lo16(cvtpk(f, f)); }
__device__ __forceinline__ unsigned noempty(unsigned v) { return v < EMPTY - 1u ? v : EMPTY - 1u; }
__device__ __forceinline__ float dot2(unsigned a, unsigned b, float c) {
  return __builtin_amdgcn_fdot2_f32_bf16(__builtin_bit_cast(bf16x2, a), __builtin_bit_cast(bf16x2, b), c, false);
}
template <int CTRL> __device__ __forceinline__ float dpp(float v) {
  return asf(__builtin_amdgcn_mov_dpp(asu(v), CTRL, 0xf, 0xf, true));
}
constexpr int ROR8 = 0x128, ROR4 = 0x124, ROR2 = 0x122, ROR1 = 0x121, XOR2 = 0x4E, XOR1 = 0xB1;
template <int CTRL> __device__ __forceinline__ unsigned dppu(unsigned v) { return __builtin_amdgcn_mov_dpp(v, CTRL, 0xf, 0xf, true); }
__device__ __forceinline__ float sw32_add(float a, float b) {
  const auto r = __builtin_amdgcn_permlane32_swap(asu(a), asu(b), false, false);
  return asf(r[0]) + asf(r[1]);
}
__device__ __forceinline__ float sw16_add(float a, float b) {
  const auto r = __builtin_amdgcn_permlane16_swap(asu(a), asu(b), false, false);
  return asf(r[0]) + asf(r[1]);
}
__device__ __forceinline__ float xrow_max(float v) {
  const auto r = __builtin_amdgcn_permlane16_swap(asu(v), asu(v), false, false);
  v = fmaxf(asf(r[0]), asf(r[1]));
  const auto s = __builtin_amdgcn_permlane32_swap(asu(v), asu(v), false, false);
  return fmaxf(asf(s[0]), asf(s[1]));
}
__device__ __forceinline__ unsigned pack_halves(float v) {
  const auto r = __builtin_amdgcn_permlane32_swap(asu(v), asu(v), false, false);
  return __builtin_amdgcn_perm(r[1], r[0], 0x07060302u);
}
__device__ __forceinline__ float row_sum(float v) { v += dpp<ROR8>(v); v += dpp<ROR4>(v); v += dpp<ROR2>(v); v += dpp<ROR1>(v); return v; }
__device__ __forceinline__ float row_max(float v) { v = fmaxf(v, dpp<ROR8>(v)); v = fmaxf(v, dpp<ROR4>(v)); v = fmaxf(v, dpp<ROR2>(v)); return fmaxf(v, dpp<ROR1>(v)); }
__device__ __forceinline__ float wave_sum(float v) { v = row_sum(v); v = sw16_add(v, v); return sw32_add(v, v); }
__device__ __forceinline__ void rows_reduce8(const float (&a)[8], float& u0, float& u1) {
  const float r0 = sw32_add(a[0], a[4]), r1 = sw32_add(a[1], a[5]), r2 = sw32_add(a[2], a[6]), r3 = sw32_add(a[3], a[7]);
  u0 = sw16_add(r0, r2); u1 = sw16_add(r1, r3);
}
__device__ __forceinline__ void fma8(float (&a)[8], float h, v4u w) {
#pragma unroll
  for (int e = 0; e < 4; ++e) { a[2 * e] = fmaf(h, lo16(w[e]), a[2 * e]); a[2 * e + 1] = fmaf(h, hi16(w[e]), a[2 * e + 1]); }
}
__device__ __forceinline__ void kpair(v4u& a, v4u& b) {
  const v4u r0 = a, r1 = b;
  a = v4u{__builtin_amdgcn_perm(r1[0], r0[0], 0x05040100u), __builtin_amdgcn_perm(r1[0], r0[0], 0x07060302u),
          __builtin_amdgcn_perm(r1[1], r0[1], 0x05040100u), __builtin_amdgcn_perm(r1[1], r0[1], 0x07060302u)};
  b = v4u{__builtin_amdgcn_perm(r1[2], r0[2], 0x05040100u), __builtin_amdgcn_perm(r1[2], r0[2], 0x07060302u),
          __builtin_amdgcn_perm(r1[3], r0[3], 0x05040100u), __builtin_amdgcn_perm(r1[3], r0[3], 0x07060302u)};
}
__device__ __forceinline__ void dot8(float (&acc)[8], unsigned hp, v4u a, v4u b) {
#pragma unroll
  for (int e = 0; e < 4; ++e) { acc[e] = dot2(a[e], hp, acc[e]); acc[4 + e] = dot2(b[e], hp, acc[4 + e]); }
}
__device__ __forceinline__ f32x4 mfma(v4u a, v4u b, f32x4 c) {
  return __builtin_amdgcn_mfma_f32_16x16x32_bf16(__builtin_bit_cast(bf16x8, a), __builtin_bit_cast(bf16x8, b), c, 0, 0, 0);
}
typedef __amdgpu_buffer_rsrc_t rsrc_t;
__device__ __forceinline__ rsrc_t mkrsrc(const void* p, unsigned bytes) { return __builtin_amdgcn_make_buffer_rsrc((void*)p, 0, bytes, 0x00020000); }
constexpr int SC1 = 16, NT = 2;
template <int AUX> __device__ __forceinline__ v4u ld128(rsrc_t r, unsigned off) { return __builtin_amdgcn_raw_buffer_load_b128(r, off, 0, AUX); }
template <int AUX> __device__ __forceinline__ v2u ld64(rsrc_t r, unsigned off) { return __builtin_amdgcn_raw_buffer_load_b64(r, off, 0, AUX); }
template <int AUX> __device__ __forceinline__ unsigned ld32(rsrc_t r, unsigned off) { return __builtin_amdgcn_raw_buffer_load_b32(r, off, 0, AUX); }
__device__ __forceinline__ void st128(rsrc_t r, unsigned off, v4u v) { __builtin_amdgcn_raw_buffer_store_b128(v, r, off, 0, SC1); }
__device__ __forceinline__ void st64(rsrc_t r, unsigned off, v2u v) { __builtin_amdgcn_raw_buffer_store_b64(v, r, off, 0, SC1); }
__device__ __forceinline__ void st32(rsrc_t r, unsigned off, unsigned v) { __builtin_amdgcn_raw_buffer_store_b32(v, r, off, 0, SC1); }
__device__ __forceinline__ unsigned umax(unsigned a, unsigned b) { return a > b ? a : b; }
__device__ __forceinline__ unsigned mx4(unsigned m, v4u v) { return umax(umax(umax(m, v[0]), v[1]), umax(v[2], v[3])); }
__device__ __forceinline__ unsigned mx2(unsigned m, v2u v) { return umax(umax(m, v[0]), v[1]); }
__device__ __forceinline__ v4u noempty4(f32x4 v) { return v4u{noempty(asu(v[0])), noempty(asu(v[1])), noempty(asu(v[2])), noempty(asu(v[3]))}; }
struct Tag { unsigned lo, hi; };
template <int LB> __device__ __forceinline__ unsigned tg_hi(Tag t) { return LB ? __builtin_amdgcn_alignbit(t.hi, t.lo, LB) : t.lo; }
template <int LB> __device__ __forceinline__ unsigned gr_lo(float v, Tag t) { return ((asu(v) + ((1u << LB) >> 1)) & (~0u << LB)) | (t.lo & ((1u << LB) - 1u)); }
template <int LB> __device__ __forceinline__ v2u gr1(float a, Tag t) { return v2u{gr_lo<LB>(a, t), tg_hi<LB>(t)}; }
template <int LB> __device__ __forceinline__ v4u gr2(float a, float b, Tag t) { return v4u{gr_lo<LB>(a, t), tg_hi<LB>(t), gr_lo<LB>(b, t), tg_hi<LB>(t)}; }
template <int LB> __device__ __forceinline__ unsigned gr_bad(v4u g, Tag t) {
  return (((g[0] ^ t.lo) | (g[2] ^ t.lo)) & ((1u << LB) - 1u)) | (g[1] ^ tg_hi<LB>(t)) | (g[3] ^ tg_hi<LB>(t));
}
template <int LB> __device__ __forceinline__ float gr_val(unsigned lo) { return asf(lo & (~0u << LB)); }
template <int LB> __device__ __forceinline__ float gr_sum(v4u g) { const float s = sw32_add(gr_val<LB>(g[0]), gr_val<LB>(g[2])); return sw16_add(s, s); }
__device__ __forceinline__ Tag next_tag(v2u ep) { const u64 e = (((u64)ep[1] << 32) | ep[0]) + 1ull; return Tag{(unsigned)e, (unsigned)(e >> 32)}; }
__device__ __forceinline__ v4u sel4(bool t, v4u a, v4u b) { return v4u{t ? a[0] : b[0], 0u, t ? a[2] : b[2], 0u}; }
#define MEMFENCE() asm volatile("" ::: "memory")
#define LGKM0() asm volatile("s_waitcnt lgkmcnt(0)" ::: "memory")
#define VM0() asm volatile("s_waitcnt vmcnt(0)" ::: "memory")
#define PIN4(a) asm volatile("" : "+v"((a)[0]), "+v"((a)[1]), "+v"((a)[2]), "+v"((a)[3]))
template <int N> __device__ __forceinline__ void vmwait() { asm volatile("s_waitcnt vmcnt(%0)" ::"n"(N) : "memory"); }
__device__ __forceinline__ void wg_barrier() { LGKM0(); __builtin_amdgcn_s_barrier(); MEMFENCE(); }
template <typename T> __device__ __forceinline__ volatile LAS T* lds_at(unsigned off) { return (volatile LAS T*)(size_t)off; }
template <typename T> __device__ __forceinline__ LAS T* lds_pl(unsigned off) { return (LAS T*)(size_t)off; }
__device__ __forceinline__ void lds_wait_ge(unsigned addr, unsigned need, u64 t0) {
  for (unsigned sp = 0; *lds_at<unsigned>(addr) < need; ++sp) { __builtin_amdgcn_s_sleep(1); if ((sp & 63u) == 63u && rt() - t0 > TLIMIT) break; }
  MEMFENCE();
}

#define EXPIRED(it) (((it) & 63u) == 63u && rt() - t0 > TLIMIT)

__device__ __forceinline__ void dma4(v4i srd, unsigned soff, unsigned ldsb, unsigned v0, unsigned v1, unsigned v2, unsigned v3) {
  asm volatile("s_mov_b32 m0, %0\n\ts_nop 0\n\t"
               "buffer_load_dwordx4 %1, %5, %6 offen nt lds\n\t"
               "buffer_load_dwordx4 %2, %5, %6 offen offset:1024 nt lds\n\t"
               "buffer_load_dwordx4 %3, %5, %6 offen offset:2048 nt lds\n\t"
               "buffer_load_dwordx4 %4, %5, %6 offen offset:3072 nt lds"
               ::"s"(ldsb), "v"(v0), "v"(v1), "v"(v2), "v"(v3), "s"(srd), "s"(soff) : "memory");
}

__device__ __forceinline__ float silu_bf(float v) {
  const float t = -v * LOG2E, r = fmaf(-v, 1.9259629911e-8f, fmaf(-v, LOG2E, -t));
  const float e = __builtin_amdgcn_exp2f(t);
  return rbf(v / (1.f + (e < INFINITY ? fmaf(e * 0.6931471806f, r, e) : e)));
}
__device__ __forceinline__ void ln_round(float mean, float var, float& mb, float& sd) {
  mb = rbf(mean);
  sd = rbf(__builtin_amdgcn_sqrtf(rbf(rbf(var) + 1e-5f)));
}
__device__ __forceinline__ float mod_round(float n, float sc, float sh) { return rbf(rbf(rbf(n) * rbf(1.f + sc)) + sh); }

__global__ void __launch_bounds__(NTHR, 2) block_decode(Args a) {
  extern __shared__ __attribute__((aligned(16))) unsigned char smem[];
  const int tid = threadIdx.x, lane = tid & 63, wave = __builtin_amdgcn_readfirstlane(tid >> 6);
  const int b = blockIdx.x, x = b & 7, j = b >> 3;
  const u64 t0 = rt();
  const unsigned lb = (unsigned)(size_t)(LAS unsigned char*)smem;
  const rsrc_t ws = mkrsrc(a.ws, WS_END);
  const int kq = j >> 3, cb = x + 8 * (j & 7);
  const int gq = j >> 2;
  const bool mlp = (x & 1) == 0;
  const int og = (x & 6) + (j >> 4), oc0 = 32 * (j & 15);
  const bool has_gu = mlp && 4 * gq + (x >> 1) < 28;
  const int up = 4 * (4 * gq + (x >> 1)) + (j & 3);
  const int fc0 = 64 * x + 4 * j;

  if (tid < 20) *lds_pl<unsigned>(lb + L_CTL + 4 * tid) = (tid == 1 || tid == 3) ? (unsigned)INF : 0u;
  asm volatile("" :: "s"(a.K), "s"(a.ws));
  wg_barrier();
  int w_a = wave, w_b = wave;
  asm volatile("" : "+s"(w_a));
  if (w_a == 4) {
    const u64 kb = (u64)(a.K + (size_t)j * SK * DH), vb = (u64)(a.V + (size_t)j * SK * DH);
    const v4i ksrd = {(int)kb, (int)(kb >> 32), (int)HEAD_BYTES, 0x00020000}, vsrd = {(int)vb, (int)(vb >> 32), (int)HEAD_BYTES, 0x00020000};
    const unsigned g = lane >> 4, c = lane & 15;
    const unsigned k0 = g * 256 + ((c ^ g) << 4), k1 = g * 256 + ((c ^ (4 + g)) << 4), k2 = g * 256 + ((c ^ (8 + g)) << 4), k3 = g * 256 + ((c ^ (12 + g)) << 4);
    const unsigned vl = lane * 16;
    unsigned sl = lb + L_RING;
    auto fill = [&](v4i srd, int n, unsigned v0, unsigned v1, unsigned v2, unsigned v3) {
      const unsigned o = ((unsigned)n >> 2) * 65536u + ((unsigned)n & 3u) * 8192u;
      dma4(srd, o, sl, v0, v1, v2, v3);
      dma4(srd, o + 4096, sl + 4096, v0, v1, v2, v3);
      dma4(srd, o + 32768, sl + 8192, v0, v1, v2, v3);
      dma4(srd, o + 36864, sl + 12288, v0, v1, v2, v3);
      sl = sl + SLOT == lb + L_RING + NSLOT * SLOT ? lb + L_RING : sl + SLOT;
    };
    volatile LAS u64* FT = lds_at<u64>(lb + L_FT);
    int pub = 0;
    unsigned total = (unsigned)INF;
    auto publish = [&](int n) { pub = n > pub ? n : pub; *FT = (u64)(unsigned)pub | ((u64)total << 32); };
    auto wait_free = [&](int i) {
      const int p = i - NSLOT;
      volatile LAS u64* dn = lds_at<u64>(lb + L_DONE + 8 * (p & 1));
      const unsigned need = (unsigned)(p >> 1) + 1u;
      auto freed = [&]() { const u64 d = *dn; return (unsigned)d >= need && (unsigned)(d >> 32) >= need; };
      if (!freed()) {
        VM0(); publish(i);
        for (unsigned sp = 0; !freed(); ++sp) { __builtin_amdgcn_s_sleep(1); if (EXPIRED(sp)) break; }
      }
      MEMFENCE();
    };
    for (unsigned sp = 0; *lds_at<unsigned>(lb + L_ISSUED) < 5u && sp < 4096u; ++sp) {}
    unsigned tn = 1;
    auto take = [&]() {
      u64 tv;
      for (unsigned sp = 0; (unsigned)((tv = *lds_at<u64>(lb + L_TK)) >> 32) < tn; ++sp) if (EXPIRED(sp)) { tv = EMPTY; break; }
      ++tn;
      return (unsigned)__builtin_amdgcn_readfirstlane((unsigned)tv);
    };
    int i = 0, nt = 0, next = POOL0 + x * NP, left = NP;
    bool asked = true;
    for (;; ++i) {
      fill(ksrd, next, k0, k1, k2, k3);
      ++next; --left;
      if (i >= BURST) publish(i - 1);
      if (i == NP + SO - 5) *lds_at<unsigned>(lb + L_TKC) = tn;
      if (left == 0) {
        if (i == NP - 1) { next = x * SO; left = SO; }
        else {
          const unsigned t = asked ? take() : EMPTY;
          if (t >= (unsigned)NKT) break;
          left = 4; next = KD0 + 4 * (int)t;
          *lds_at<unsigned>(lb + L_LIST + 4 * nt) = t; ++nt;
          asked = i + 1 - NP + left + 4 <= CAP;
          if (asked) *lds_at<unsigned>(lb + L_TKC) = tn;
        }
      }
      if (i + 1 >= NSLOT) wait_free(i + 1);
      if (i + 1 >= BURST) vmwait<16>();
    }
    const int nk = i + 1, vown = 2 * nk - NP;
    *lds_at<unsigned>(lb + L_NK) = (unsigned)nk;
    wait_free(i + 1);
    vmwait<16>();
    next = x * SO; left = SO;
    int rt_ = 0;
    for (++i;; ++i) {
      fill(vsrd, next, vl, vl, vl, vl);
      ++next; --left;
      publish(i - 1);
      if (i == vown - 3) *lds_at<unsigned>(lb + L_TKC) = tn;
      if (left == 0) {
        if (rt_ < nt) { const unsigned t = __builtin_amdgcn_readfirstlane(*lds_at<unsigned>(lb + L_LIST + 4 * rt_)); ++rt_; left = 4; next = KD0 + 4 * (int)t; }
        else {
          const unsigned t = take();
          if (t >= (unsigned)NPT) break;
          *lds_at<unsigned>(lb + L_TKC) = tn;
          left = 2; next = POOL0 + 2 * (int)t;
        }
      }
      wait_free(i + 1);
      vmwait<16>();
    }
    total = (unsigned)i + 1u;
    publish(i - 1);
    vmwait<16>(); publish(i);
    VM0(); publish(i + 1);
    return;
  }

  asm volatile("" : "+s"(w_b));
  if (w_b == 5) {
    __builtin_amdgcn_s_setprio(3);
    const rsrc_t rwm = mkrsrc(a.wm, DM * 3072 * 2), rc = mkrsrc(a.c, DM * 2);
    const int n0 = 512 * (gq & 1) + 64 * x + 16 * (j & 3);
    const int ar = 128 * kq + 4 * (lane >> 1);
    const v2u c4 = ld64<0>(rc, ar * 2);
    MEMFENCE();
    v4u w1[4];
#pragma unroll
    for (int i = 0; i < 4; ++i) w1[i] = ld128<NT>(rwm, (unsigned)((ar + i) * 3072 + n0 + 8 * (lane & 1)) * 2);
    const v2u ep = ld64<SC1>(ws, WS_EPOCH);
    const int mr = 64 * gq + 8 * (lane >> 3), mc = 64 * (x + 8 * (j & 3));
    const v4u cv = ld128<0>(rc, mr * 2);
    MEMFENCE();
    if (lane == 0) __hip_atomic_fetch_add(lds_pl<unsigned>(lb + L_ISSUED), 1u, __ATOMIC_RELAXED, __HIP_MEMORY_SCOPE_WORKGROUP);
    st128(ws, (lane < 8 && !mlp) ? WS_YP + (og * 512 + oc0) * 4 + lane * 16 : OOB, v4u{EMPTY, EMPTY, EMPTY, EMPTY});
    st128(ws, has_gu ? WS_DP + up * 2048 + lane * 16 : OOB, v4u{EMPTY, EMPTY, EMPTY, EMPTY});
    st128(ws, has_gu ? WS_DP + up * 2048 + 1024 + lane * 16 : OOB, v4u{EMPTY, EMPTY, EMPTY, EMPTY});
    st128(ws, lane < 33 ? WS_PO + b * REC + lane * 16 : OOB, v4u{EMPTY, EMPTY, EMPTY, EMPTY});
    st32(ws, (lane < 2 && x == 0) ? WS_CTR + j * 256 + lane * 128 : OOB, 0u);
    const Tag tag = next_tag(ep);
    {
      float s8[8] = {0.f, 0.f, 0.f, 0.f, 0.f, 0.f, 0.f, 0.f};
      fma8(s8, silu_bf(lo16(c4[0])), w1[0]); fma8(s8, silu_bf(hi16(c4[0])), w1[1]);
      fma8(s8, silu_bf(lo16(c4[1])), w1[2]); fma8(s8, silu_bf(hi16(c4[1])), w1[3]);
      float u0, u1;
      rows_reduce8(s8, u0, u1);
      u0 += dpp<ROR8>(u0); u1 += dpp<ROR8>(u1); u0 += dpp<ROR4>(u0); u1 += dpp<ROR4>(u1); u0 += dpp<XOR2>(u0); u1 += dpp<XOR2>(u1);
      const int col = n0 + 8 * (lane & 1) + 2 * (lane >> 4);
      st128(ws, (lane & 15) < 2 ? WS_P1 + (kq * 1024 + col) * 8 : OOB, gr2<0>(u0, u1, tag));
      *lds_at<unsigned>(lb + L_AUXPUB) = 1u;
    }
    __builtin_amdgcn_s_setprio(0);
    auto stream_at = [&](unsigned fillno) {
      for (unsigned sp = 0; *lds_at<unsigned>(lb + L_FT) < fillno; ++sp) { __builtin_amdgcn_s_sleep(8); if ((sp & 63u) == 63u && rt() - t0 > TLIMIT) break; }
      MEMFENCE();
    };
    stream_at(NSLOT + 4);
    {
      v4u w2[8];
#pragma unroll
      for (int i = 0; i < 8; ++i) w2[i] = ld128<NT>(rwm, (unsigned)((mr + i) * 3072 + 1024 + mc + 8 * (lane & 7)) * 2);
      float s8[8] = {0.f, 0.f, 0.f, 0.f, 0.f, 0.f, 0.f, 0.f};
#pragma unroll
      for (int e = 0; e < 4; ++e) { fma8(s8, silu_bf(lo16(cv[e])), w2[2 * e]); fma8(s8, silu_bf(hi16(cv[e])), w2[2 * e + 1]); }
      float u0, u1;
      rows_reduce8(s8, u0, u1);
      u0 += dpp<ROR8>(u0); u1 += dpp<ROR8>(u1);
      st128(ws, (lane & 8) == 0 ? WS_M2P + (gq * 2048 + mc + 8 * (lane & 7) + 2 * (lane >> 4)) * 8 : OOB, gr2<4>(u0, u1, tag));
    }
    stream_at(NSLOT + 16);
    {
      v4u m;
      for (;;) {
        MEMFENCE();
        m = ld128<SC1>(ws, WS_M2P + (((lane >> 2) & 7) * 2048 + 8 * b + 2 * (lane & 3)) * 8);
        if (__all(gr_bad<4>(m, tag) == 0u) || rt() - t0 > TLIMIT) break;
        __builtin_amdgcn_s_sleep(32);
      }
      float u0 = gr_val<4>(m[0]), u1 = gr_val<4>(m[2]);
      u0 = sw16_add(u0, u0); u1 = sw16_add(u1, u1);
      u0 += dpp<ROR8>(u0); u1 += dpp<ROR8>(u1); u0 += dpp<ROR4>(u0); u1 += dpp<ROR4>(u1);
      VM0();
      st128(ws, lane < 4 ? WS_M2 + b * 64 + lane * 16 : OOB, gr2<4>(rbf(u0), rbf(u1), tag));
    }
    stream_at(NSLOT + 28);
#pragma unroll 1
    for (int u = 0; u < 16; ++u) {
      v4u m;
      for (;;) {
        MEMFENCE();
        m = ld128<SC1>(ws, WS_M2 + u * 1024 + lane * 16);
        if (__all(gr_bad<4>(m, tag) == 0u) || rt() - t0 > TLIMIT) break;
        __builtin_amdgcn_s_sleep(32);
      }
      *lds_pl<unsigned>(lb + L_M2 + u * 256 + lane * 4) = __builtin_amdgcn_perm(m[2], m[0], 0x07060302u);
      __builtin_amdgcn_s_sleep(16);
    }
    LGKM0();
    *lds_at<unsigned>(lb + L_M2RDY) = 1u;
    GAS unsigned* ctr = (GAS unsigned*)(a.ws + WS_CTR + j * 256);
    unsigned n = 1;
    auto draw = [&](int which) {
      unsigned v = 0;
      if (lane == 0) v = __hip_atomic_fetch_add(ctr + 32 * which, 1u, __ATOMIC_RELAXED, __HIP_MEMORY_SCOPE_AGENT);
      v = __builtin_amdgcn_readfirstlane(v);
      *lds_at<u64>(lb + L_TK) = ((u64)n << 32) | v;
      ++n;
      return v;
    };
    for (;;) {
      bool over = false;
      for (unsigned sp = 0; *lds_at<unsigned>(lb + L_TKC) < n; ++sp) {
        if (*lds_at<unsigned>(lb + L_NK) != (unsigned)INF || EXPIRED(sp)) { over = true; break; }
        __builtin_amdgcn_s_sleep(2);
      }
      if (over || *lds_at<unsigned>(lb + L_NK) != (unsigned)INF || draw(0) >= (unsigned)NKT) break;
    }
    lds_wait_ge(lb + L_KCNT, 5u, t0);
    __builtin_amdgcn_s_sleep(127);
    {
      v2u mx;
      for (;;) {
        MEMFENCE();
        mx = ld64<SC1>(ws, WS_MX + j * 64 + (lane & 7) * 8);
        if (__all(gr_bad<4>(v4u{mx[0], mx[1], mx[0], mx[1]}, tag) == 0u) || rt() - t0 > TLIMIT) break;
        __builtin_amdgcn_s_sleep(32);
      }
      *lds_at<float>(lb + L_MH) = row_max(gr_val<4>(mx[0]));
      LGKM0();
    }
    {
      unsigned dq = 0, pend = 0;
      v4u sc = {0u, 0u, 0u, 0u};
      auto flush = [&]() {
        if ((unsigned)lane < 8u * pend) *lds_pl<v4u>(lb + L_SC + dq * 128 + lane * 16) = sc;
        dq += pend; pend = 0;
        LGKM0();
        *lds_at<unsigned>(lb + L_SCR) = dq;
      };
      for (;;) {
        if (__builtin_amdgcn_readfirstlane(*lds_at<unsigned>(lb + L_TKC)) < n) flush();
        for (unsigned sp = 0; *lds_at<unsigned>(lb + L_TKC) < n; ++sp) { __builtin_amdgcn_s_sleep(2); if (EXPIRED(sp)) break; }
        const unsigned v = draw(1);
        flush();
        if (v >= (unsigned)NPT || rt() - t0 > TLIMIT) break;
        pend = 2u;
        sc = ld128<SC1 | NT>(ws, lane < 16 ? WS_SG + (j * 8 * NP + 2 * v) * 128 + lane * 16 : OOB);
        MEMFENCE();
      }
    }
    return;
  }

  const int g = lane >> 4, c = lane & 15;
  const rsrc_t rx = mkrsrc(a.x, DM * 2), rwq = mkrsrc(a.wq, DM * 4096 * 2);
  const rsrc_t rwd = mkrsrc(a.wd, DF * DM * 2);
  const int r0 = 128 * kq + 32 * wave;
  v4u wqv[4];
#pragma unroll
  for (int i = 0; i < 4; ++i) wqv[i] = ld128<NT>(rwq, (unsigned)((r0 + 4 * (lane >> 3) + i) * 4096 + 64 * cb + 8 * (lane & 7)) * 2);
  const v2u ep = ld64<SC1>(ws, WS_EPOCH);
  const v4u x8 = ld128<0>(rx, lane * 16);
  const unsigned x2 = ld32<0>(rx, tid * 4);
  const unsigned xh = ld32<0>(rx, (r0 + 2 * c) * 2);
  MEMFENCE();
  if (lane == 0) __hip_atomic_fetch_add(lds_pl<unsigned>(lb + L_ISSUED), 1u, __ATOMIC_RELAXED, __HIP_MEMORY_SCOPE_WORKGROUP);
  float mb, sd;
  {
    float xs[8];
#pragma unroll
    for (int e = 0; e < 4; ++e) { xs[2 * e] = lo16(x8[e]); xs[2 * e + 1] = hi16(x8[e]); }
    float s = 0.f;
#pragma unroll
    for (int e = 0; e < 8; ++e) s += xs[e];
    const float mean = wave_sum(s) * (1.f / 512.f);
    float v = 0.f;
#pragma unroll
    for (int e = 0; e < 8; ++e) { const float d = xs[e] - mean; v = fmaf(d, d, v); }
    ln_round(mean, wave_sum(v) * (1.f / 512.f), mb, sd);
  }
  float nrow = rbf((lane >= 32 ? hi16(xh) : lo16(xh)) - mb) / sd;
  kpair(wqv[0], wqv[1]); kpair(wqv[2], wqv[3]);
  PIN4(wqv); asm volatile("" : "+v"(nrow));
  const Tag tag = next_tag(ep);
  lds_wait_ge(lb + L_AUXPUB, 1u, t0);
  {
    const unsigned o1 = WS_P1 + (g * 1024 + r0 + 2 * c) * 8;
    v4u gs = {0u, 0u, 0u, 0u}, gc = gs;
    bool bad = true;
    __builtin_amdgcn_s_sleep(32);
    for (;;) {
      MEMFENCE();
      const v4u ns_ = ld128<SC1>(ws, bad ? o1 : OOB), nc_ = ld128<SC1>(ws, bad ? o1 + 4096 : OOB);
      const bool tk = bad & ((gr_bad<0>(ns_, tag) | gr_bad<0>(nc_, tag)) == 0u);
      gs = sel4(tk, ns_, gs); gc = sel4(tk, nc_, gc); bad = bad & !tk;
      if (!__any(bad) || rt() - t0 > TLIMIT) break;
    }
    const unsigned hp = pack_halves(mod_round(nrow, rbf(gr_sum<0>(gc)), rbf(gr_sum<0>(gs))));
    float acc[8] = {0.f, 0.f, 0.f, 0.f, 0.f, 0.f, 0.f, 0.f};
    dot8(acc, __builtin_amdgcn_ds_bpermute((lane >> 3) * 8, hp), wqv[0], wqv[1]);
    dot8(acc, __builtin_amdgcn_ds_bpermute((lane >> 3) * 8 + 4, hp), wqv[2], wqv[3]);
    float u0, u1;
    rows_reduce8(acc, u0, u1);
    u0 += dpp<ROR8>(u0); u1 += dpp<ROR8>(u1);
    if ((lane & 8) == 0) *lds_pl<f32x2>(lb + L_RED + wave * 256 + (8 * (lane & 7) + 2 * g) * 4) = f32x2{u0, u1};
    LGKM0();
    unsigned old = 0;
    if (lane == 0) old = __hip_atomic_fetch_add(lds_pl<unsigned>(lb + L_QCNT), 1u, __ATOMIC_RELAXED, __HIP_MEMORY_SCOPE_WORKGROUP);
    old = __builtin_amdgcn_readfirstlane(old);
    MEMFENCE();
    if (old == 3u) {
      const unsigned ra = lb + L_RED + lane * 4;
      const float qp = (*lds_pl<float>(ra) + *lds_pl<float>(ra + 256)) + (*lds_pl<float>(ra + 512) + *lds_pl<float>(ra + 768));
      st64(ws, WS_QS + (kq * 4096 + 64 * cb + lane) * 8, gr1<2>(qp, tag));
    }
  }
  const int r8 = 8 * (tid >> 2);
  v4u wv[8];
  unsigned wdv[16];
#pragma unroll
  for (int i = 0; i < 8; ++i) { wv[i] = v4u{0u, 0u, 0u, 0u}; wdv[2 * i] = 0u; wdv[2 * i + 1] = 0u; }
  const GAS v4u* wp = (const GAS v4u*)(mlp ? ((tid & 2) ? a.wu : a.wg) + (r8 * DF + 16 * (has_gu ? up : 0) + 8 * (tid & 1)) : a.wo + ((512 * og + r8) * DM + oc0 + 8 * (tid & 3)));
  const int wstride = mlp ? DF / 8 : DM / 8;
  const unsigned wdo = has_gu ? (unsigned)(16 * up * DM + 2 * tid) * 2 : OOB;

  const unsigned key = 4 * (c & 3) + (c >> 2);
  unsigned ka[4], msk[4];
#pragma unroll
  for (int st = 0; st < 4; ++st) {
    ka[st] = lb + L_RING + (wave & 1) * 8192 + key * 256 + (((st + 4 * g) ^ key) << 4);
    msk[st] = ((c & 7) >> 1) == st ? ((c & 1) ? 0xffff0000u : 0x0000ffffu) : 0u;
  }
  const unsigned va = lb + L_RING + (wave & 1) * 8192 + lane * 16;
  const bool is_hi = c < 8;
  volatile LAS unsigned* DN = lds_at<unsigned>(lb + L_DONE + 4 * wave);
  unsigned so = (wave >> 1) * SLOT;
  v4u sv[8], sw[8], ud[8], qf[4];
#pragma unroll
  for (int i = 0; i < 8; ++i) { sv[i] = v4u{0u, 0u, 0u, 0u}; sw[i] = sv[i]; ud[i] = sv[i]; }
  int ns = 0;
  auto release = [&](v4u (&d)[8], int i) {
    LGKM0();
    *DN = (unsigned)(i >> 1) + 1u;
    asm volatile("" : "+v"(d[0]), "+v"(d[1]), "+v"(d[2]), "+v"(d[3]), "+v"(d[4]), "+v"(d[5]), "+v"(d[6]), "+v"(d[7]) :: "memory");
    so = (so + 2 * SLOT) & (NSLOT * SLOT - 1);
  };
  auto read_k = [&](v4u (&d)[8], int i) {
#pragma unroll
    for (int st = 0; st < 4; ++st) { d[st] = *lds_pl<v4u>(ka[st] + so); d[4 + st] = *lds_pl<v4u>(ka[st] + so + 4096); }
    release(d, i);
  };
  int total = INF, nk = INF;
  auto wait_full = [&](int i, bool in_k) {
    for (unsigned sp = 0;; ++sp) {
      const v4u ct = *(volatile LAS v4u*)(size_t)(lb + L_FT);
      total = __builtin_amdgcn_readfirstlane((int)ct[1]);
      nk = __builtin_amdgcn_readfirstlane((int)ct[3]);
      if (EXPIRED(sp)) { total = 0; nk = nk < i ? nk : i; break; }
      if (__builtin_amdgcn_readfirstlane((int)ct[0]) > i || (in_k && nk <= i)) break;
      if (total == INF) __builtin_amdgcn_s_sleep(1);
    }
    MEMFENCE();
  };
  float mraw = -INFINITY;
  auto scores = [&](const v4u (&d)[8], int i) {
    f32x4 sa = {0.f, 0.f, 0.f, 0.f}, sb = {0.f, 0.f, 0.f, 0.f};
#pragma unroll
    for (int st = 0; st < 4; ++st) { sa = mfma(d[st], qf[st], sa); sb = mfma(d[4 + st], qf[st], sb); }
    mraw = fmaxf(fmaxf(fmaxf(mraw, sa[0]), fmaxf(sa[1], sa[2])), fmaxf(fmaxf(sa[3], sb[0]), fmaxf(fmaxf(sb[1], sb[2]), sb[3])));
    v4u pk = {cvtpk(sa[0], sa[1]), cvtpk(sa[2], sa[3]), cvtpk(sb[0], sb[1]), cvtpk(sb[2], sb[3])};
#pragma unroll
    for (int u = 0; u < 4; ++u) pk[u] = cvtpk(lo16(pk[u]) * SCALE, hi16(pk[u]) * SCALE);
    const unsigned so_ = (wave & 1) * 64 + g * 16;
    const unsigned dst = i < NP ? L_SH + i * 128 : L_SC + (i - NP) * 128;
    if (c == 0) *lds_pl<v4u>(lb + dst + so_) = pk;
  };
  auto stash = [&]() {
    const int full = __builtin_amdgcn_readfirstlane((int)*lds_at<unsigned>(lb + L_FT)), i0 = wave >> 1;
    MEMFENCE();
    if (ns == 0 && full > i0) { read_k(wv, i0); ns = 1; }
    if (ns == 1 && full > i0 + 2) { read_k(sv, i0 + 2); ns = 2; }
    if (ns == 2 && full > i0 + 4) { read_k(sw, i0 + 4); ns = 3; }
    if (ns == 3 && full > i0 + 6) { read_k(ud, i0 + 6); ns = 4; }
  };
  {
    const unsigned o2 = WS_QS + (g * 4096 + 128 * j + 32 * wave + 2 * c) * 8;
    v4u gq4 = {0u, 0u, 0u, 0u};
    bool bad = true;
    stash();
    {
      const v4u nq = ld128<SC1>(ws, o2);
      bad = gr_bad<2>(nq, tag) != 0u;
      gq4 = nq;
    }
    asm volatile("" : "+v"(gq4) :: "memory");
    stash();
    for (unsigned it = 0; __any(bad) && !EXPIRED(it); ++it) {
      MEMFENCE();
      const v4u nq = ld128<SC1>(ws, bad ? o2 : OOB);
      const bool tk = bad & (gr_bad<2>(nq, tag) == 0u);
      gq4 = sel4(tk, nq, gq4); bad = bad & !tk;
    }
    *lds_pl<unsigned>(lb + L_QSC + (16 * wave + c) * 4) = pack_halves(rbf(gr_sum<2>(gq4)));
    LGKM0();
    if (lane == 0) __hip_atomic_fetch_add(lds_pl<unsigned>(lb + L_QIN), 1u, __ATOMIC_RELAXED, __HIP_MEMORY_SCOPE_WORKGROUP);
    lds_wait_ge(lb + L_QIN, 4u, t0);
#pragma unroll
    for (int st = 0; st < 4; ++st) qf[st] = *lds_pl<v4u>(lb + L_QSC + (st + 4 * g) * 16);
  }
  if (ns >= 1) scores(wv, wave >> 1);
  if (ns >= 2) scores(sv, (wave >> 1) + 2);
  if (ns >= 3) scores(sw, (wave >> 1) + 4);
  if (ns >= 4) scores(ud, (wave >> 1) + 6);
  int i = (wave >> 1) + 2 * ns;
  for (;; i += 2) {
    wait_full(i, true);
    if (nk <= i) break;
    read_k(ud, i);
    scores(ud, i);
  }
  float m2;
  {
    *lds_pl<float>(lb + L_MXW + wave * 4) = rbf(rbf(xrow_max(mraw)) * SCALE);
    LGKM0();
    unsigned old = 0;
    if (lane == 0) old = __hip_atomic_fetch_add(lds_pl<unsigned>(lb + L_KCNT), 1u, __ATOMIC_RELAXED, __HIP_MEMORY_SCOPE_WORKGROUP);
    old = __builtin_amdgcn_readfirstlane(old);
    lds_wait_ge(lb + L_KCNT, 4u, t0);
    const f32x4 mw = *lds_pl<f32x4>(lb + L_MXW);
    const float mg = fmaxf(fmaxf(mw[0], mw[1]), fmaxf(mw[2], mw[3]));
    if (old == 3u) {
#pragma unroll
      for (int u = 0; u < (NP + 7) / 8; ++u)
        __builtin_amdgcn_raw_buffer_store_b128(*lds_pl<v4u>(lb + L_SH + u * 1024 + lane * 16), ws,
                                               u * 1024 + lane * 16 < NP * 128 ? WS_SG + b * NP * 128 + u * 1024 + lane * 16 : OOB, 0, SC1 | NT);
      VM0();
      st64(ws, lane == 0 ? WS_MX + b * 8 : OOB, gr1<4>(mg, tag));
      *lds_at<unsigned>(lb + L_KCNT) = 5u;
    }
    m2 = mg * LOG2E;
  }
  float lsum = 0.f;
  f32x4 acc = {0.f, 0.f, 0.f, 0.f};
  {
    int wk = (mlp && !has_gu) ? 11 : 0;
    bool done = false, own = true;
    for (;; i += 2) {
      int e = i - nk;
      if (!done && e >= nk - NP) {
        e -= nk - NP;
        for (unsigned sp = 0;; ++sp) {
          const v4u ct = *(volatile LAS v4u*)(size_t)(lb + L_FT);
          if (__builtin_amdgcn_readfirstlane((int)ct[2]) > e) break;
          if (__builtin_amdgcn_readfirstlane((int)ct[1]) <= i || EXPIRED(sp)) { done = true; break; }
          __builtin_amdgcn_s_sleep(1);
        }
        MEMFENCE();
        if (own) {
          const float mh = *lds_at<float>(lb + L_MH) * LOG2E, al = __builtin_amdgcn_exp2f(m2 - mh);
          acc *= al; lsum *= al; m2 = mh; own = false;
        }
      }
      if (!done) {
        const v4u sc = *lds_pl<v4u>(lb + L_SC + e * 128 + (wave & 1) * 64 + g * 16);
        v4u pa[8];
#pragma unroll
        for (int u = 0; u < 8; ++u) {
          const float p = __builtin_amdgcn_exp2f(fmaf((u & 1) ? hi16(sc[u >> 1]) : lo16(sc[u >> 1]), LOG2E, -m2));
          lsum += p;
          const float ph = hi16(asu(p));
          const float xv = is_hi ? ph : p - ph;
          const unsigned pk = cvtpk(xv, xv);
          pa[u] = v4u{pk & msk[0], pk & msk[1], pk & msk[2], pk & msk[3]};
        }
        asm volatile("" : "+v"(pa[0]), "+v"(pa[1]), "+v"(pa[2]), "+v"(pa[3]), "+v"(pa[4]), "+v"(pa[5]), "+v"(pa[6]), "+v"(pa[7]));
        wait_full(i, false);
#pragma unroll
        for (int u = 0; u < 8; ++u) ud[u] = *lds_pl<v4u>(va + so + u * 1024);
        release(ud, i);
#pragma unroll
        for (int u = 0; u < 8; ++u) acc = mfma(pa[u], ud[u], acc);
        done = i + 2 >= total && !own;
      }
      MEMFENCE();
      if (wk <= 10 && (done || rt() - t0 >= (u64)(TW + TD * wk + (wk == 10 ? 400 : 0)))) {
#pragma unroll
        for (int u = 0; u < 8; ++u) if (wk == u) wv[u] = __builtin_nontemporal_load(wp + u * wstride);
#pragma unroll
        for (int v = 0; v < 2; ++v) if (wk == 8 + v) {
#pragma unroll
          for (int u = 8 * v; u < 8 * v + 8; ++u) wdv[u] = ld32<NT>(rwd, wdo + u * (DM * 2));
        }
        if (wk == 10) {
#pragma unroll
          for (int u = 0; u < 8; u += 2) kpair(wv[u], wv[u + 1]);
#pragma unroll
          for (int u = 0; u < 16; u += 2) {
            const unsigned lo = __builtin_amdgcn_perm(wdv[u + 1], wdv[u], 0x05040100u), hi = __builtin_amdgcn_perm(wdv[u + 1], wdv[u], 0x07060302u);
            wdv[u] = lo; wdv[u + 1] = hi;
          }
        }
        ++wk;
      }
      MEMFENCE();
      if (done && wk > 10) break;
    }
  }
  {
    f32x4 o;
#pragma unroll
    for (int r = 0; r < 4; ++r) o[r] = sw32_add(acc[r], acc[r]);
    float l = sw16_add(lsum, lsum); l = sw32_add(l, l);
    *lds_pl<f32x4>(lb + L_MRG + wave * 512 + (8 * c + 4 * (g & 1)) * 4) = o;
    *lds_pl<float>(lb + L_LW + wave * 4) = l;
    wg_barrier();
    if (wave == 0) {
      const unsigned ra = lb + L_MRG + (lane & 31) * 16;
      const f32x4 lw = *lds_pl<f32x4>(lb + L_LW);
      f32x4 O = (*lds_pl<f32x4>(ra) + *lds_pl<f32x4>(ra + 512)) + (*lds_pl<f32x4>(ra + 1024) + *lds_pl<f32x4>(ra + 1536));
      if (lane >= 32) O = f32x4{(lw[0] + lw[1]) + (lw[2] + lw[3]), 0.f, 0.f, 0.f};
      st128(ws, lane < 33 ? WS_PO + b * REC + lane * 16 : OOB, noempty4(O));
    }
  }
  if (!mlp) {
    const unsigned hb = WS_PO + (4 * og + wave) * 8 * REC;
    v2u po[8];
    unsigned ls = EMPTY;
#pragma unroll
    for (int s = 0; s < 8; ++s) po[s] = v2u{0u, 0u};
    unsigned miss = 0x1FFu;
    for (unsigned it = 0;; ++it) {
      MEMFENCE();
      if (miss & 0x100u) ls = ld32<SC1>(ws, (lane & 15) < 8 ? hb + (lane & 15) * REC + 512 : OOB);
#pragma unroll
      for (int s = 0; s < 8; ++s) if (miss & (1u << s)) po[s] = ld64<SC1>(ws, hb + s * REC + lane * 8);
      if ((miss & 0x100u) && __all(ls != EMPTY)) miss &= ~0x100u;
#pragma unroll
      for (int s = 0; s < 8; ++s) if ((miss & (1u << s)) && __all(mx2(0u, po[s]) != EMPTY)) miss &= ~(1u << s);
      if (miss == 0u || EXPIRED(it)) break;
    }
    const float inv = __builtin_amdgcn_rcpf(row_sum(asf(ls)));
    const unsigned op = cvtpk((((asf(po[0][0]) + asf(po[1][0])) + (asf(po[2][0]) + asf(po[3][0]))) + ((asf(po[4][0]) + asf(po[5][0])) + (asf(po[6][0]) + asf(po[7][0])))) * inv,
                              (((asf(po[0][1]) + asf(po[1][1])) + (asf(po[2][1]) + asf(po[3][1]))) + ((asf(po[4][1]) + asf(po[5][1])) + (asf(po[6][1]) + asf(po[7][1])))) * inv);
    float y[8] = {0.f, 0.f, 0.f, 0.f, 0.f, 0.f, 0.f, 0.f};
    dot8(y, dppu<0x00>(op), wv[0], wv[1]); dot8(y, dppu<0x55>(op), wv[2], wv[3]);
    dot8(y, dppu<0xAA>(op), wv[4], wv[5]); dot8(y, dppu<0xFF>(op), wv[6], wv[7]);
    float u0, u1;
    rows_reduce8(y, u0, u1);
    u0 += dpp<ROR8>(u0); u1 += dpp<ROR8>(u1); u0 += dpp<ROR4>(u0); u1 += dpp<ROR4>(u1);
    *lds_pl<f32x2>(lb + L_RED3 + wave * 128 + (8 * (c & 3) + 2 * g) * 4) = f32x2{u0, u1};
    wg_barrier();
    if (wave == 0) {
      const unsigned ra = lb + L_RED3 + (lane & 7) * 16;
      const f32x4 t = (*lds_pl<f32x4>(ra) + *lds_pl<f32x4>(ra + 128)) + (*lds_pl<f32x4>(ra + 256) + *lds_pl<f32x4>(ra + 384));
      st128(ws, lane < 8 ? WS_YP + (og * 512 + oc0) * 4 + lane * 16 : OOB, noempty4(t));
    }
    return;
  }
  lds_wait_ge(lb + L_M2RDY, 1u, t0);
  float gbf = lo16(*lds_pl<u16>(lb + L_M2 + 3072 + (fc0 + g) * 2));
  {
    const unsigned gaw = *lds_pl<unsigned>(lb + L_M2 + tid * 4), sbw = *lds_pl<unsigned>(lb + L_M2 + 1024 + tid * 4), cbw = *lds_pl<unsigned>(lb + L_M2 + 2048 + tid * 4);
    float ga0 = lo16(gaw), ga1 = hi16(gaw), sb0 = lo16(sbw), sb1 = hi16(sbw), t0c = rbf(1.f + lo16(cbw)), t1c = rbf(1.f + hi16(cbw)), xa = lo16(x2), xb = hi16(x2);
    asm volatile("" : "+v"(ga0), "+v"(ga1), "+v"(sb0), "+v"(sb1), "+v"(t0c), "+v"(t1c), "+v"(xa), "+v"(xb), "+v"(gbf));
    v2u yp[8];
#pragma unroll
    for (int s = 0; s < 8; ++s) yp[s] = v2u{0u, 0u};
    unsigned miss = 0xFFu;
    for (unsigned it = 0;; ++it) {
      MEMFENCE();
#pragma unroll
      for (int s = 0; s < 8; ++s) if (miss & (1u << s)) yp[s] = ld64<SC1>(ws, WS_YP + s * 2048 + tid * 8);
#pragma unroll
      for (int s = 0; s < 8; ++s) if ((miss & (1u << s)) && __all(mx2(0u, yp[s]) != EMPTY)) miss &= ~(1u << s);
      if (miss == 0u || EXPIRED(it)) break;
    }
    const unsigned atp = cvtpk(((asf(yp[0][0]) + asf(yp[1][0])) + (asf(yp[2][0]) + asf(yp[3][0]))) + ((asf(yp[4][0]) + asf(yp[5][0])) + (asf(yp[6][0]) + asf(yp[7][0]))),
                               ((asf(yp[0][1]) + asf(yp[1][1])) + (asf(yp[2][1]) + asf(yp[3][1]))) + ((asf(yp[4][1]) + asf(yp[5][1])) + (asf(yp[6][1]) + asf(yp[7][1]))));
    const unsigned gap = cvtpk(ga0 * lo16(atp), ga1 * hi16(atp));
    const unsigned x1p = cvtpk(xa + lo16(gap), xb + hi16(gap));
    const float x1a = lo16(x1p), x1b = hi16(x1p);
    const float sh = asf(__builtin_amdgcn_readfirstlane(asu(x1a)));
    const float da = x1a - sh, db = x1b - sh;
    const float s1 = wave_sum(da + db), s2 = wave_sum(fmaf(da, da, db * db));
    *lds_pl<f32x2>(lb + L_ST + wave * 8) = f32x2{fmaf(s1, 1.f / 128.f, sh), fmaf(-s1 * (1.f / 128.f), s1, s2)};
    *lds_pl<f32x2>(lb + L_X1 + tid * 8) = f32x2{x1a, x1b};
    wg_barrier();
    if (has_gu) {
      const f32x4 sA = *lds_pl<f32x4>(lb + L_ST), sB = *lds_pl<f32x4>(lb + L_ST + 16);
      const float mean = ((sA[0] + sA[2]) + (sB[0] + sB[2])) * 0.25f;
      const float d0 = sA[0] - mean, d1 = sA[2] - mean, d2 = sB[0] - mean, d3 = sB[2] - mean;
      const float var = fmaxf(((sA[1] + sA[3]) + (sB[1] + sB[3])) + 128.f * ((d0 * d0 + d1 * d1) + (d2 * d2 + d3 * d3)), 0.f) * (1.f / 512.f);
      float mb2, sd2;
      ln_round(mean, var, mb2, sd2);
      const float rs = __builtin_amdgcn_rcpf(sd2);
      const unsigned e0 = cvtpk(x1a - mb2, x1b - mb2), e1 = cvtpk(lo16(e0) * rs, hi16(e0) * rs), e2 = cvtpk(lo16(e1) * t0c, hi16(e1) * t1c);
      const unsigned hq = cvtpk(lo16(e2) + sb0, hi16(e2) + sb1);
      float y[8] = {0.f, 0.f, 0.f, 0.f, 0.f, 0.f, 0.f, 0.f};
      dot8(y, dppu<0x00>(hq), wv[0], wv[1]); dot8(y, dppu<0x55>(hq), wv[2], wv[3]);
      dot8(y, dppu<0xAA>(hq), wv[4], wv[5]); dot8(y, dppu<0xFF>(hq), wv[6], wv[7]);
      float u0, u1;
      rows_reduce8(y, u0, u1);
      u0 += dpp<ROR8>(u0); u1 += dpp<ROR8>(u1); u0 += dpp<ROR4>(u0); u1 += dpp<ROR4>(u1);
      *lds_pl<f32x2>(lb + L_RED3 + wave * 128 + (8 * (c & 3) + 2 * g) * 4) = f32x2{u0, u1};
      wg_barrier();
      const unsigned ra = lb + L_RED3 + c * 4;
      const float gs = rbf((*lds_pl<float>(ra) + *lds_pl<float>(ra + 128)) + (*lds_pl<float>(ra + 256) + *lds_pl<float>(ra + 384)));
      const float us = rbf((*lds_pl<float>(ra + 64) + *lds_pl<float>(ra + 192)) + (*lds_pl<float>(ra + 320) + *lds_pl<float>(ra + 448)));
      const float z = 0.7978845608028654f * fmaf(0.044715f * gs * gs, gs, gs);
      const float th = fmaf(-2.f, __builtin_amdgcn_rcpf(1.f + __expf(2.f * z)), 1.f);
      const float h2 = rbf(rbf(0.5f * gs * (1.f + th)) * us);
      const unsigned hp = __builtin_amdgcn_perm(asu(dpp<XOR1>(h2)), asu(h2), 0x07060302u);
      float d0a = 0.f, d1a = 0.f;
#pragma unroll
      for (int e = 0; e < 16; e += 2) {
        const unsigned he = __builtin_amdgcn_readlane(hp, e);
        d0a = dot2(wdv[e], he, d0a); d1a = dot2(wdv[e + 1], he, d1a);
      }
      st64(ws, WS_DP + up * 2048 + tid * 8, v2u{noempty(asu(d0a)), noempty(asu(d1a))});
      st64(ws, (b | tid) == 0 ? WS_EPOCH : OOB, v2u{tag.lo, tag.hi});
    }
  }
  if (wave != 0) return;
  {
    v4u da = {EMPTY, 0u, 0u, 0u}, db = {lane < NGU - 64 ? EMPTY : 0u, 0u, 0u, 0u};
    const float x1o = *lds_pl<float>(lb + L_X1 + (fc0 + g) * 4);
    for (unsigned it = 0;; ++it) {
      MEMFENCE();
      const bool ma = mx4(0u, da) == EMPTY, mb_ = mx4(0u, db) == EMPTY;
      if (!__any(ma | mb_) || EXPIRED(it)) break;
      const v4u na = ld128<SC1>(ws, ma ? WS_DP + lane * 2048 + fc0 * 4 : OOB), nb = ld128<SC1>(ws, mb_ ? WS_DP + (lane + 64) * 2048 + fc0 * 4 : OOB);
      da = ma ? na : da; db = mb_ ? nb : db;
    }
    float t4[4];
#pragma unroll
    for (int r = 0; r < 4; ++r) t4[r] = asf(da[r]) + asf(db[r]);
    const float r = row_sum(sw16_add(sw32_add(t4[0], t4[2]), sw32_add(t4[1], t4[3])));
    const float res = x1o + rbf(gbf * rbf(r));
    if (c == 0) a.out[fc0 + g] = (u16)(cvtpk(res, res) & 0xffffu);
  }
}
}

extern "C" void kernel_launch(void* const* d_in, const int* in_sizes, int n_in, void* d_out, int out_size, void* d_ws, size_t ws_size,
                              hipStream_t stream) {
  Args a;
  a.x = (const u16*)d_in[0]; a.K = (const u16*)d_in[1]; a.V = (const u16*)d_in[2]; a.wq = (const u16*)d_in[3]; a.wo = (const u16*)d_in[4];
  a.wg = (const u16*)d_in[6]; a.wu = (const u16*)d_in[7]; a.wd = (const u16*)d_in[8]; a.c = (const u16*)d_in[9]; a.wm = (const u16*)d_in[10];
  a.out = (u16*)d_out; a.ws = (unsigned char*)d_ws;
  (void)hipFuncSetAttribute((const void*)block_decode, hipFuncAttributeMaxDynamicSharedMemorySize, (int)LDS_BYTES);
  hipLaunchKernelGGL(block_decode, dim3(NWG), dim3(NTHR), LDS_BYTES, stream, a);
}
```

```cpp
#include <hip/hip_runtime.h>
#include <hip/hip_bf16.h>

#define GAS __attribute__((address_space(1)))
#define LAS __attribute__((address_space(3)))
typedef unsigned short u16;
typedef unsigned long long u64;
typedef unsigned v4u __attribute__((ext_vector_type(4)));
typedef unsigned v2u __attribute__((ext_vector_type(2)));
typedef int v4i __attribute__((ext_vector_type(4)));
typedef float f32x4 __attribute__((ext_vector_type(4)));
typedef __bf16 bf16x8 __attribute__((ext_vector_type(8)));
typedef __bf16 bf16x2 __attribute__((ext_vector_type(2)));
typedef float f32x2 __attribute__((ext_vector_type(2)));

#define PW 22
#define PG 8
#define PFROM 20
namespace {
constexpr int DM = 512, SK = 65536, DH = 128, DF = 1792;
constexpr int NWG = 256, NTHR = 384;
constexpr int SLOT = 16384, NSLOT = 8, FH = 1024;
constexpr int SO = 112;
constexpr int NP = 8;
constexpr int POOL0 = 8 * SO, KD0 = POOL0 + 8 * NP, NKT = (FH - KD0) / 4, NPT = 8 * NP / 2;
constexpr int CAP = 144;
static_assert((FH - KD0) % 4 == 0 && SO % 4 == 0 && NP % 2 == 0 && 8 * NP + 16 <= SO && NP <= 8 && 8 * (CAP - 7 - SO) >= FH - KD0, "fill plan");
constexpr int BURST = 3;
constexpr unsigned TW = 10800, TD = 110;
constexpr int INF = 0x7FFFFFFF;
constexpr unsigned HEAD_BYTES = (unsigned)SK * DH * 2;
constexpr unsigned EMPTY = 0xFFFFFFFFu;
constexpr unsigned OOB = 0x7FFF0000u;
constexpr u64 TLIMIT = 5000000ull;
constexpr float LOG2E = 1.4426950408889634f, SCALE = 0.08838834764831845f;
constexpr int NGU = DF / 16, REC = 640;
constexpr unsigned WS_EPOCH = 0, WS_P1 = 4096, WS_QS = WS_P1 + 32768, WS_M2P = WS_QS + 131072, WS_M2 = WS_M2P + 131072, WS_CTR = WS_M2 + 16384,
                   WS_MX = WS_CTR + 8192, WS_YP = WS_MX + 2048, WS_PO = WS_YP + 16384, WS_DP = WS_PO + NWG * REC,
                   WS_SG = WS_DP + NGU * 2048, WS_END = WS_SG + NWG * NP * 128;
constexpr unsigned L_RING = 0, L_CTL = NSLOT * SLOT, L_FT = L_CTL, L_SCR = L_CTL + 8, L_NK = L_CTL + 12, L_DONE = L_CTL + 16, L_QCNT = L_CTL + 32,
                   L_AUXPUB = L_CTL + 36, L_QIN = L_CTL + 40, L_ISSUED = L_CTL + 44, L_M2RDY = L_CTL + 48, L_TKC = L_CTL + 52, L_TK = L_CTL + 56,
                   L_KCNT = L_CTL + 64, L_MH = L_CTL + 68, L_MXW = L_CTL + 80, L_LW = L_CTL + 96, L_LIST = L_CTL + 128, L_RED = L_LIST + 128, L_QSC = L_RED + 1024, L_RED3 = L_QSC + 256,
                   L_ST = L_RED3 + 512, L_M2 = L_ST + 64, L_X1 = L_M2 + 4096, L_MRG = L_X1 + 2048, L_SH = L_X1, L_SC = L_MRG + 2048, L_END = L_SC + CAP * 128;
constexpr unsigned LDS_BYTES = L_END;
static_assert(LDS_BYTES <= 160 * 1024 && LDS_BYTES > 80 * 1024, "one workgroup per CU");

struct Args {
  const u16 *x, *K, *V, *wq, *wo, *wg, *wu, *wd, *c, *wm;
  u16* out;
  unsigned char* ws;
};

__device__ __forceinline__ u64 rt() { return __builtin_amdgcn_s_memrealtime(); }
__device__ __forceinline__ float asf(unsigned u) { return __builtin_bit_cast(float, u); }
__device__ __forceinline__ unsigned asu(float f) { return __builtin_bit_cast(unsigned, f); }
__device__ __forceinline__ float lo16(unsigned u) { return asf(u << 16); }
__device__ __forceinline__ float hi16(unsigned u) { return asf(u & 0xffff0000u); }
__device__ __forceinline__ unsigned cvtpk(float a, float b) {
  const f32x2 v = {a, b};
  return __builtin_bit_cast(unsigned, __builtin_convertvector(v, bf16x2));
}
__device__ __forceinline__ float rbf(float f) { return lo16(cvtpk(f, f)); }
__device__ __forceinline__ unsigned noempty(unsigned v) { return v < EMPTY - 1u ? v : EMPTY - 1u; }
__device__ __forceinline__ float dot2(unsigned a, unsigned b, float c) {
  return __builtin_amdgcn_fdot2_f32_bf16(__builtin_bit_cast(bf16x2, a), __builtin_bit_cast(bf16x2, b), c, false);
}
template <int CTRL> __device__ __forceinline__ float dpp(float v) {
  return asf(__builtin_amdgcn_mov_dpp(asu(v), CTRL, 0xf, 0xf, true));
}
constexpr int ROR8 = 0x128, ROR4 = 0x124, ROR2 = 0x122, ROR1 = 0x121, XOR2 = 0x4E, XOR1 = 0xB1;
template <int CTRL> __device__ __forceinline__ unsigned dppu(unsigned v) { return __builtin_amdgcn_mov_dpp(v, CTRL, 0xf, 0xf, true); }
__device__ __forceinline__ float sw32_add(float a, float b) {
  const auto r = __builtin_amdgcn_permlane32_swap(asu(a), asu(b), false, false);
  return asf(r[0]) + asf(r[1]);
}
__device__ __forceinline__ float sw16_add(float a, float b) {
  const auto r = __builtin_amdgcn_permlane16_swap(asu(a), asu(b), false, false);
  return asf(r[0]) + asf(r[1]);
}
__device__ __forceinline__ float xrow_max(float v) {
  const auto r = __builtin_amdgcn_permlane16_swap(asu(v), asu(v), false, false);
  v = fmaxf(asf(r[0]), asf(r[1]));
  const auto s = __builtin_amdgcn_permlane32_swap(asu(v), asu(v), false, false);
  return fmaxf(asf(s[0]), asf(s[1]));
}
__device__ __forceinline__ unsigned pack_halves(float v) {
  const auto r = __builtin_amdgcn_permlane32_swap(asu(v), asu(v), false, false);
  return __builtin_amdgcn_perm(r[1], r[0], 0x07060302u);
}
__device__ __forceinline__ float row_sum(float v) { v += dpp<ROR8>(v); v += dpp<ROR4>(v); v += dpp<ROR2>(v); v += dpp<ROR1>(v); return v; }
__device__ __forceinline__ float row_max(float v) { v = fmaxf(v, dpp<ROR8>(v)); v = fmaxf(v, dpp<ROR4>(v)); v = fmaxf(v, dpp<ROR2>(v)); return fmaxf(v, dpp<ROR1>(v)); }
__device__ __forceinline__ float wave_sum(float v) { v = row_sum(v); v = sw16_add(v, v); return sw32_add(v, v); }
__device__ __forceinline__ void rows_reduce8(const float (&a)[8], float& u0, float& u1) {
  const float r0 = sw32_add(a[0], a[4]), r1 = sw32_add(a[1], a[5]), r2 = sw32_add(a[2], a[6]), r3 = sw32_add(a[3], a[7]);
  u0 = sw16_add(r0, r2); u1 = sw16_add(r1, r3);
}
__device__ __forceinline__ void fma8(float (&a)[8], float h, v4u w) {
#pragma unroll
  for (int e = 0; e < 4; ++e) { a[2 * e] = fmaf(h, lo16(w[e]), a[2 * e]); a[2 * e + 1] = fmaf(h, hi16(w[e]), a[2 * e + 1]); }
}
__device__ __forceinline__ void kpair(v4u& a, v4u& b) {
  const v4u r0 = a, r1 = b;
  a = v4u{__builtin_amdgcn_perm(r1[0], r0[0], 0x05040100u), __builtin_amdgcn_perm(r1[0], r0[0], 0x07060302u),
          __builtin_amdgcn_perm(r1[1], r0[1], 0x05040100u), __builtin_amdgcn_perm(r1[1], r0[1], 0x07060302u)};
  b = v4u{__builtin_amdgcn_perm(r1[2], r0[2], 0x05040100u), __builtin_amdgcn_perm(r1[2], r0[2], 0x07060302u),
          __builtin_amdgcn_perm(r1[3], r0[3], 0x05040100u), __builtin_amdgcn_perm(r1[3], r0[3], 0x07060302u)};
}
__device__ __forceinline__ void dot8(float (&acc)[8], unsigned hp, v4u a, v4u b) {
#pragma unroll
  for (int e = 0; e < 4; ++e) { acc[e] = dot2(a[e], hp, acc[e]); acc[4 + e] = dot2(b[e], hp, acc[4 + e]); }
}
__device__ __forceinline__ f32x4 mfma(v4u a, v4u b, f32x4 c) {
  return __builtin_amdgcn_mfma_f32_16x16x32_bf16(__builtin_bit_cast(bf16x8, a), __builtin_bit_cast(bf16x8, b), c, 0, 0, 0);
}
typedef __amdgpu_buffer_rsrc_t rsrc_t;
__device__ __forceinline__ rsrc_t mkrsrc(const void* p, unsigned bytes) { return __builtin_amdgcn_make_buffer_rsrc((void*)p, 0, bytes, 0x00020000); }
constexpr int SC1 = 16, NT = 2;
template <int AUX> __device__ __forceinline__ v4u ld128(rsrc_t r, unsigned off) { return __builtin_amdgcn_raw_buffer_load_b128(r, off, 0, AUX); }
template <int AUX> __device__ __forceinline__ v2u ld64(rsrc_t r, unsigned off) { return __builtin_amdgcn_raw_buffer_load_b64(r, off, 0, AUX); }
template <int AUX> __device__ __forceinline__ unsigned ld32(rsrc_t r, unsigned off) { return __builtin_amdgcn_raw_buffer_load_b32(r, off, 0, AUX); }
__device__ __forceinline__ void st128(rsrc_t r, unsigned off, v4u v) { __builtin_amdgcn_raw_buffer_store_b128(v, r, off, 0, SC1); }
__device__ __forceinline__ void st64(rsrc_t r, unsigned off, v2u v) { __builtin_amdgcn_raw_buffer_store_b64(v, r, off, 0, SC1); }
__device__ __forceinline__ void st32(rsrc_t r, unsigned off, unsigned v) { __builtin_amdgcn_raw_buffer_store_b32(v, r, off, 0, SC1); }
__device__ __forceinline__ unsigned umax(unsigned a, unsigned b) { return a > b ? a : b; }
__device__ __forceinline__ unsigned mx4(unsigned m, v4u v) { return umax(umax(umax(m, v[0]), v[1]), umax(v[2], v[3])); }
__device__ __forceinline__ unsigned mx2(unsigned m, v2u v) { return umax(umax(m, v[0]), v[1]); }
__device__ __forceinline__ v4u noempty4(f32x4 v) { return v4u{noempty(asu(v[0])), noempty(asu(v[1])), noempty(asu(v[2])), noempty(asu(v[3]))}; }
struct Tag { unsigned lo, hi; };
template <int LB> __device__ __forceinline__ unsigned tg_hi(Tag t) { return LB ? __builtin_amdgcn_alignbit(t.hi, t.lo, LB) : t.lo; }
template <int LB> __device__ __forceinline__ unsigned gr_lo(float v, Tag t) { return ((asu(v) + ((1u << LB) >> 1)) & (~0u << LB)) | (t.lo & ((1u << LB) - 1u)); }
template <int LB> __device__ __forceinline__ v2u gr1(float a, Tag t) { return v2u{gr_lo<LB>(a, t), tg_hi<LB>(t)}; }
template <int LB> __device__ __forceinline__ v4u gr2(float a, float b, Tag t) { return v4u{gr_lo<LB>(a, t), tg_hi<LB>(t), gr_lo<LB>(b, t), tg_hi<LB>(t)}; }
template <int LB> __device__ __forceinline__ unsigned gr_bad(v4u g, Tag t) {
  return (((g[0] ^ t.lo) | (g[2] ^ t.lo)) & ((1u << LB) - 1u)) | (g[1] ^ tg_hi<LB>(t)) | (g[3] ^ tg_hi<LB>(t));
}
template <int LB> __device__ __forceinline__ float gr_val(unsigned lo) { return asf(lo & (~0u << LB)); }
template <int LB> __device__ __forceinline__ float gr_sum(v4u g) { const float s = sw32_add(gr_val<LB>(g[0]), gr_val<LB>(g[2])); return sw16_add(s, s); }
__device__ __forceinline__ Tag next_tag(v2u ep) { const u64 e = (((u64)ep[1] << 32) | ep[0]) + 1ull; return Tag{(unsigned)e, (unsigned)(e >> 32)}; }
__device__ __forceinline__ v4u sel4(bool t, v4u a, v4u b) { return v4u{t ? a[0] : b[0], 0u, t ? a[2] : b[2], 0u}; }
#define MEMFENCE() asm volatile("" ::: "memory")
#define LGKM0() asm volatile("s_waitcnt lgkmcnt(0)" ::: "memory")
#define VM0() asm volatile("s_waitcnt vmcnt(0)" ::: "memory")
#define PIN4(a) asm volatile("" : "+v"((a)[0]), "+v"((a)[1]), "+v"((a)[2]), "+v"((a)[3]))
template <int N> __device__ __forceinline__ void vmwait() { asm volatile("s_waitcnt vmcnt(%0)" ::"n"(N) : "memory"); }
__device__ __forceinline__ void wg_barrier() { LGKM0(); __builtin_amdgcn_s_barrier(); MEMFENCE(); }
template <typename T> __device__ __forceinline__ volatile LAS T* lds_at(unsigned off) { return (volatile LAS T*)(size_t)off; }
template <typename T> __device__ __forceinline__ LAS T* lds_pl(unsigned off) { return (LAS T*)(size_t)off; }
__device__ __forceinline__ void lds_wait_ge(unsigned addr, unsigned need, u64 t0) {
  for (unsigned sp = 0; *lds_at<unsigned>(addr) < need; ++sp) { __builtin_amdgcn_s_sleep(1); if ((sp & 63u) == 63u && rt() - t0 > TLIMIT) break; }
  MEMFENCE();
}

#define EXPIRED(it) (((it) & 63u) == 63u && rt() - t0 > TLIMIT)

__device__ __forceinline__ void dma4(v4i srd, unsigned soff, unsigned ldsb, unsigned v0, unsigned v1, unsigned v2, unsigned v3) {
  asm volatile("s_mov_b32 m0, %0\n\ts_nop 0\n\t"
               "buffer_load_dwordx4 %1, %5, %6 offen nt lds\n\t"
               "buffer_load_dwordx4 %2, %5, %6 offen offset:1024 nt lds\n\t"
               "buffer_load_dwordx4 %3, %5, %6 offen offset:2048 nt lds\n\t"
               "buffer_load_dwordx4 %4, %5, %6 offen offset:3072 nt lds"
               ::"s"(ldsb), "v"(v0), "v"(v1), "v"(v2), "v"(v3), "s"(srd), "s"(soff) : "memory");
}

__device__ __forceinline__ float silu_bf(float v) {
  const float t = -v * LOG2E, r = fmaf(-v, 1.9259629911e-8f, fmaf(-v, LOG2E, -t));
  const float e = __builtin_amdgcn_exp2f(t);
  return rbf(v / (1.f + (e < INFINITY ? fmaf(e * 0.6931471806f, r, e) : e)));
}
__device__ __forceinline__ void ln_round(float mean, float var, float& mb, float& sd) {
  mb = rbf(mean);
  sd = rbf(__builtin_amdgcn_sqrtf(rbf(rbf(var) + 1e-5f)));
}
__device__ __forceinline__ float mod_round(float n, float sc, float sh) { return rbf(rbf(rbf(n) * rbf(1.f + sc)) + sh); }

__global__ void __launch_bounds__(NTHR, 2) block_decode(Args a) {
  extern __shared__ __attribute__((aligned(16))) unsigned char smem[];
  const int tid = threadIdx.x, lane = tid & 63, wave = __builtin_amdgcn_readfirstlane(tid >> 6);
  const int b = blockIdx.x, x = b & 7, j = b >> 3;
  const u64 t0 = rt();
  const unsigned lb = (unsigned)(size_t)(LAS unsigned char*)smem;
  const rsrc_t ws = mkrsrc(a.ws, WS_END);
  const int kq = j >> 3, cb = x + 8 * (j & 7);
  const int gq = j >> 2;
  const bool mlp = (x & 1) == 0;
  const int og = (x & 6) + (j >> 4), oc0 = 32 * (j & 15);
  const bool has_gu = mlp && 4 * gq + (x >> 1) < 28;
  const int up = 4 * (4 * gq + (x >> 1)) + (j & 3);
  const int fc0 = 64 * x + 4 * j;

  if (tid < 20) *lds_pl<unsigned>(lb + L_CTL + 4 * tid) = (tid == 1 || tid == 3) ? (unsigned)INF : 0u;
  asm volatile("" :: "s"(a.K), "s"(a.ws));
  wg_barrier();
  int w_a = wave, w_b = wave;
  asm volatile("" : "+s"(w_a));
  if (w_a == 4) {
    const u64 kb = (u64)(a.K + (size_t)j * SK * DH), vb = (u64)(a.V + (size_t)j * SK * DH);
    const v4i ksrd = {(int)kb, (int)(kb >> 32), (int)HEAD_BYTES, 0x00020000}, vsrd = {(int)vb, (int)(vb >> 32), (int)HEAD_BYTES, 0x00020000};
    const unsigned g = lane >> 4, c = lane & 15;
    const unsigned k0 = g * 256 + ((c ^ g) << 4), k1 = g * 256 + ((c ^ (4 + g)) << 4), k2 = g * 256 + ((c ^ (8 + g)) << 4), k3 = g * 256 + ((c ^ (12 + g)) << 4);
    const unsigned vl = lane * 16;
    unsigned sl = lb + L_RING;
    auto fill = [&](v4i srd, int n, unsigned v0, unsigned v1, unsigned v2, unsigned v3) {
      const unsigned o = ((unsigned)n >> 2) * 65536u + ((unsigned)n & 3u) * 8192u;
      dma4(srd, o, sl, v0, v1, v2, v3);
      dma4(srd, o + 4096, sl + 4096, v0, v1, v2, v3);
      dma4(srd, o + 32768, sl + 8192, v0, v1, v2, v3);
      dma4(srd, o + 36864, sl + 12288, v0, v1, v2, v3);
      sl = sl + SLOT == lb + L_RING + NSLOT * SLOT ? lb + L_RING : sl + SLOT;
    };
    volatile LAS u64* FT = lds_at<u64>(lb + L_FT);
    int pub = 0;
    unsigned total = (unsigned)INF;
    auto publish = [&](int n) { pub = n > pub ? n : pub; *FT = (u64)(unsigned)pub | ((u64)total << 32); };
    auto wait_free = [&](int i) {
      const int p = i - NSLOT;
      volatile LAS u64* dn = lds_at<u64>(lb + L_DONE + 8 * (p & 1));
      const unsigned need = (unsigned)(p >> 1) + 1u;
      auto freed = [&]() { const u64 d = *dn; return (unsigned)d >= need && (unsigned)(d >> 32) >= need; };
      if (!freed()) {
        VM0(); publish(i);
        for (unsigned sp = 0; !freed(); ++sp) { __builtin_amdgcn_s_sleep(1); if (EXPIRED(sp)) break; }
      }
      MEMFENCE();
    };
    for (unsigned sp = 0; *lds_at<unsigned>(lb + L_ISSUED) < 5u && sp < 4096u; ++sp) {}
    auto pace = []() { vmwait<PW>(); __builtin_amdgcn_s_sleep(PG); };
    unsigned tn = 1;
    auto take = [&]() {
      u64 tv;
      for (unsigned sp = 0; (unsigned)((tv = *lds_at<u64>(lb + L_TK)) >> 32) < tn; ++sp) if (EXPIRED(sp)) { tv = EMPTY; break; }
      ++tn;
      return (unsigned)__builtin_amdgcn_readfirstlane((unsigned)tv);
    };
    int i = 0, nt = 0, next = POOL0 + x * NP, left = NP;
    bool asked = true;
    for (;; ++i) {
      fill(ksrd, next, k0, k1, k2, k3);
      ++next; --left;
      if (i >= BURST) { vmwait<32>(); publish(i - 1); }
      if (i == NP + SO - 5) *lds_at<unsigned>(lb + L_TKC) = tn;
      if (left == 0) {
        if (i == NP - 1) { next = x * SO; left = SO; }
        else {
          const unsigned t = asked ? take() : EMPTY;
          if (t >= (unsigned)NKT) break;
          left = 4; next = KD0 + 4 * (int)t;
          *lds_at<unsigned>(lb + L_LIST + 4 * nt) = t; ++nt;
          asked = i + 1 - NP + left + 4 <= CAP;
          if (asked) *lds_at<unsigned>(lb + L_TKC) = tn;
        }
      }
      if (i + 1 >= NSLOT) wait_free(i + 1);
      if (i + 1 >= PFROM) pace(); else if (i + 1 >= BURST) vmwait<16>();
    }
    const int nk = i + 1, vown = 2 * nk - NP;
    *lds_at<unsigned>(lb + L_NK) = (unsigned)nk;
    wait_free(i + 1);
    pace();
    next = x * SO; left = SO;
    int rt_ = 0;
    for (++i;; ++i) {
      fill(vsrd, next, vl, vl, vl, vl);
      ++next; --left;
      vmwait<32>();
      publish(i - 1);
      if (i == vown - 3) *lds_at<unsigned>(lb + L_TKC) = tn;
      if (left == 0) {
        if (rt_ < nt) { const unsigned t = __builtin_amdgcn_readfirstlane(*lds_at<unsigned>(lb + L_LIST + 4 * rt_)); ++rt_; left = 4; next = KD0 + 4 * (int)t; }
        else {
          const unsigned t = take();
          if (t >= (unsigned)NPT) break;
          *lds_at<unsigned>(lb + L_TKC) = tn;
          left = 2; next = POOL0 + 2 * (int)t;
        }
      }
      wait_free(i + 1);
      pace();
    }
    total = (unsigned)i + 1u;
    publish(i - 1);
    vmwait<16>(); publish(i);
    VM0(); publish(i + 1);
    return;
  }

  asm volatile("" : "+s"(w_b));
  if (w_b == 5) {
    __builtin_amdgcn_s_setprio(3);
    const rsrc_t rwm = mkrsrc(a.wm, DM * 3072 * 2), rc = mkrsrc(a.c, DM * 2);
    const int n0 = 512 * (gq & 1) + 64 * x + 16 * (j & 3);
    const int ar = 128 * kq + 4 * (lane >> 1);
    const v2u c4 = ld64<0>(rc, ar * 2);
    MEMFENCE();
    v4u w1[4];
#pragma unroll
    for (int i = 0; i < 4; ++i) w1[i] = ld128<NT>(rwm, (unsigned)((ar + i) * 3072 + n0 + 8 * (lane & 1)) * 2);
    const v2u ep = ld64<SC1>(ws, WS_EPOCH);
    const int mr = 64 * gq + 8 * (lane >> 3), mc = 64 * (x + 8 * (j & 3));
    const v4u cv = ld128<0>(rc, mr * 2);
    MEMFENCE();
    if (lane == 0) __hip_atomic_fetch_add(lds_pl<unsigned>(lb + L_ISSUED), 1u, __ATOMIC_RELAXED, __HIP_MEMORY_SCOPE_WORKGROUP);
    st128(ws, (lane < 8 && !mlp) ? WS_YP + (og * 512 + oc0) * 4 + lane * 16 : OOB, v4u{EMPTY, EMPTY, EMPTY, EMPTY});
    st128(ws, has_gu ? WS_DP + up * 2048 + lane * 16 : OOB, v4u{EMPTY, EMPTY, EMPTY, EMPTY});
    st128(ws, has_gu ? WS_DP + up * 2048 + 1024 + lane * 16 : OOB, v4u{EMPTY, EMPTY, EMPTY, EMPTY});
    st128(ws, lane < 33 ? WS_PO + b * REC + lane * 16 : OOB, v4u{EMPTY, EMPTY, EMPTY, EMPTY});
    st32(ws, (lane < 2 && x == 0) ? WS_CTR + j * 256 + lane * 128 : OOB, 0u);
    const Tag tag = next_tag(ep);
    {
      float s8[8] = {0.f, 0.f, 0.f, 0.f, 0.f, 0.f, 0.f, 0.f};
      fma8(s8, silu_bf(lo16(c4[0])), w1[0]); fma8(s8, silu_bf(hi16(c4[0])), w1[1]);
      fma8(s8, silu_bf(lo16(c4[1])), w1[2]); fma8(s8, silu_bf(hi16(c4[1])), w1[3]);
      float u0, u1;
      rows_reduce8(s8, u0, u1);
      u0 += dpp<ROR8>(u0); u1 += dpp<ROR8>(u1); u0 += dpp<ROR4>(u0); u1 += dpp<ROR4>(u1); u0 += dpp<XOR2>(u0); u1 += dpp<XOR2>(u1);
      const int col = n0 + 8 * (lane & 1) + 2 * (lane >> 4);
      st128(ws, (lane & 15) < 2 ? WS_P1 + (kq * 1024 + col) * 8 : OOB, gr2<0>(u0, u1, tag));
      *lds_at<unsigned>(lb + L_AUXPUB) = 1u;
    }
    __builtin_amdgcn_s_setprio(0);
    auto stream_at = [&](unsigned fillno) {
      for (unsigned sp = 0; *lds_at<unsigned>(lb + L_FT) < fillno; ++sp) { __builtin_amdgcn_s_sleep(8); if ((sp & 63u) == 63u && rt() - t0 > TLIMIT) break; }
      MEMFENCE();
    };
    stream_at(NSLOT + 4);
    {
      v4u w2[8];
#pragma unroll
      for (int i = 0; i < 8; ++i) w2[i] = ld128<NT>(rwm, (unsigned)((mr + i) * 3072 + 1024 + mc + 8 * (lane & 7)) * 2);
      float s8[8] = {0.f, 0.f, 0.f, 0.f, 0.f, 0.f, 0.f, 0.f};
#pragma unroll
      for (int e = 0; e < 4; ++e) { fma8(s8, silu_bf(lo16(cv[e])), w2[2 * e]); fma8(s8, silu_bf(hi16(cv[e])), w2[2 * e + 1]); }
      float u0, u1;
      rows_reduce8(s8, u0, u1);
      u0 += dpp<ROR8>(u0); u1 += dpp<ROR8>(u1);
      st128(ws, (lane & 8) == 0 ? WS_M2P + (gq * 2048 + mc + 8 * (lane & 7) + 2 * (lane >> 4)) * 8 : OOB, gr2<4>(u0, u1, tag));
    }
    stream_at(NSLOT + 16);
    {
      v4u m;
      for (;;) {
        MEMFENCE();
        m = ld128<SC1>(ws, WS_M2P + (((lane >> 2) & 7) * 2048 + 8 * b + 2 * (lane & 3)) * 8);
        if (__all(gr_bad<4>(m, tag) == 0u) || rt() - t0 > TLIMIT) break;
        __builtin_amdgcn_s_sleep(32);
      }
      float u0 = gr_val<4>(m[0]), u1 = gr_val<4>(m[2]);
      u0 = sw16_add(u0, u0); u1 = sw16_add(u1, u1);
      u0 += dpp<ROR8>(u0); u1 += dpp<ROR8>(u1); u0 += dpp<ROR4>(u0); u1 += dpp<ROR4>(u1);
      VM0();
      st128(ws, lane < 4 ? WS_M2 + b * 64 + lane * 16 : OOB, gr2<4>(rbf(u0), rbf(u1), tag));
    }
    stream_at(NSLOT + 28);
#pragma unroll 1
    for (int u = 0; u < 16; ++u) {
      v4u m;
      for (;;) {
        MEMFENCE();
        m = ld128<SC1>(ws, WS_M2 + u * 1024 + lane * 16);
        if (__all(gr_bad<4>(m, tag) == 0u) || rt() - t0 > TLIMIT) break;
        __builtin_amdgcn_s_sleep(32);
      }
      *lds_pl<unsigned>(lb + L_M2 + u * 256 + lane * 4) = __builtin_amdgcn_perm(m[2], m[0], 0x07060302u);
      __builtin_amdgcn_s_sleep(16);
    }
    LGKM0();
    *lds_at<unsigned>(lb + L_M2RDY) = 1u;
    GAS unsigned* ctr = (GAS unsigned*)(a.ws + WS_CTR + j * 256);
    unsigned n = 1;
    auto draw = [&](int which) {
      unsigned v = 0;
      if (lane == 0) v = __hip_atomic_fetch_add(ctr + 32 * which, 1u, __ATOMIC_RELAXED, __HIP_MEMORY_SCOPE_AGENT);
      v = __builtin_amdgcn_readfirstlane(v);
      *lds_at<u64>(lb + L_TK) = ((u64)n << 32) | v;
      ++n;
      return v;
    };
    for (;;) {
      bool over = false;
      for (unsigned sp = 0; *lds_at<unsigned>(lb + L_TKC) < n; ++sp) {
        if (*lds_at<unsigned>(lb + L_NK) != (unsigned)INF || EXPIRED(sp)) { over = true; break; }
        __builtin_amdgcn_s_sleep(2);
      }
      if (over || *lds_at<unsigned>(lb + L_NK) != (unsigned)INF || draw(0) >= (unsigned)NKT) break;
    }
    lds_wait_ge(lb + L_KCNT, 5u, t0);
    __builtin_amdgcn_s_sleep(127);
    {
      v2u mx;
      for (;;) {
        MEMFENCE();
        mx = ld64<SC1>(ws, WS_MX + j * 64 + (lane & 7) * 8);
        if (__all(gr_bad<4>(v4u{mx[0], mx[1], mx[0], mx[1]}, tag) == 0u) || rt() - t0 > TLIMIT) break;
        __builtin_amdgcn_s_sleep(32);
      }
      *lds_at<float>(lb + L_MH) = row_max(gr_val<4>(mx[0]));
      LGKM0();
    }
    {
      unsigned dq = 0, pend = 0;
      v4u sc = {0u, 0u, 0u, 0u};
      auto flush = [&]() {
        if ((unsigned)lane < 8u * pend) *lds_pl<v4u>(lb + L_SC + dq * 128 + lane * 16) = sc;
        dq += pend; pend = 0;
        LGKM0();
        *lds_at<unsigned>(lb + L_SCR) = dq;
      };
      for (;;) {
        if (__builtin_amdgcn_readfirstlane(*lds_at<unsigned>(lb + L_TKC)) < n) flush();
        for (unsigned sp = 0; *lds_at<unsigned>(lb + L_TKC) < n; ++sp) { __builtin_amdgcn_s_sleep(2); if (EXPIRED(sp)) break; }
        const unsigned v = draw(1);
        flush();
        if (v >= (unsigned)NPT || rt() - t0 > TLIMIT) break;
        pend = 2u;
        sc = ld128<SC1 | NT>(ws, lane < 16 ? WS_SG + (j * 8 * NP + 2 * v) * 128 + lane * 16 : OOB);
        MEMFENCE();
      }
    }
    return;
  }

  const int g = lane >> 4, c = lane & 15;
  const rsrc_t rx = mkrsrc(a.x, DM * 2), rwq = mkrsrc(a.wq, DM * 4096 * 2);
  const rsrc_t rwd = mkrsrc(a.wd, DF * DM * 2);
  const int r0 = 128 * kq + 32 * wave;
  v4u wqv[4];
#pragma unroll
  for (int i = 0; i < 4; ++i) wqv[i] = ld128<NT>(rwq, (unsigned)((r0 + 4 * (lane >> 3) + i) * 4096 + 64 * cb + 8 * (lane & 7)) * 2);
  const v2u ep = ld64<SC1>(ws, WS_EPOCH);
  const v4u x8 = ld128<0>(rx, lane * 16);
  const unsigned x2 = ld32<0>(rx, tid * 4);
  const unsigned xh = ld32<0>(rx, (r0 + 2 * c) * 2);
  MEMFENCE();
  if (lane == 0) __hip_atomic_fetch_add(lds_pl<unsigned>(lb + L_ISSUED), 1u, __ATOMIC_RELAXED, __HIP_MEMORY_SCOPE_WORKGROUP);
  float mb, sd;
  {
    float xs[8];
#pragma unroll
    for (int e = 0; e < 4; ++e) { xs[2 * e] = lo16(x8[e]); xs[2 * e + 1] = hi16(x8[e]); }
    float s = 0.f;
#pragma unroll
    for (int e = 0; e < 8; ++e) s += xs[e];
    const float mean = wave_sum(s) * (1.f / 512.f);
    float v = 0.f;
#pragma unroll
    for (int e = 0; e < 8; ++e) { const float d = xs[e] - mean; v = fmaf(d, d, v); }
    ln_round(mean, wave_sum(v) * (1.f / 512.f), mb, sd);
  }
  float nrow = rbf((lane >= 32 ? hi16(xh) : lo16(xh)) - mb) / sd;
  kpair(wqv[0], wqv[1]); kpair(wqv[2], wqv[3]);
  PIN4(wqv); asm volatile("" : "+v"(nrow));
  const Tag tag = next_tag(ep);
  lds_wait_ge(lb + L_AUXPUB, 1u, t0);
  {
    const unsigned o1 = WS_P1 + (g * 1024 + r0 + 2 * c) * 8;
    v4u gs = {0u, 0u, 0u, 0u}, gc = gs;
    bool bad = true;
    __builtin_amdgcn_s_sleep(32);
    for (;;) {
      MEMFENCE();
      const v4u ns_ = ld128<SC1>(ws, bad ? o1 : OOB), nc_ = ld128<SC1>(ws, bad ? o1 + 4096 : OOB);
      const bool tk = bad & ((gr_bad<0>(ns_, tag) | gr_bad<0>(nc_, tag)) == 0u);
      gs = sel4(tk, ns_, gs); gc = sel4(tk, nc_, gc); bad = bad & !tk;
      if (!__any(bad) || rt() - t0 > TLIMIT) break;
    }
    const unsigned hp = pack_halves(mod_round(nrow, rbf(gr_sum<0>(gc)), rbf(gr_sum<0>(gs))));
    float acc[8] = {0.f, 0.f, 0.f, 0.f, 0.f, 0.f, 0.f, 0.f};
    dot8(acc, __builtin_amdgcn_ds_bpermute((lane >> 3) * 8, hp), wqv[0], wqv[1]);
    dot8(acc, __builtin_amdgcn_ds_bpermute((lane >> 3) * 8 + 4, hp), wqv[2], wqv[3]);
    float u0, u1;
    rows_reduce8(acc, u0, u1);
    u0 += dpp<ROR8>(u0); u1 += dpp<ROR8>(u1);
    if ((lane & 8) == 0) *lds_pl<f32x2>(lb + L_RED + wave * 256 + (8 * (lane & 7) + 2 * g) * 4) = f32x2{u0, u1};
    LGKM0();
    unsigned old = 0;
    if (lane == 0) old = __hip_atomic_fetch_add(lds_pl<unsigned>(lb + L_QCNT), 1u, __ATOMIC_RELAXED, __HIP_MEMORY_SCOPE_WORKGROUP);
    old = __builtin_amdgcn_readfirstlane(old);
    MEMFENCE();
    if (old == 3u) {
      const unsigned ra = lb + L_RED + lane * 4;
      const float qp = (*lds_pl<float>(ra) + *lds_pl<float>(ra + 256)) + (*lds_pl<float>(ra + 512) + *lds_pl<float>(ra + 768));
      st64(ws, WS_QS + (kq * 4096 + 64 * cb + lane) * 8, gr1<2>(qp, tag));
    }
  }
  const int r8 = 8 * (tid >> 2);
  v4u wv[8];
  unsigned wdv[16];
#pragma unroll
  for (int i = 0; i < 8; ++i) { wv[i] = v4u{0u, 0u, 0u, 0u}; wdv[2 * i] = 0u; wdv[2 * i + 1] = 0u; }
  const GAS v4u* wp = (const GAS v4u*)(mlp ? ((tid & 2) ? a.wu : a.wg) + (r8 * DF + 16 * (has_gu ? up : 0) + 8 * (tid & 1)) : a.wo + ((512 * og + r8) * DM + oc0 + 8 * (tid & 3)));
  const int wstride = mlp ? DF / 8 : DM / 8;
  const unsigned wdo = has_gu ? (unsigned)(16 * up * DM + 2 * tid) * 2 : OOB;

  const unsigned key = 4 * (c & 3) + (c >> 2);
  unsigned ka[4], msk[4];
#pragma unroll
  for (int st = 0; st < 4; ++st) {
    ka[st] = lb + L_RING + (wave & 1) * 8192 + key * 256 + (((st + 4 * g) ^ key) << 4);
    msk[st] = ((c & 7) >> 1) == st ? ((c & 1) ? 0xffff0000u : 0x0000ffffu) : 0u;
  }
  const unsigned va = lb + L_RING + (wave & 1) * 8192 + lane * 16;
  const bool is_hi = c < 8;
  volatile LAS unsigned* DN = lds_at<unsigned>(lb + L_DONE + 4 * wave);
  unsigned so = (wave >> 1) * SLOT;
  v4u sv[8], sw[8], ud[8], qf[4];
#pragma unroll
  for (int i = 0; i < 8; ++i) { sv[i] = v4u{0u, 0u, 0u, 0u}; sw[i] = sv[i]; ud[i] = sv[i]; }
  int ns = 0;
  auto release = [&](v4u (&d)[8], int i) {
    LGKM0();
    *DN = (unsigned)(i >> 1) + 1u;
    asm volatile("" : "+v"(d[0]), "+v"(d[1]), "+v"(d[2]), "+v"(d[3]), "+v"(d[4]), "+v"(d[5]), "+v"(d[6]), "+v"(d[7]) :: "memory");
    so = (so + 2 * SLOT) & (NSLOT * SLOT - 1);
  };
  auto read_k = [&](v4u (&d)[8], int i) {
#pragma unroll
    for (int st = 0; st < 4; ++st) { d[st] = *lds_pl<v4u>(ka[st] + so); d[4 + st] = *lds_pl<v4u>(ka[st] + so + 4096); }
    release(d, i);
  };
  int total = INF, nk = INF;
  auto wait_full = [&](int i, bool in_k) {
    for (unsigned sp = 0;; ++sp) {
      const v4u ct = *(volatile LAS v4u*)(size_t)(lb + L_FT);
      total = __builtin_amdgcn_readfirstlane((int)ct[1]);
      nk = __builtin_amdgcn_readfirstlane((int)ct[3]);
      if (EXPIRED(sp)) { total = 0; nk = nk < i ? nk : i; break; }
      if (__builtin_amdgcn_readfirstlane((int)ct[0]) > i || (in_k && nk <= i)) break;
      if (total == INF) __builtin_amdgcn_s_sleep(1);
    }
    MEMFENCE();
  };
  float mraw = -INFINITY;
  auto scores = [&](const v4u (&d)[8], int i) {
    f32x4 sa = {0.f, 0.f, 0.f, 0.f}, sb = {0.f, 0.f, 0.f, 0.f};
#pragma unroll
    for (int st = 0; st < 4; ++st) { sa = mfma(d[st], qf[st], sa); sb = mfma(d[4 + st], qf[st], sb); }
    mraw = fmaxf(fmaxf(fmaxf(mraw, sa[0]), fmaxf(sa[1], sa[2])), fmaxf(fmaxf(sa[3], sb[0]), fmaxf(fmaxf(sb[1], sb[2]), sb[3])));
    v4u pk = {cvtpk(sa[0], sa[1]), cvtpk(sa[2], sa[3]), cvtpk(sb[0], sb[1]), cvtpk(sb[2], sb[3])};
#pragma unroll
    for (int u = 0; u < 4; ++u) pk[u] = cvtpk(lo16(pk[u]) * SCALE, hi16(pk[u]) * SCALE);
    const unsigned so_ = (wave & 1) * 64 + g * 16;
    const unsigned dst = i < NP ? L_SH + i * 128 : L_SC + (i - NP) * 128;
    if (c == 0) *lds_pl<v4u>(lb + dst + so_) = pk;
  };
  auto stash = [&]() {
    const int full = __builtin_amdgcn_readfirstlane((int)*lds_at<unsigned>(lb + L_FT)), i0 = wave >> 1;
    MEMFENCE();
    if (ns == 0 && full > i0) { read_k(wv, i0); ns = 1; }
    if (ns == 1 && full > i0 + 2) { read_k(sv, i0 + 2); ns = 2; }
    if (ns == 2 && full > i0 + 4) { read_k(sw, i0 + 4); ns = 3; }
    if (ns == 3 && full > i0 + 6) { read_k(ud, i0 + 6); ns = 4; }
  };
  {
    const unsigned o2 = WS_QS + (g * 4096 + 128 * j + 32 * wave + 2 * c) * 8;
    v4u gq4 = {0u, 0u, 0u, 0u};
    bool bad = true;
    stash();
    {
      const v4u nq = ld128<SC1>(ws, o2);
      bad = gr_bad<2>(nq, tag) != 0u;
      gq4 = nq;
    }
    asm volatile("" : "+v"(gq4) :: "memory");
    stash();
    for (unsigned it = 0; __any(bad) && !EXPIRED(it); ++it) {
      MEMFENCE();
      const v4u nq = ld128<SC1>(ws, bad ? o2 : OOB);
      const bool tk = bad & (gr_bad<2>(nq, tag) == 0u);
      gq4 = sel4(tk, nq, gq4); bad = bad & !tk;
    }
    *lds_pl<unsigned>(lb + L_QSC + (16 * wave + c) * 4) = pack_halves(rbf(gr_sum<2>(gq4)));
    LGKM0();
    if (lane == 0) __hip_atomic_fetch_add(lds_pl<unsigned>(lb + L_QIN), 1u, __ATOMIC_RELAXED, __HIP_MEMORY_SCOPE_WORKGROUP);
    lds_wait_ge(lb + L_QIN, 4u, t0);
#pragma unroll
    for (int st = 0; st < 4; ++st) qf[st] = *lds_pl<v4u>(lb + L_QSC + (st + 4 * g) * 16);
  }
  if (ns >= 1) scores(wv, wave >> 1);
  if (ns >= 2) scores(sv, (wave >> 1) + 2);
  if (ns >= 3) scores(sw, (wave >> 1) + 4);
  if (ns >= 4) scores(ud, (wave >> 1) + 6);
  int i = (wave >> 1) + 2 * ns;
  for (;; i += 2) {
    wait_full(i, true);
    if (nk <= i) break;
    read_k(ud, i);
    scores(ud, i);
  }
  float m2;
  {
    *lds_pl<float>(lb + L_MXW + wave * 4) = rbf(rbf(xrow_max(mraw)) * SCALE);
    LGKM0();
    unsigned old = 0;
    if (lane == 0) old = __hip_atomic_fetch_add(lds_pl<unsigned>(lb + L_KCNT), 1u, __ATOMIC_RELAXED, __HIP_MEMORY_SCOPE_WORKGROUP);
    old = __builtin_amdgcn_readfirstlane(old);
    lds_wait_ge(lb + L_KCNT, 4u, t0);
    const f32x4 mw = *lds_pl<f32x4>(lb + L_MXW);
    const float mg = fmaxf(fmaxf(mw[0], mw[1]), fmaxf(mw[2], mw[3]));
    if (old == 3u) {
#pragma unroll
      for (int u = 0; u < (NP + 7) / 8; ++u)
        __builtin_amdgcn_raw_buffer_store_b128(*lds_pl<v4u>(lb + L_SH + u * 1024 + lane * 16), ws,
                                               u * 1024 + lane * 16 < NP * 128 ? WS_SG + b * NP * 128 + u * 1024 + lane * 16 : OOB, 0, SC1 | NT);
      VM0();
      st64(ws, lane == 0 ? WS_MX + b * 8 : OOB, gr1<4>(mg, tag));
      *lds_at<unsigned>(lb + L_KCNT) = 5u;
    }
    m2 = mg * LOG2E;
  }
  float lsum = 0.f;
  f32x4 acc = {0.f, 0.f, 0.f, 0.f};
  {
    int wk = (mlp && !has_gu) ? 11 : 0;
    bool done = false, own = true;
    for (;; i += 2) {
      int e = i - nk;
      if (!done && e >= nk - NP) {
        e -= nk - NP;
        for (unsigned sp = 0;; ++sp) {
          const v4u ct = *(volatile LAS v4u*)(size_t)(lb + L_FT);
          if (__builtin_amdgcn_readfirstlane((int)ct[2]) > e) break;
          if (__builtin_amdgcn_readfirstlane((int)ct[1]) <= i || EXPIRED(sp)) { done = true; break; }
          __builtin_amdgcn_s_sleep(1);
        }
        MEMFENCE();
        if (own) {
          const float mh = *lds_at<float>(lb + L_MH) * LOG2E, al = __builtin_amdgcn_exp2f(m2 - mh);
          acc *= al; lsum *= al; m2 = mh; own = false;
        }
      }
      if (!done) {
        const v4u sc = *lds_pl<v4u>(lb + L_SC + e * 128 + (wave & 1) * 64 + g * 16);
        v4u pa[8];
#pragma unroll
        for (int u = 0; u < 8; ++u) {
          const float p = __builtin_amdgcn_exp2f(fmaf((u & 1) ? hi16(sc[u >> 1]) : lo16(sc[u >> 1]), LOG2E, -m2));
          lsum += p;
          const float ph = hi16(asu(p));
          const float xv = is_hi ? ph : p - ph;
          const unsigned pk = cvtpk(xv, xv);
          pa[u] = v4u{pk & msk[0], pk & msk[1], pk & msk[2], pk & msk[3]};
        }
        asm volatile("" : "+v"(pa[0]), "+v"(pa[1]), "+v"(pa[2]), "+v"(pa[3]), "+v"(pa[4]), "+v"(pa[5]), "+v"(pa[6]), "+v"(pa[7]));
        wait_full(i, false);
#pragma unroll
        for (int u = 0; u < 8; ++u) ud[u] = *lds_pl<v4u>(va + so + u * 1024);
        release(ud, i);
#pragma unroll
        for (int u = 0; u < 8; ++u) acc = mfma(pa[u], ud[u], acc);
        done = i + 2 >= total && !own;
      }
      MEMFENCE();
      if (wk <= 10 && (done || rt() - t0 >= (u64)(TW + TD * wk + (wk == 10 ? 400 : 0)))) {
#pragma unroll
        for (int u = 0; u < 8; ++u) if (wk == u) wv[u] = __builtin_nontemporal_load(wp + u * wstride);
#pragma unroll
        for (int v = 0; v < 2; ++v) if (wk == 8 + v) {
#pragma unroll
          for (int u = 8 * v; u < 8 * v + 8; ++u) wdv[u] = ld32<NT>(rwd, wdo + u * (DM * 2));
        }
        if (wk == 10) {
#pragma unroll
          for (int u = 0; u < 8; u += 2) kpair(wv[u], wv[u + 1]);
#pragma unroll
          for (int u = 0; u < 16; u += 2) {
            const unsigned lo = __builtin_amdgcn_perm(wdv[u + 1], wdv[u], 0x05040100u), hi = __builtin_amdgcn_perm(wdv[u + 1], wdv[u], 0x07060302u);
            wdv[u] = lo; wdv[u + 1] = hi;
          }
        }
        ++wk;
      }
      MEMFENCE();
      if (done && wk > 10) break;
    }
  }
  {
    f32x4 o;
#pragma unroll
    for (int r = 0; r < 4; ++r) o[r] = sw32_add(acc[r], acc[r]);
    float l = sw16_add(lsum, lsum); l = sw32_add(l, l);
    *lds_pl<f32x4>(lb + L_MRG + wave * 512 + (8 * c + 4 * (g & 1)) * 4) = o;
    *lds_pl<float>(lb + L_LW + wave * 4) = l;
    wg_barrier();
    if (wave == 0) {
      const unsigned ra = lb + L_MRG + (lane & 31) * 16;
      const f32x4 lw = *lds_pl<f32x4>(lb + L_LW);
      f32x4 O = (*lds_pl<f32x4>(ra) + *lds_pl<f32x4>(ra + 512)) + (*lds_pl<f32x4>(ra + 1024) + *lds_pl<f32x4>(ra + 1536));
      if (lane >= 32) O = f32x4{(lw[0] + lw[1]) + (lw[2] + lw[3]), 0.f, 0.f, 0.f};
      st128(ws, lane < 33 ? WS_PO + b * REC + lane * 16 : OOB, noempty4(O));
    }
  }
  if (!mlp) {
    const unsigned hb = WS_PO + (4 * og + wave) * 8 * REC;
    v2u po[8];
    unsigned ls = EMPTY;
#pragma unroll
    for (int s = 0; s < 8; ++s) po[s] = v2u{0u, 0u};
    unsigned miss = 0x1FFu;
    for (unsigned it = 0;; ++it) {
      MEMFENCE();
      if (miss & 0x100u) ls = ld32<SC1>(ws, (lane & 15) < 8 ? hb + (lane & 15) * REC + 512 : OOB);
#pragma unroll
      for (int s = 0; s < 8; ++s) if (miss & (1u << s)) po[s] = ld64<SC1>(ws, hb + s * REC + lane * 8);
      if ((miss & 0x100u) && __all(ls != EMPTY)) miss &= ~0x100u;
#pragma unroll
      for (int s = 0; s < 8; ++s) if ((miss & (1u << s)) && __all(mx2(0u, po[s]) != EMPTY)) miss &= ~(1u << s);
      if (miss == 0u || EXPIRED(it)) break;
    }
    const float inv = __builtin_amdgcn_rcpf(row_sum(asf(ls)));
    const unsigned op = cvtpk((((asf(po[0][0]) + asf(po[1][0])) + (asf(po[2][0]) + asf(po[3][0]))) + ((asf(po[4][0]) + asf(po[5][0])) + (asf(po[6][0]) + asf(po[7][0])))) * inv,
                              (((asf(po[0][1]) + asf(po[1][1])) + (asf(po[2][1]) + asf(po[3][1]))) + ((asf(po[4][1]) + asf(po[5][1])) + (asf(po[6][1]) + asf(po[7][1])))) * inv);
    float y[8] = {0.f, 0.f, 0.f, 0.f, 0.f, 0.f, 0.f, 0.f};
    dot8(y, dppu<0x00>(op), wv[0], wv[1]); dot8(y, dppu<0x55>(op), wv[2], wv[3]);
    dot8(y, dppu<0xAA>(op), wv[4], wv[5]); dot8(y, dppu<0xFF>(op), wv[6], wv[7]);
    float u0, u1;
    rows_reduce8(y, u0, u1);
    u0 += dpp<ROR8>(u0); u1 += dpp<ROR8>(u1); u0 += dpp<ROR4>(u0); u1 += dpp<ROR4>(u1);
    *lds_pl<f32x2>(lb + L_RED3 + wave * 128 + (8 * (c & 3) + 2 * g) * 4) = f32x2{u0, u1};
    wg_barrier();
    if (wave == 0) {
      const unsigned ra = lb + L_RED3 + (lane & 7) * 16;
      const f32x4 t = (*lds_pl<f32x4>(ra) + *lds_pl<f32x4>(ra + 128)) + (*lds_pl<f32x4>(ra + 256) + *lds_pl<f32x4>(ra + 384));
      st128(ws, lane < 8 ? WS_YP + (og * 512 + oc0) * 4 + lane * 16 : OOB, noempty4(t));
    }
    return;
  }
  lds_wait_ge(lb + L_M2RDY, 1u, t0);
  float gbf = lo16(*lds_pl<u16>(lb + L_M2 + 3072 + (fc0 + g) * 2));
  {
    const unsigned gaw = *lds_pl<unsigned>(lb + L_M2 + tid * 4), sbw = *lds_pl<unsigned>(lb + L_M2 + 1024 + tid * 4), cbw = *lds_pl<unsigned>(lb + L_M2 + 2048 + tid * 4);
    float ga0 = lo16(gaw), ga1 = hi16(gaw), sb0 = lo16(sbw), sb1 = hi16(sbw), t0c = rbf(1.f + lo16(cbw)), t1c = rbf(1.f + hi16(cbw)), xa = lo16(x2), xb = hi16(x2);
    asm volatile("" : "+v"(ga0), "+v"(ga1), "+v"(sb0), "+v"(sb1), "+v"(t0c), "+v"(t1c), "+v"(xa), "+v"(xb), "+v"(gbf));
    v2u yp[8];
#pragma unroll
    for (int s = 0; s < 8; ++s) yp[s] = v2u{0u, 0u};
    unsigned miss = 0xFFu;
    for (unsigned it = 0;; ++it) {
      MEMFENCE();
#pragma unroll
      for (int s = 0; s < 8; ++s) if (miss & (1u << s)) yp[s] = ld64<SC1>(ws, WS_YP + s * 2048 + tid * 8);
#pragma unroll
      for (int s = 0; s < 8; ++s) if ((miss & (1u << s)) && __all(mx2(0u, yp[s]) != EMPTY)) miss &= ~(1u << s);
      if (miss == 0u || EXPIRED(it)) break;
    }
    const unsigned atp = cvtpk(((asf(yp[0][0]) + asf(yp[1][0])) + (asf(yp[2][0]) + asf(yp[3][0]))) + ((asf(yp[4][0]) + asf(yp[5][0])) + (asf(yp[6][0]) + asf(yp[7][0]))),
                               ((asf(yp[0][1]) + asf(yp[1][1])) + (asf(yp[2][1]) + asf(yp[3][1]))) + ((asf(yp[4][1]) + asf(yp[5][1])) + (asf(yp[6][1]) + asf(yp[7][1]))));
    const unsigned gap = cvtpk(ga0 * lo16(atp), ga1 * hi16(atp));
    const unsigned x1p = cvtpk(xa + lo16(gap), xb + hi16(gap));
    const float x1a = lo16(x1p), x1b = hi16(x1p);
    const float sh = asf(__builtin_amdgcn_readfirstlane(asu(x1a)));
    const float da = x1a - sh, db = x1b - sh;
    const float s1 = wave_sum(da + db), s2 = wave_sum(fmaf(da, da, db * db));
    *lds_pl<f32x2>(lb + L_ST + wave * 8) = f32x2{fmaf(s1, 1.f / 128.f, sh), fmaf(-s1 * (1.f / 128.f), s1, s2)};
    *lds_pl<f32x2>(lb + L_X1 + tid * 8) = f32x2{x1a, x1b};
    wg_barrier();
    if (has_gu) {
      const f32x4 sA = *lds_pl<f32x4>(lb + L_ST), sB = *lds_pl<f32x4>(lb + L_ST + 16);
      const float mean = ((sA[0] + sA[2]) + (sB[0] + sB[2])) * 0.25f;
      const float d0 = sA[0] - mean, d1 = sA[2] - mean, d2 = sB[0] - mean, d3 = sB[2] - mean;
      const float var = fmaxf(((sA[1] + sA[3]) + (sB[1] + sB[3])) + 128.f * ((d0 * d0 + d1 * d1) + (d2 * d2 + d3 * d3)), 0.f) * (1.f / 512.f);
      float mb2, sd2;
      ln_round(mean, var, mb2, sd2);
      const float rs = __builtin_amdgcn_rcpf(sd2);
      const unsigned e0 = cvtpk(x1a - mb2, x1b - mb2), e1 = cvtpk(lo16(e0) * rs, hi16(e0) * rs), e2 = cvtpk(lo16(e1) * t0c, hi16(e1) * t1c);
      const unsigned hq = cvtpk(lo16(e2) + sb0, hi16(e2) + sb1);
      float y[8] = {0.f, 0.f, 0.f, 0.f, 0.f, 0.f, 0.f, 0.f};
      dot8(y, dppu<0x00>(hq), wv[0], wv[1]); dot8(y, dppu<0x55>(hq), wv[2], wv[3]);
      dot8(y, dppu<0xAA>(hq), wv[4], wv[5]); dot8(y, dppu<0xFF>(hq), wv[6], wv[7]);
      float u0, u1;
      rows_reduce8(y, u0, u1);
      u0 += dpp<ROR8>(u0); u1 += dpp<ROR8>(u1); u0 += dpp<ROR4>(u0); u1 += dpp<ROR4>(u1);
      *lds_pl<f32x2>(lb + L_RED3 + wave * 128 + (8 * (c & 3) + 2 * g) * 4) = f32x2{u0, u1};
      wg_barrier();
      const unsigned ra = lb + L_RED3 + c * 4;
      const float gs = rbf((*lds_pl<float>(ra) + *lds_pl<float>(ra + 128)) + (*lds_pl<float>(ra + 256) + *lds_pl<float>(ra + 384)));
      const float us = rbf((*lds_pl<float>(ra + 64) + *lds_pl<float>(ra + 192)) + (*lds_pl<float>(ra + 320) + *lds_pl<float>(ra + 448)));
      const float z = 0.7978845608028654f * fmaf(0.044715f * gs * gs, gs, gs);
      const float th = fmaf(-2.f, __builtin_amdgcn_rcpf(1.f + __expf(2.f * z)), 1.f);
      const float h2 = rbf(rbf(0.5f * gs * (1.f + th)) * us);
      const unsigned hp = __builtin_amdgcn_perm(asu(dpp<XOR1>(h2)), asu(h2), 0x07060302u);
      float d0a = 0.f, d1a = 0.f;
#pragma unroll
      for (int e = 0; e < 16; e += 2) {
        const unsigned he = __builtin_amdgcn_readlane(hp, e);
        d0a = dot2(wdv[e], he, d0a); d1a = dot2(wdv[e + 1], he, d1a);
      }
      st64(ws, WS_DP + up * 2048 + tid * 8, v2u{noempty(asu(d0a)), noempty(asu(d1a))});
      st64(ws, (b | tid) == 0 ? WS_EPOCH : OOB, v2u{tag.lo, tag.hi});
    }
  }
  if (wave != 0) return;
  {
    v4u da = {EMPTY, 0u, 0u, 0u}, db = {lane < NGU - 64 ? EMPTY : 0u, 0u, 0u, 0u};
    const float x1o = *lds_pl<float>(lb + L_X1 + (fc0 + g) * 4);
    for (unsigned it = 0;; ++it) {
      MEMFENCE();
      const bool ma = mx4(0u, da) == EMPTY, mb_ = mx4(0u, db) == EMPTY;
      if (!__any(ma | mb_) || EXPIRED(it)) break;
      const v4u na = ld128<SC1>(ws, ma ? WS_DP + lane * 2048 + fc0 * 4 : OOB), nb = ld128<SC1>(ws, mb_ ? WS_DP + (lane + 64) * 2048 + fc0 * 4 : OOB);
      da = ma ? na : da; db = mb_ ? nb : db;
    }
    float t4[4];
#pragma unroll
    for (int r = 0; r < 4; ++r) t4[r] = asf(da[r]) + asf(db[r]);
    const float r = row_sum(sw16_add(sw32_add(t4[0], t4[2]), sw32_add(t4[1], t4[3])));
    const float res = x1o + rbf(gbf * rbf(r));
    if (c == 0) a.out[fc0 + g] = (u16)(cvtpk(res, res) & 0xffffu);
  }
}
}

extern "C" void kernel_launch(void* const* d_in, const int* in_sizes, int n_in, void* d_out, int out_size, void* d_ws, size_t ws_size,
                              hipStream_t stream) {
  Args a;
  a.x = (const u16*)d_in[0]; a.K = (const u16*)d_in[1]; a.V = (const u16*)d_in[2]; a.wq = (const u16*)d_in[3]; a.wo = (const u16*)d_in[4];
  a.wg = (const u16*)d_in[6]; a.wu = (const u16*)d_in[7]; a.wd = (const u16*)d_in[8]; a.c = (const u16*)d_in[9]; a.wm = (const u16*)d_in[10];
  a.out = (u16*)d_out; a.ws = (unsigned char*)d_ws;
  (void)hipFuncSetAttribute((const void*)block_decode, hipFuncAttributeMaxDynamicSharedMemorySize, (int)LDS_BYTES);
  hipLaunchKernelGGL(block_decode, dim3(NWG), dim3(NTHR), LDS_BYTES, stream, a);
}
```

```cpp
#include <hip/hip_runtime.h>
#include <hip/hip_bf16.h>

#define GAS __attribute__((address_space(1)))
#define LAS __attribute__((address_space(3)))
typedef unsigned short u16;
typedef unsigned long long u64;
typedef unsigned v4u __attribute__((ext_vector_type(4)));
typedef unsigned v2u __attribute__((ext_vector_type(2)));
typedef int v4i __attribute__((ext_vector_type(4)));
typedef float f32x4 __attribute__((ext_vector_type(4)));
typedef __bf16 bf16x8 __attribute__((ext_vector_type(8)));
typedef __bf16 bf16x2 __attribute__((ext_vector_type(2)));
typedef float f32x2 __attribute__((ext_vector_type(2)));

namespace {
constexpr int DM = 512, SK = 65536, DH = 128, DF = 1792;
constexpr int NWG = 256, NTHR = 384;
constexpr int SLOT = 16384, NSLOT = 8, FH = 1024;
constexpr int SO = 116;
constexpr int NP = 6;
constexpr int POOL0 = 8 * SO, KD0 = POOL0 + 8 * NP, NKT = (FH - KD0) / 4, NPT = 8 * NP / 2;
constexpr int BURST = 3;
constexpr int CAP = 144;
static_assert(NP > BURST && (FH - KD0) % 4 == 0 && SO % 4 == 0 && NP % 2 == 0 && 8 * NP + 16 <= SO && NP <= 8 && 8 * (CAP - 7 - SO) >= FH - KD0, "fill plan");
constexpr int PACE_FROM = 16, PACE_WAIT = 20, PACE_PAUSE = 9;
constexpr unsigned TW = 10800, TD = 110;
constexpr int INF = 0x7FFFFFFF;
constexpr unsigned HEAD_BYTES = (unsigned)SK * DH * 2;
constexpr unsigned EMPTY = 0xFFFFFFFFu;
constexpr unsigned OOB = 0x7FFF0000u;
constexpr u64 TLIMIT = 5000000ull;
constexpr float LOG2E = 1.4426950408889634f, SCALE = 0.08838834764831845f;
constexpr int NGU = DF / 16, REC = 640;
constexpr unsigned WS_EPOCH = 0, WS_P1 = 4096, WS_QS = WS_P1 + 32768, WS_M2P = WS_QS + 131072, WS_M2 = WS_M2P + 131072, WS_CTR = WS_M2 + 16384,
                   WS_MX = WS_CTR + 8192, WS_YP = WS_MX + 2048, WS_PO = WS_YP + 16384, WS_DP = WS_PO + NWG * REC,
                   WS_SG = WS_DP + NGU * 2048, WS_END = WS_SG + NWG * NP * 128;
constexpr unsigned L_RING = 0, L_CTL = NSLOT * SLOT, L_FT = L_CTL, L_SCR = L_CTL + 8, L_NK = L_CTL + 12, L_DONE = L_CTL + 16, L_QCNT = L_CTL + 32,
                   L_AUXPUB = L_CTL + 36, L_QIN = L_CTL + 40, L_ISSUED = L_CTL + 44, L_M2RDY = L_CTL + 48, L_TKC = L_CTL + 52, L_TK = L_CTL + 56,
                   L_KCNT = L_CTL + 64, L_MH = L_CTL + 68, L_MXW = L_CTL + 80, L_LW = L_CTL + 96, L_LIST = L_CTL + 128, L_RED = L_LIST + 128, L_QSC = L_RED + 1024, L_RED3 = L_QSC + 256,
                   L_ST = L_RED3 + 512, L_M2 = L_ST + 64, L_X1 = L_M2 + 4096, L_MRG = L_X1 + 2048, L_SH = L_X1, L_SC = L_MRG + 2048, L_END = L_SC + CAP * 128;
constexpr unsigned LDS_BYTES = L_END;
static_assert(LDS_BYTES <= 160 * 1024 && LDS_BYTES > 80 * 1024, "one workgroup per CU");

struct Args {
  const u16 *x, *K, *V, *wq, *wo, *wg, *wu, *wd, *c, *wm;
  u16* out;
  unsigned char* ws;
};

__device__ __forceinline__ u64 rt() { return __builtin_amdgcn_s_memrealtime(); }
__device__ __forceinline__ float asf(unsigned u) { return __builtin_bit_cast(float, u); }
__device__ __forceinline__ unsigned asu(float f) { return __builtin_bit_cast(unsigned, f); }
__device__ __forceinline__ float lo16(unsigned u) { return asf(u << 16); }
__device__ __forceinline__ float hi16(unsigned u) { return asf(u & 0xffff0000u); }
__device__ __forceinline__ unsigned cvtpk(float a, float b) {
  const f32x2 v = {a, b};
  return __builtin_bit_cast(unsigned, __builtin_convertvector(v, bf16x2));
}
__device__ __forceinline__ float rbf(float f) { return lo16(cvtpk(f, f)); }
__device__ __forceinline__ unsigned noempty(unsigned v) { return v < EMPTY - 1u ? v : EMPTY - 1u; }
__device__ __forceinline__ float dot2(unsigned a, unsigned b, float c) {
  return __builtin_amdgcn_fdot2_f32_bf16(__builtin_bit_cast(bf16x2, a), __builtin_bit_cast(bf16x2, b), c, false);
}
template <int CTRL> __device__ __forceinline__ float dpp(float v) {
  return asf(__builtin_amdgcn_mov_dpp(asu(v), CTRL, 0xf, 0xf, true));
}
constexpr int ROR8 = 0x128, ROR4 = 0x124, ROR2 = 0x122, ROR1 = 0x121, XOR2 = 0x4E, XOR1 = 0xB1;
template <int CTRL> __device__ __forceinline__ unsigned dppu(unsigned v) { return __builtin_amdgcn_mov_dpp(v, CTRL, 0xf, 0xf, true); }
__device__ __forceinline__ float sw32_add(float a, float b) {
  const auto r = __builtin_amdgcn_permlane32_swap(asu(a), asu(b), false, false);
  return asf(r[0]) + asf(r[1]);
}
__device__ __forceinline__ float sw16_add(float a, float b) {
  const auto r = __builtin_amdgcn_permlane16_swap(asu(a), asu(b), false, false);
  return asf(r[0]) + asf(r[1]);
}
__device__ __forceinline__ float xrow_max(float v) {
  const auto r = __builtin_amdgcn_permlane16_swap(asu(v), asu(v), false, false);
  v = fmaxf(asf(r[0]), asf(r[1]));
  const auto s = __builtin_amdgcn_permlane32_swap(asu(v), asu(v), false, false);
  return fmaxf(asf(s[0]), asf(s[1]));
}
__device__ __forceinline__ unsigned pack_halves(float v) {
  const auto r = __builtin_amdgcn_permlane32_swap(asu(v), asu(v), false, false);
  return __builtin_amdgcn_perm(r[1], r[0], 0x07060302u);
}
__device__ __forceinline__ float row_sum(float v) { v += dpp<ROR8>(v); v += dpp<ROR4>(v); v += dpp<ROR2>(v); v += dpp<ROR1>(v); return v; }
__device__ __forceinline__ float row_max(float v) { v = fmaxf(v, dpp<ROR8>(v)); v = fmaxf(v, dpp<ROR4>(v)); v = fmaxf(v, dpp<ROR2>(v)); return fmaxf(v, dpp<ROR1>(v)); }
__device__ __forceinline__ float wave_sum(float v) { v = row_sum(v); v = sw16_add(v, v); return sw32_add(v, v); }
__device__ __forceinline__ void rows_reduce8(const float (&a)[8], float& u0, float& u1) {
  const float r0 = sw32_add(a[0], a[4]), r1 = sw32_add(a[1], a[5]), r2 = sw32_add(a[2], a[6]), r3 = sw32_add(a[3], a[7]);
  u0 = sw16_add(r0, r2); u1 = sw16_add(r1, r3);
}
__device__ __forceinline__ void fma8(float (&a)[8], float h, v4u w) {
#pragma unroll
  for (int e = 0; e < 4; ++e) { a[2 * e] = fmaf(h, lo16(w[e]), a[2 * e]); a[2 * e + 1] = fmaf(h, hi16(w[e]), a[2 * e + 1]); }
}
__device__ __forceinline__ void kpair(v4u& a, v4u& b) {
  const v4u r0 = a, r1 = b;
  a = v4u{__builtin_amdgcn_perm(r1[0], r0[0], 0x05040100u), __builtin_amdgcn_perm(r1[0], r0[0], 0x07060302u),
          __builtin_amdgcn_perm(r1[1], r0[1], 0x05040100u), __builtin_amdgcn_perm(r1[1], r0[1], 0x07060302u)};
  b = v4u{__builtin_amdgcn_perm(r1[2], r0[2], 0x05040100u), __builtin_amdgcn_perm(r1[2], r0[2], 0x07060302u),
          __builtin_amdgcn_perm(r1[3], r0[3], 0x05040100u), __builtin_amdgcn_perm(r1[3], r0[3], 0x07060302u)};
}
__device__ __forceinline__ void dot8(float (&acc)[8], unsigned hp, v4u a, v4u b) {
#pragma unroll
  for (int e = 0; e < 4; ++e) { acc[e] = dot2(a[e], hp, acc[e]); acc[4 + e] = dot2(b[e], hp, acc[4 + e]); }
}
__device__ __forceinline__ f32x4 mfma(v4u a, v4u b, f32x4 c) {
  return __builtin_amdgcn_mfma_f32_16x16x32_bf16(__builtin_bit_cast(bf16x8, a), __builtin_bit_cast(bf16x8, b), c, 0, 0, 0);
}
typedef __amdgpu_buffer_rsrc_t rsrc_t;
__device__ __forceinline__ rsrc_t mkrsrc(const void* p, unsigned bytes) { return __builtin_amdgcn_make_buffer_rsrc((void*)p, 0, bytes, 0x00020000); }
constexpr int SC1 = 16, NT = 2;
template <int AUX> __device__ __forceinline__ v4u ld128(rsrc_t r, unsigned off) { return __builtin_amdgcn_raw_buffer_load_b128(r, off, 0, AUX); }
template <int AUX> __device__ __forceinline__ v2u ld64(rsrc_t r, unsigned off) { return __builtin_amdgcn_raw_buffer_load_b64(r, off, 0, AUX); }
template <int AUX> __device__ __forceinline__ unsigned ld32(rsrc_t r, unsigned off) { return __builtin_amdgcn_raw_buffer_load_b32(r, off, 0, AUX); }
__device__ __forceinline__ void st128(rsrc_t r, unsigned off, v4u v) { __builtin_amdgcn_raw_buffer_store_b128(v, r, off, 0, SC1); }
__device__ __forceinline__ void st64(rsrc_t r, unsigned off, v2u v) { __builtin_amdgcn_raw_buffer_store_b64(v, r, off, 0, SC1); }
__device__ __forceinline__ void st32(rsrc_t r, unsigned off, unsigned v) { __builtin_amdgcn_raw_buffer_store_b32(v, r, off, 0, SC1); }
__device__ __forceinline__ unsigned umax(unsigned a, unsigned b) { return a > b ? a : b; }
__device__ __forceinline__ unsigned mx4(unsigned m, v4u v) { return umax(umax(umax(m, v[0]), v[1]), umax(v[2], v[3])); }
__device__ __forceinline__ unsigned mx2(unsigned m, v2u v) { return umax(umax(m, v[0]), v[1]); }
__device__ __forceinline__ v4u noempty4(f32x4 v) { return v4u{noempty(asu(v[0])), noempty(asu(v[1])), noempty(asu(v[2])), noempty(asu(v[3]))}; }
struct Tag { unsigned lo, hi; };
template <int LB> __device__ __forceinline__ unsigned tg_hi(Tag t) { return LB ? __builtin_amdgcn_alignbit(t.hi, t.lo, LB) : t.lo; }
template <int LB> __device__ __forceinline__ unsigned gr_lo(float v, Tag t) { return ((asu(v) + ((1u << LB) >> 1)) & (~0u << LB)) | (t.lo & ((1u << LB) - 1u)); }
template <int LB> __device__ __forceinline__ v2u gr1(float a, Tag t) { return v2u{gr_lo<LB>(a, t), tg_hi<LB>(t)}; }
template <int LB> __device__ __forceinline__ v4u gr2(float a, float b, Tag t) { return v4u{gr_lo<LB>(a, t), tg_hi<LB>(t), gr_lo<LB>(b, t), tg_hi<LB>(t)}; }
template <int LB> __device__ __forceinline__ unsigned gr_bad(v4u g, Tag t) {
  return (((g[0] ^ t.lo) | (g[2] ^ t.lo)) & ((1u << LB) - 1u)) | (g[1] ^ tg_hi<LB>(t)) | (g[3] ^ tg_hi<LB>(t));
}
template <int LB> __device__ __forceinline__ float gr_val(unsigned lo) { return asf(lo & (~0u << LB)); }
template <int LB> __device__ __forceinline__ float gr_sum(v4u g) { const float s = sw32_add(gr_val<LB>(g[0]), gr_val<LB>(g[2])); return sw16_add(s, s); }
__device__ __forceinline__ Tag next_tag(v2u ep) { const u64 e = (((u64)ep[1] << 32) | ep[0]) + 1ull; return Tag{(unsigned)e, (unsigned)(e >> 32)}; }
__device__ __forceinline__ v4u sel4(bool t, v4u a, v4u b) { return v4u{t ? a[0] : b[0], 0u, t ? a[2] : b[2], 0u}; }
#define MEMFENCE() asm volatile("" ::: "memory")
#define LGKM0() asm volatile("s_waitcnt lgkmcnt(0)" ::: "memory")
#define VM0() asm volatile("s_waitcnt vmcnt(0)" ::: "memory")
#define PIN4(a) asm volatile("" : "+v"((a)[0]), "+v"((a)[1]), "+v"((a)[2]), "+v"((a)[3]))
template <int N> __device__ __forceinline__ void vmwait() { asm volatile("s_waitcnt vmcnt(%0)" ::"n"(N) : "memory"); }
__device__ __forceinline__ void wg_barrier() { LGKM0(); __builtin_amdgcn_s_barrier(); MEMFENCE(); }
template <typename T> __device__ __forceinline__ volatile LAS T* lds_at(unsigned off) { return (volatile LAS T*)(size_t)off; }
template <typename T> __device__ __forceinline__ LAS T* lds_pl(unsigned off) { return (LAS T*)(size_t)off; }
__device__ __forceinline__ void lds_wait_ge(unsigned addr, unsigned need, u64 t0) {
  for (unsigned sp = 0; *lds_at<unsigned>(addr) < need; ++sp) { __builtin_amdgcn_s_sleep(1); if ((sp & 63u) == 63u && rt() - t0 > TLIMIT) break; }
  MEMFENCE();
}

#define EXPIRED(it) (((it) & 63u) == 63u && rt() - t0 > TLIMIT)

#define DMA4(M, O) "s_mov_b32 m0, " M "\n\ts_nop 0\n\t" \
                   "buffer_load_dwordx4 %[v0], %[d], " O " offen nt lds\n\tbuffer_load_dwordx4 %[v1], %[d], " O " offen offset:1024 nt lds\n\t" \
                   "buffer_load_dwordx4 %[v2], %[d], " O " offen offset:2048 nt lds\n\tbuffer_load_dwordx4 %[v3], %[d], " O " offen offset:3072 nt lds\n\t"
template <int WAIT, int PAUSE> __device__ __forceinline__ void dma_fill(v4i srd, unsigned o, unsigned sl, unsigned v0, unsigned v1, unsigned v2, unsigned v3) {
  asm volatile("s_waitcnt vmcnt(%[w])\n\ts_sleep %[p]\n\t" DMA4("%[m0]", "%[o0]") DMA4("%[m1]", "%[o1]") DMA4("%[m2]", "%[o2]") DMA4("%[m3]", "%[o3]")
               ::[w] "n"(WAIT), [p] "n"(PAUSE), [d] "s"(srd), [v0] "v"(v0), [v1] "v"(v1), [v2] "v"(v2), [v3] "v"(v3),
                 [m0] "s"(sl), [m1] "s"(sl + 4096), [m2] "s"(sl + 8192), [m3] "s"(sl + 12288),
                 [o0] "s"(o), [o1] "s"(o + 4096), [o2] "s"(o + 32768), [o3] "s"(o + 36864) : "memory");
}

__device__ __forceinline__ float silu_bf(float v) {
  const float t = -v * LOG2E, r = fmaf(-v, 1.9259629911e-8f, fmaf(-v, LOG2E, -t));
  const float e = __builtin_amdgcn_exp2f(t);
  return rbf(v / (1.f + (e < INFINITY ? fmaf(e * 0.6931471806f, r, e) : e)));
}
__device__ __forceinline__ void ln_round(float mean, float var, float& mb, float& sd) {
  mb = rbf(mean);
  sd = rbf(__builtin_amdgcn_sqrtf(rbf(rbf(var) + 1e-5f)));
}
__device__ __forceinline__ float mod_round(float n, float sc, float sh) { return rbf(rbf(rbf(n) * rbf(1.f + sc)) + sh); }

__global__ void __launch_bounds__(NTHR, 2) block_decode(Args a) {
  extern __shared__ __attribute__((aligned(16))) unsigned char smem[];
  const int tid = threadIdx.x, lane = tid & 63, wave = __builtin_amdgcn_readfirstlane(tid >> 6);
  const int b = blockIdx.x, x = b & 7, j = b >> 3;
  const u64 t0 = rt();
  const unsigned lb = (unsigned)(size_t)(LAS unsigned char*)smem;
  const rsrc_t ws = mkrsrc(a.ws, WS_END);
  const int kq = j >> 3, cb = x + 8 * (j & 7);
  const int gq = j >> 2;
  const bool mlp = (x & 1) == 0;
  const int og = (x & 6) + (j >> 4), oc0 = 32 * (j & 15);
  const bool has_gu = mlp && 4 * gq + (x >> 1) < 28;
  const int up = 4 * (4 * gq + (x >> 1)) + (j & 3);
  const int fc0 = 64 * x + 4 * j;

  if (tid < 20) *lds_pl<unsigned>(lb + L_CTL + 4 * tid) = (tid == 1 || tid == 3) ? (unsigned)INF : 0u;
  asm volatile("" :: "s"(a.K), "s"(a.ws));
  wg_barrier();
  int w_a = wave, w_b = wave;
  asm volatile("" : "+s"(w_a));
  if (w_a == 4) {
    const u64 kb = (u64)(a.K + (size_t)j * SK * DH), vb = (u64)(a.V + (size_t)j * SK * DH);
    const v4i ksrd = {(int)kb, (int)(kb >> 32), (int)HEAD_BYTES, 0x00020000}, vsrd = {(int)vb, (int)(vb >> 32), (int)HEAD_BYTES, 0x00020000};
    const unsigned g = lane >> 4, c = lane & 15;
    const unsigned k0 = g * 256 + ((c ^ g) << 4), k1 = g * 256 + ((c ^ (4 + g)) << 4), k2 = g * 256 + ((c ^ (8 + g)) << 4), k3 = g * 256 + ((c ^ (12 + g)) << 4);
    const unsigned vl = lane * 16;
    unsigned sl = lb + L_RING;
    auto foff = [](int n) { return ((unsigned)n >> 2) * 65536u + ((unsigned)n & 3u) * 8192u; };
    auto step = [&]() { sl = sl + SLOT == lb + L_RING + NSLOT * SLOT ? lb + L_RING : sl + SLOT; };
    volatile LAS u64* FT = lds_at<u64>(lb + L_FT);
    int pub = 0;
    unsigned total = (unsigned)INF;
    auto publish = [&](int n) { pub = n > pub ? n : pub; *FT = (u64)(unsigned)pub | ((u64)total << 32); };
    auto wait_free = [&](int i) {
      const int p = i - NSLOT;
      volatile LAS u64* dn = lds_at<u64>(lb + L_DONE + 8 * (p & 1));
      const unsigned need = (unsigned)(p >> 1) + 1u;
      auto freed = [&]() { const u64 d = *dn; return (unsigned)d >= need && (unsigned)(d >> 32) >= need; };
      if (!freed()) {
        VM0(); publish(i);
        for (unsigned sp = 0; !freed(); ++sp) { __builtin_amdgcn_s_sleep(1); if (EXPIRED(sp)) break; }
      }
      MEMFENCE();
    };
    for (unsigned sp = 0; *lds_at<unsigned>(lb + L_ISSUED) < 5u && sp < 4096u; ++sp) {}
    unsigned tn = 1;
    auto take = [&]() {
      u64 tv;
      for (unsigned sp = 0; (unsigned)((tv = *lds_at<u64>(lb + L_TK)) >> 32) < tn; ++sp) if (EXPIRED(sp)) { tv = EMPTY; break; }
      ++tn;
      return (unsigned)__builtin_amdgcn_readfirstlane((unsigned)tv);
    };
#pragma unroll
    for (int i = 0; i < BURST; ++i) { dma_fill<63, 0>(ksrd, foff(POOL0 + x * NP + i), sl, k0, k1, k2, k3); step(); }
    int i = BURST, nt = 0, next = POOL0 + x * NP + BURST, left = NP - BURST;
    bool asked = true;
    for (;; ++i) {
      if (i < PACE_FROM) dma_fill<16, 0>(ksrd, foff(next), sl, k0, k1, k2, k3);
      else dma_fill<PACE_WAIT, PACE_PAUSE>(ksrd, foff(next), sl, k0, k1, k2, k3);
      step();
      ++next; --left;
      vmwait<32>();
      publish(i - 1);
      if (i == NP + SO - 5) *lds_at<unsigned>(lb + L_TKC) = tn;
      if (left == 0) {
        if (i == NP - 1) { next = x * SO; left = SO; }
        else {
          const unsigned t = asked ? take() : EMPTY;
          if (t >= (unsigned)NKT) break;
          left = 4; next = KD0 + 4 * (int)t;
          *lds_at<unsigned>(lb + L_LIST + 4 * nt) = t; ++nt;
          asked = i + 1 - NP + left + 4 <= CAP;
          if (asked) *lds_at<unsigned>(lb + L_TKC) = tn;
        }
      }
      if (i + 1 >= NSLOT) wait_free(i + 1);
    }
    const int nk = i + 1, vown = 2 * nk - NP;
    *lds_at<unsigned>(lb + L_NK) = (unsigned)nk;
    wait_free(i + 1);
    next = x * SO; left = SO;
    int rt_ = 0;
    for (++i;; ++i) {
      dma_fill<PACE_WAIT, PACE_PAUSE>(vsrd, foff(next), sl, vl, vl, vl, vl);
      step();
      ++next; --left;
      vmwait<32>();
      publish(i - 1);
      if (i == vown - 3) *lds_at<unsigned>(lb + L_TKC) = tn;
      if (left == 0) {
        if (rt_ < nt) { const unsigned t = __builtin_amdgcn_readfirstlane(*lds_at<unsigned>(lb + L_LIST + 4 * rt_)); ++rt_; left = 4; next = KD0 + 4 * (int)t; }
        else {
          const unsigned t = take();
          if (t >= (unsigned)NPT) break;
          *lds_at<unsigned>(lb + L_TKC) = tn;
          left = 2; next = POOL0 + 2 * (int)t;
        }
      }
      wait_free(i + 1);
    }
    total = (unsigned)i + 1u;
    publish(i - 1);
    vmwait<16>(); publish(i);
    VM0(); publish(i + 1);
    return;
  }

  asm volatile("" : "+s"(w_b));
  if (w_b == 5) {
    __builtin_amdgcn_s_setprio(3);
    const rsrc_t rwm = mkrsrc(a.wm, DM * 3072 * 2), rc = mkrsrc(a.c, DM * 2);
    const int n0 = 512 * (gq & 1) + 64 * x + 16 * (j & 3);
    const int ar = 128 * kq + 4 * (lane >> 1);
    const v2u c4 = ld64<0>(rc, ar * 2);
    MEMFENCE();
    v4u w1[4];
#pragma unroll
    for (int i = 0; i < 4; ++i) w1[i] = ld128<NT>(rwm, (unsigned)((ar + i) * 3072 + n0 + 8 * (lane & 1)) * 2);
    const v2u ep = ld64<SC1>(ws, WS_EPOCH);
    const int mr = 64 * gq + 8 * (lane >> 3), mc = 64 * (x + 8 * (j & 3));
    const v4u cv = ld128<0>(rc, mr * 2);
    MEMFENCE();
    if (lane == 0) __hip_atomic_fetch_add(lds_pl<unsigned>(lb + L_ISSUED), 1u, __ATOMIC_RELAXED, __HIP_MEMORY_SCOPE_WORKGROUP);
    st128(ws, (lane < 8 && !mlp) ? WS_YP + (og * 512 + oc0) * 4 + lane * 16 : OOB, v4u{EMPTY, EMPTY, EMPTY, EMPTY});
    st128(ws, has_gu ? WS_DP + up * 2048 + lane * 16 : OOB, v4u{EMPTY, EMPTY, EMPTY, EMPTY});
    st128(ws, has_gu ? WS_DP + up * 2048 + 1024 + lane * 16 : OOB, v4u{EMPTY, EMPTY, EMPTY, EMPTY});
    st128(ws, lane < 33 ? WS_PO + b * REC + lane * 16 : OOB, v4u{EMPTY, EMPTY, EMPTY, EMPTY});
    st32(ws, (lane < 2 && x == 0) ? WS_CTR + j * 256 + lane * 128 : OOB, 0u);
    const Tag tag = next_tag(ep);
    {
      float s8[8] = {0.f, 0.f, 0.f, 0.f, 0.f, 0.f, 0.f, 0.f};
      fma8(s8, silu_bf(lo16(c4[0])), w1[0]); fma8(s8, silu_bf(hi16(c4[0])), w1[1]);
      fma8(s8, silu_bf(lo16(c4[1])), w1[2]); fma8(s8, silu_bf(hi16(c4[1])), w1[3]);
      float u0, u1;
      rows_reduce8(s8, u0, u1);
      u0 += dpp<ROR8>(u0); u1 += dpp<ROR8>(u1); u0 += dpp<ROR4>(u0); u1 += dpp<ROR4>(u1); u0 += dpp<XOR2>(u0); u1 += dpp<XOR2>(u1);
      const int col = n0 + 8 * (lane & 1) + 2 * (lane >> 4);
      st128(ws, (lane & 15) < 2 ? WS_P1 + (kq * 1024 + col) * 8 : OOB, gr2<0>(u0, u1, tag));
      *lds_at<unsigned>(lb + L_AUXPUB) = 1u;
    }
    __builtin_amdgcn_s_setprio(0);
    auto stream_at = [&](unsigned fillno) {
      for (unsigned sp = 0; *lds_at<unsigned>(lb + L_FT) < fillno; ++sp) { __builtin_amdgcn_s_sleep(8); if ((sp & 63u) == 63u && rt() - t0 > TLIMIT) break; }
      MEMFENCE();
    };
    stream_at(NSLOT + 4);
    {
      v4u w2[8];
#pragma unroll
      for (int i = 0; i < 8; ++i) w2[i] = ld128<NT>(rwm, (unsigned)((mr + i) * 3072 + 1024 + mc + 8 * (lane & 7)) * 2);
      float s8[8] = {0.f, 0.f, 0.f, 0.f, 0.f, 0.f, 0.f, 0.f};
#pragma unroll
      for (int e = 0; e < 4; ++e) { fma8(s8, silu_bf(lo16(cv[e])), w2[2 * e]); fma8(s8, silu_bf(hi16(cv[e])), w2[2 * e + 1]); }
      float u0, u1;
      rows_reduce8(s8, u0, u1);
      u0 += dpp<ROR8>(u0); u1 += dpp<ROR8>(u1);
      st128(ws, (lane & 8) == 0 ? WS_M2P + (gq * 2048 + mc + 8 * (lane & 7) + 2 * (lane >> 4)) * 8 : OOB, gr2<4>(u0, u1, tag));
    }
    stream_at(NSLOT + 16);
    {
      v4u m;
      for (;;) {
        MEMFENCE();
        m = ld128<SC1>(ws, WS_M2P + (((lane >> 2) & 7) * 2048 + 8 * b + 2 * (lane & 3)) * 8);
        if (__all(gr_bad<4>(m, tag) == 0u) || rt() - t0 > TLIMIT) break;
        __builtin_amdgcn_s_sleep(32);
      }
      float u0 = gr_val<4>(m[0]), u1 = gr_val<4>(m[2]);
      u0 = sw16_add(u0, u0); u1 = sw16_add(u1, u1);
      u0 += dpp<ROR8>(u0); u1 += dpp<ROR8>(u1); u0 += dpp<ROR4>(u0); u1 += dpp<ROR4>(u1);
      VM0();
      st128(ws, lane < 4 ? WS_M2 + b * 64 + lane * 16 : OOB, gr2<4>(rbf(u0), rbf(u1), tag));
    }
    stream_at(NSLOT + 28);
#pragma unroll 1
    for (int u = 0; u < 16; ++u) {
      v4u m;
      for (;;) {
        MEMFENCE();
        m = ld128<SC1>(ws, WS_M2 + u * 1024 + lane * 16);
        if (__all(gr_bad<4>(m, tag) == 0u) || rt() - t0 > TLIMIT) break;
        __builtin_amdgcn_s_sleep(32);
      }
      *lds_pl<unsigned>(lb + L_M2 + u * 256 + lane * 4) = __builtin_amdgcn_perm(m[2], m[0], 0x07060302u);
      __builtin_amdgcn_s_sleep(16);
    }
    LGKM0();
    *lds_at<unsigned>(lb + L_M2RDY) = 1u;
    GAS unsigned* ctr = (GAS unsigned*)(a.ws + WS_CTR + j * 256);
    unsigned n = 1;
    auto draw = [&](int which) {
      unsigned v = 0;
      if (lane == 0) v = __hip_atomic_fetch_add(ctr + 32 * which, 1u, __ATOMIC_RELAXED, __HIP_MEMORY_SCOPE_AGENT);
      v = __builtin_amdgcn_readfirstlane(v);
      *lds_at<u64>(lb + L_TK) = ((u64)n << 32) | v;
      ++n;
      return v;
    };
    for (;;) {
      bool over = false;
      for (unsigned sp = 0; *lds_at<unsigned>(lb + L_TKC) < n; ++sp) {
        if (*lds_at<unsigned>(lb + L_NK) != (unsigned)INF || EXPIRED(sp)) { over = true; break; }
        __builtin_amdgcn_s_sleep(2);
      }
      if (over || *lds_at<unsigned>(lb + L_NK) != (unsigned)INF || draw(0) >= (unsigned)NKT) break;
    }
    lds_wait_ge(lb + L_KCNT, 5u, t0);
    __builtin_amdgcn_s_sleep(127);
    {
      v2u mx;
      for (;;) {
        MEMFENCE();
        mx = ld64<SC1>(ws, WS_MX + j * 64 + (lane & 7) * 8);
        if (__all(gr_bad<4>(v4u{mx[0], mx[1], mx[0], mx[1]}, tag) == 0u) || rt() - t0 > TLIMIT) break;
        __builtin_amdgcn_s_sleep(32);
      }
      *lds_at<float>(lb + L_MH) = row_max(gr_val<4>(mx[0]));
      LGKM0();
    }
    {
      unsigned dq = 0, pend = 0;
      v4u sc = {0u, 0u, 0u, 0u};
      auto flush = [&]() {
        if ((unsigned)lane < 8u * pend) *lds_pl<v4u>(lb + L_SC + dq * 128 + lane * 16) = sc;
        dq += pend; pend = 0;
        LGKM0();
        *lds_at<unsigned>(lb + L_SCR) = dq;
      };
      for (;;) {
        if (__builtin_amdgcn_readfirstlane(*lds_at<unsigned>(lb + L_TKC)) < n) flush();
        for (unsigned sp = 0; *lds_at<unsigned>(lb + L_TKC) < n; ++sp) { __builtin_amdgcn_s_sleep(2); if (EXPIRED(sp)) break; }
        const unsigned v = draw(1);
        flush();
        if (v >= (unsigned)NPT || rt() - t0 > TLIMIT) break;
        pend = 2u;
        sc = ld128<SC1 | NT>(ws, lane < 16 ? WS_SG + (j * 8 * NP + 2 * v) * 128 + lane * 16 : OOB);
        MEMFENCE();
      }
    }
    return;
  }

  const int g = lane >> 4, c = lane & 15;
  const rsrc_t rx = mkrsrc(a.x, DM * 2), rwq = mkrsrc(a.wq, DM * 4096 * 2);
  const rsrc_t rwd = mkrsrc(a.wd, DF * DM * 2);
  const int r0 = 128 * kq + 32 * wave;
  v4u wqv[4];
#pragma unroll
  for (int i = 0; i < 4; ++i) wqv[i] = ld128<NT>(rwq, (unsigned)((r0 + 4 * (lane >> 3) + i) * 4096 + 64 * cb + 8 * (lane & 7)) * 2);
  const v2u ep = ld64<SC1>(ws, WS_EPOCH);
  const v4u x8 = ld128<0>(rx, lane * 16);
  const unsigned x2 = ld32<0>(rx, tid * 4);
  const unsigned xh = ld32<0>(rx, (r0 + 2 * c) * 2);
  MEMFENCE();
  if (lane == 0) __hip_atomic_fetch_add(lds_pl<unsigned>(lb + L_ISSUED), 1u, __ATOMIC_RELAXED, __HIP_MEMORY_SCOPE_WORKGROUP);
  float mb, sd;
  {
    float xs[8];
#pragma unroll
    for (int e = 0; e < 4; ++e) { xs[2 * e] = lo16(x8[e]); xs[2 * e + 1] = hi16(x8[e]); }
    float s = 0.f;
#pragma unroll
    for (int e = 0; e < 8; ++e) s += xs[e];
    const float mean = wave_sum(s) * (1.f / 512.f);
    float v = 0.f;
#pragma unroll
    for (int e = 0; e < 8; ++e) { const float d = xs[e] - mean; v = fmaf(d, d, v); }
    ln_round(mean, wave_sum(v) * (1.f / 512.f), mb, sd);
  }
  float nrow = rbf((lane >= 32 ? hi16(xh) : lo16(xh)) - mb) / sd;
  kpair(wqv[0], wqv[1]); kpair(wqv[2], wqv[3]);
  PIN4(wqv); asm volatile("" : "+v"(nrow));
  const Tag tag = next_tag(ep);
  lds_wait_ge(lb + L_AUXPUB, 1u, t0);
  {
    const unsigned o1 = WS_P1 + (g * 1024 + r0 + 2 * c) * 8;
    v4u gs = {0u, 0u, 0u, 0u}, gc = gs;
    bool bad = true;
    __builtin_amdgcn_s_sleep(32);
    for (;;) {
      MEMFENCE();
      const v4u ns_ = ld128<SC1>(ws, bad ? o1 : OOB), nc_ = ld128<SC1>(ws, bad ? o1 + 4096 : OOB);
      const bool tk = bad & ((gr_bad<0>(ns_, tag) | gr_bad<0>(nc_, tag)) == 0u);
      gs = sel4(tk, ns_, gs); gc = sel4(tk, nc_, gc); bad = bad & !tk;
      if (!__any(bad) || rt() - t0 > TLIMIT) break;
    }
    const unsigned hp = pack_halves(mod_round(nrow, rbf(gr_sum<0>(gc)), rbf(gr_sum<0>(gs))));
    float acc[8] = {0.f, 0.f, 0.f, 0.f, 0.f, 0.f, 0.f, 0.f};
    dot8(acc, __builtin_amdgcn_ds_bpermute((lane >> 3) * 8, hp), wqv[0], wqv[1]);
    dot8(acc, __builtin_amdgcn_ds_bpermute((lane >> 3) * 8 + 4, hp), wqv[2], wqv[3]);
    float u0, u1;
    rows_reduce8(acc, u0, u1);
    u0 += dpp<ROR8>(u0); u1 += dpp<ROR8>(u1);
    if ((lane & 8) == 0) *lds_pl<f32x2>(lb + L_RED + wave * 256 + (8 * (lane & 7) + 2 * g) * 4) = f32x2{u0, u1};
    LGKM0();
    unsigned old = 0;
    if (lane == 0) old = __hip_atomic_fetch_add(lds_pl<unsigned>(lb + L_QCNT), 1u, __ATOMIC_RELAXED, __HIP_MEMORY_SCOPE_WORKGROUP);
    old = __builtin_amdgcn_readfirstlane(old);
    MEMFENCE();
    if (old == 3u) {
      const unsigned ra = lb + L_RED + lane * 4;
      const float qp = (*lds_pl<float>(ra) + *lds_pl<float>(ra + 256)) + (*lds_pl<float>(ra + 512) + *lds_pl<float>(ra + 768));
      st64(ws, WS_QS + (kq * 4096 + 64 * cb + lane) * 8, gr1<2>(qp, tag));
    }
  }
  const int r8 = 8 * (tid >> 2);
  v4u wv[8];
  unsigned wdv[16];
#pragma unroll
  for (int i = 0; i < 8; ++i) { wv[i] = v4u{0u, 0u, 0u, 0u}; wdv[2 * i] = 0u; wdv[2 * i + 1] = 0u; }
  const GAS v4u* wp = (const GAS v4u*)(mlp ? ((tid & 2) ? a.wu : a.wg) + (r8 * DF + 16 * (has_gu ? up : 0) + 8 * (tid & 1)) : a.wo + ((512 * og + r8) * DM + oc0 + 8 * (tid & 3)));
  const int wstride = mlp ? DF / 8 : DM / 8;
  const unsigned wdo = has_gu ? (unsigned)(16 * up * DM + 2 * tid) * 2 : OOB;

  const unsigned key = 4 * (c & 3) + (c >> 2);
  unsigned ka[4], msk[4];
#pragma unroll
  for (int st = 0; st < 4; ++st) {
    ka[st] = lb + L_RING + (wave & 1) * 8192 + key * 256 + (((st + 4 * g) ^ key) << 4);
    msk[st] = ((c & 7) >> 1) == st ? ((c & 1) ? 0xffff0000u : 0x0000ffffu) : 0u;
  }
  const unsigned va = lb + L_RING + (wave & 1) * 8192 + lane * 16;
  const bool is_hi = c < 8;
  volatile LAS unsigned* DN = lds_at<unsigned>(lb + L_DONE + 4 * wave);
  unsigned so = (wave >> 1) * SLOT;
  v4u sv[8], sw[8], ud[8], qf[4];
#pragma unroll
  for (int i = 0; i < 8; ++i) { sv[i] = v4u{0u, 0u, 0u, 0u}; sw[i] = sv[i]; ud[i] = sv[i]; }
  int ns = 0;
  auto release = [&](v4u (&d)[8], int i) {
    LGKM0();
    *DN = (unsigned)(i >> 1) + 1u;
    asm volatile("" : "+v"(d[0]), "+v"(d[1]), "+v"(d[2]), "+v"(d[3]), "+v"(d[4]), "+v"(d[5]), "+v"(d[6]), "+v"(d[7]) :: "memory");
    so = (so + 2 * SLOT) & (NSLOT * SLOT - 1);
  };
  auto read_k = [&](v4u (&d)[8], int i) {
#pragma unroll
    for (int st = 0; st < 4; ++st) { d[st] = *lds_pl<v4u>(ka[st] + so); d[4 + st] = *lds_pl<v4u>(ka[st] + so + 4096); }
    release(d, i);
  };
  int total = INF, nk = INF;
  auto wait_full = [&](int i, bool in_k) {
    for (unsigned sp = 0;; ++sp) {
      const v4u ct = *(volatile LAS v4u*)(size_t)(lb + L_FT);
      total = __builtin_amdgcn_readfirstlane((int)ct[1]);
      nk = __builtin_amdgcn_readfirstlane((int)ct[3]);
      if (EXPIRED(sp)) { total = 0; nk = nk < i ? nk : i; break; }
      if (__builtin_amdgcn_readfirstlane((int)ct[0]) > i || (in_k && nk <= i)) break;
      if (total == INF) __builtin_amdgcn_s_sleep(1);
    }
    MEMFENCE();
  };
  float mraw = -INFINITY;
  auto scores = [&](const v4u (&d)[8], int i) {
    f32x4 sa = {0.f, 0.f, 0.f, 0.f}, sb = {0.f, 0.f, 0.f, 0.f};
#pragma unroll
    for (int st = 0; st < 4; ++st) { sa = mfma(d[st], qf[st], sa); sb = mfma(d[4 + st], qf[st], sb); }
    mraw = fmaxf(fmaxf(fmaxf(mraw, sa[0]), fmaxf(sa[1], sa[2])), fmaxf(fmaxf(sa[3], sb[0]), fmaxf(fmaxf(sb[1], sb[2]), sb[3])));
    v4u pk = {cvtpk(sa[0], sa[1]), cvtpk(sa[2], sa[3]), cvtpk(sb[0], sb[1]), cvtpk(sb[2], sb[3])};
#pragma unroll
    for (int u = 0; u < 4; ++u) pk[u] = cvtpk(lo16(pk[u]) * SCALE, hi16(pk[u]) * SCALE);
    const unsigned so_ = (wave & 1) * 64 + g * 16;
    const unsigned dst = i < NP ? L_SH + i * 128 : L_SC + (i - NP) * 128;
    if (c == 0) *lds_pl<v4u>(lb + dst + so_) = pk;
  };
  auto stash = [&]() {
    const int full = __builtin_amdgcn_readfirstlane((int)*lds_at<unsigned>(lb + L_FT)), i0 = wave >> 1;
    MEMFENCE();
    if (ns == 0 && full > i0) { read_k(wv, i0); ns = 1; }
    if (ns == 1 && full > i0 + 2) { read_k(sv, i0 + 2); ns = 2; }
    if (ns == 2 && full > i0 + 4) { read_k(sw, i0 + 4); ns = 3; }
    if (ns == 3 && full > i0 + 6) { read_k(ud, i0 + 6); ns = 4; }
  };
  {
    const unsigned o2 = WS_QS + (g * 4096 + 128 * j + 32 * wave + 2 * c) * 8;
    v4u gq4 = {0u, 0u, 0u, 0u};
    bool bad = true;
    stash();
    {
      const v4u nq = ld128<SC1>(ws, o2);
      bad = gr_bad<2>(nq, tag) != 0u;
      gq4 = nq;
    }
    asm volatile("" : "+v"(gq4) :: "memory");
    stash();
    for (unsigned it = 0; __any(bad) && !EXPIRED(it); ++it) {
      MEMFENCE();
      const v4u nq = ld128<SC1>(ws, bad ? o2 : OOB);
      const bool tk = bad & (gr_bad<2>(nq, tag) == 0u);
      gq4 = sel4(tk, nq, gq4); bad = bad & !tk;
    }
    *lds_pl<unsigned>(lb + L_QSC + (16 * wave + c) * 4) = pack_halves(rbf(gr_sum<2>(gq4)));
    LGKM0();
    if (lane == 0) __hip_atomic_fetch_add(lds_pl<unsigned>(lb + L_QIN), 1u, __ATOMIC_RELAXED, __HIP_MEMORY_SCOPE_WORKGROUP);
    lds_wait_ge(lb + L_QIN, 4u, t0);
#pragma unroll
    for (int st = 0; st < 4; ++st) qf[st] = *lds_pl<v4u>(lb + L_QSC + (st + 4 * g) * 16);
  }
  if (ns >= 1) scores(wv, wave >> 1);
  if (ns >= 2) scores(sv, (wave >> 1) + 2);
  if (ns >= 3) scores(sw, (wave >> 1) + 4);
  if (ns >= 4) scores(ud, (wave >> 1) + 6);
  int i = (wave >> 1) + 2 * ns;
  for (;; i += 2) {
    wait_full(i, true);
    if (nk <= i) break;
    read_k(ud, i);
    scores(ud, i);
  }
  float m2;
  {
    *lds_pl<float>(lb + L_MXW + wave * 4) = rbf(rbf(xrow_max(mraw)) * SCALE);
    LGKM0();
    unsigned old = 0;
    if (lane == 0) old = __hip_atomic_fetch_add(lds_pl<unsigned>(lb + L_KCNT), 1u, __ATOMIC_RELAXED, __HIP_MEMORY_SCOPE_WORKGROUP);
    old = __builtin_amdgcn_readfirstlane(old);
    lds_wait_ge(lb + L_KCNT, 4u, t0);
    const f32x4 mw = *lds_pl<f32x4>(lb + L_MXW);
    const float mg = fmaxf(fmaxf(mw[0], mw[1]), fmaxf(mw[2], mw[3]));
    if (old == 3u) {
#pragma unroll
      for (int u = 0; u < (NP + 7) / 8; ++u)
        __builtin_amdgcn_raw_buffer_store_b128(*lds_pl<v4u>(lb + L_SH + u * 1024 + lane * 16), ws,
                                               u * 1024 + lane * 16 < NP * 128 ? WS_SG + b * NP * 128 + u * 1024 + lane * 16 : OOB, 0, SC1 | NT);
      VM0();
      st64(ws, lane == 0 ? WS_MX + b * 8 : OOB, gr1<4>(mg, tag));
      *lds_at<unsigned>(lb + L_KCNT) = 5u;
    }
    m2 = mg * LOG2E;
  }
  float lsum = 0.f;
  f32x4 acc = {0.f, 0.f, 0.f, 0.f};
  {
    int wk = (mlp && !has_gu) ? 11 : 0;
    bool done = false, own = true;
    for (;; i += 2) {
      int e = i - nk;
      if (!done && e >= nk - NP) {
        e -= nk - NP;
        for (unsigned sp = 0;; ++sp) {
          const v4u ct = *(volatile LAS v4u*)(size_t)(lb + L_FT);
          if (__builtin_amdgcn_readfirstlane((int)ct[2]) > e) break;
          if (__builtin_amdgcn_readfirstlane((int)ct[1]) <= i || EXPIRED(sp)) { done = true; break; }
          __builtin_amdgcn_s_sleep(1);
        }
        MEMFENCE();
        if (own) {
          const float mh = *lds_at<float>(lb + L_MH) * LOG2E, al = __builtin_amdgcn_exp2f(m2 - mh);
          acc *= al; lsum *= al; m2 = mh; own = false;
        }
      }
      if (!done) {
        const v4u sc = *lds_pl<v4u>(lb + L_SC + e * 128 + (wave & 1) * 64 + g * 16);
        v4u pa[8];
#pragma unroll
        for (int u = 0; u < 8; ++u) {
          const float p = __builtin_amdgcn_exp2f(fmaf((u & 1) ? hi16(sc[u >> 1]) : lo16(sc[u >> 1]), LOG2E, -m2));
          lsum += p;
          const float ph = hi16(asu(p));
          const float xv = is_hi ? ph : p - ph;
          const unsigned pk = cvtpk(xv, xv);
          pa[u] = v4u{pk & msk[0], pk & msk[1], pk & msk[2], pk & msk[3]};
        }
        asm volatile("" : "+v"(pa[0]), "+v"(pa[1]), "+v"(pa[2]), "+v"(pa[3]), "+v"(pa[4]), "+v"(pa[5]), "+v"(pa[6]), "+v"(pa[7]));
        wait_full(i, false);
#pragma unroll
        for (int u = 0; u < 8; ++u) ud[u] = *lds_pl<v4u>(va + so + u * 1024);
        release(ud, i);
#pragma unroll
        for (int u = 0; u < 8; ++u) acc = mfma(pa[u], ud[u], acc);
        done = i + 2 >= total && !own;
      }
      MEMFENCE();
      if (wk <= 10 && (done || rt() - t0 >= (u64)(TW + TD * wk + (wk == 10 ? 400 : 0)))) {
#pragma unroll
        for (int u = 0; u < 8; ++u) if (wk == u) wv[u] = __builtin_nontemporal_load(wp + u * wstride);
#pragma unroll
        for (int v = 0; v < 2; ++v) if (wk == 8 + v) {
#pragma unroll
          for (int u = 8 * v; u < 8 * v + 8; ++u) wdv[u] = ld32<NT>(rwd, wdo + u * (DM * 2));
        }
        if (wk == 10) {
#pragma unroll
          for (int u = 0; u < 8; u += 2) kpair(wv[u], wv[u + 1]);
#pragma unroll
          for (int u = 0; u < 16; u += 2) {
            const unsigned lo = __builtin_amdgcn_perm(wdv[u + 1], wdv[u], 0x05040100u), hi = __builtin_amdgcn_perm(wdv[u + 1], wdv[u], 0x07060302u);
            wdv[u] = lo; wdv[u + 1] = hi;
          }
        }
        ++wk;
      }
      MEMFENCE();
      if (done && wk > 10) break;
    }
  }
  {
    f32x4 o;
#pragma unroll
    for (int r = 0; r < 4; ++r) o[r] = sw32_add(acc[r], acc[r]);
    float l = sw16_add(lsum, lsum); l = sw32_add(l, l);
    *lds_pl<f32x4>(lb + L_MRG + wave * 512 + (8 * c + 4 * (g & 1)) * 4) = o;
    *lds_pl<float>(lb + L_LW + wave * 4) = l;
    wg_barrier();
    if (wave == 0) {
      const unsigned ra = lb + L_MRG + (lane & 31) * 16;
      const f32x4 lw = *lds_pl<f32x4>(lb + L_LW);
      f32x4 O = (*lds_pl<f32x4>(ra) + *lds_pl<f32x4>(ra + 512)) + (*lds_pl<f32x4>(ra + 1024) + *lds_pl<f32x4>(ra + 1536));
      if (lane >= 32) O = f32x4{(lw[0] + lw[1]) + (lw[2] + lw[3]), 0.f, 0.f, 0.f};
      st128(ws, lane < 33 ? WS_PO + b * REC + lane * 16 : OOB, noempty4(O));
    }
  }
  if (!mlp) {
    const unsigned hb = WS_PO + (4 * og + wave) * 8 * REC;
    v2u po[8];
    unsigned ls = EMPTY;
#pragma unroll
    for (int s = 0; s < 8; ++s) po[s] = v2u{0u, 0u};
    unsigned miss = 0x1FFu;
    for (unsigned it = 0;; ++it) {
      MEMFENCE();
      if (miss & 0x100u) ls = ld32<SC1>(ws, (lane & 15) < 8 ? hb + (lane & 15) * REC + 512 : OOB);
#pragma unroll
      for (int s = 0; s < 8; ++s) if (miss & (1u << s)) po[s] = ld64<SC1>(ws, hb + s * REC + lane * 8);
      if ((miss & 0x100u) && __all(ls != EMPTY)) miss &= ~0x100u;
#pragma unroll
      for (int s = 0; s < 8; ++s) if ((miss & (1u << s)) && __all(mx2(0u, po[s]) != EMPTY)) miss &= ~(1u << s);
      if (miss == 0u || EXPIRED(it)) break;
    }
    const float inv = __builtin_amdgcn_rcpf(row_sum(asf(ls)));
    const unsigned op = cvtpk((((asf(po[0][0]) + asf(po[1][0])) + (asf(po[2][0]) + asf(po[3][0]))) + ((asf(po[4][0]) + asf(po[5][0])) + (asf(po[6][0]) + asf(po[7][0])))) * inv,
                              (((asf(po[0][1]) + asf(po[1][1])) + (asf(po[2][1]) + asf(po[3][1]))) + ((asf(po[4][1]) + asf(po[5][1])) + (asf(po[6][1]) + asf(po[7][1])))) * inv);
    float y[8] = {0.f, 0.f, 0.f, 0.f, 0.f, 0.f, 0.f, 0.f};
    dot8(y, dppu<0x00>(op), wv[0], wv[1]); dot8(y, dppu<0x55>(op), wv[2], wv[3]);
    dot8(y, dppu<0xAA>(op), wv[4], wv[5]); dot8(y, dppu<0xFF>(op), wv[6], wv[7]);
    float u0, u1;
    rows_reduce8(y, u0, u1);
    u0 += dpp<ROR8>(u0); u1 += dpp<ROR8>(u1); u0 += dpp<ROR4>(u0); u1 += dpp<ROR4>(u1);
    *lds_pl<f32x2>(lb + L_RED3 + wave * 128 + (8 * (c & 3) + 2 * g) * 4) = f32x2{u0, u1};
    wg_barrier();
    if (wave == 0) {
      const unsigned ra = lb + L_RED3 + (lane & 7) * 16;
      const f32x4 t = (*lds_pl<f32x4>(ra) + *lds_pl<f32x4>(ra + 128)) + (*lds_pl<f32x4>(ra + 256) + *lds_pl<f32x4>(ra + 384));
      st128(ws, lane < 8 ? WS_YP + (og * 512 + oc0) * 4 + lane * 16 : OOB, noempty4(t));
    }
    return;
  }
  lds_wait_ge(lb + L_M2RDY, 1u, t0);
  float gbf = lo16(*lds_pl<u16>(lb + L_M2 + 3072 + (fc0 + g) * 2));
  {
    const unsigned gaw = *lds_pl<unsigned>(lb + L_M2 + tid * 4), sbw = *lds_pl<unsigned>(lb + L_M2 + 1024 + tid * 4), cbw = *lds_pl<unsigned>(lb + L_M2 + 2048 + tid * 4);
    float ga0 = lo16(gaw), ga1 = hi16(gaw), sb0 = lo16(sbw), sb1 = hi16(sbw), t0c = rbf(1.f + lo16(cbw)), t1c = rbf(1.f + hi16(cbw)), xa = lo16(x2), xb = hi16(x2);
    asm volatile("" : "+v"(ga0), "+v"(ga1), "+v"(sb0), "+v"(sb1), "+v"(t0c), "+v"(t1c), "+v"(xa), "+v"(xb), "+v"(gbf));
    v2u yp[8];
#pragma unroll
    for (int s = 0; s < 8; ++s) yp[s] = v2u{0u, 0u};
    unsigned miss = 0xFFu;
    for (unsigned it = 0;; ++it) {
      MEMFENCE();
#pragma unroll
      for (int s = 0; s < 8; ++s) if (miss & (1u << s)) yp[s] = ld64<SC1>(ws, WS_YP + s * 2048 + tid * 8);
#pragma unroll
      for (int s = 0; s < 8; ++s) if ((miss & (1u << s)) && __all(mx2(0u, yp[s]) != EMPTY)) miss &= ~(1u << s);
      if (miss == 0u || EXPIRED(it)) break;
    }
    const unsigned atp = cvtpk(((asf(yp[0][0]) + asf(yp[1][0])) + (asf(yp[2][0]) + asf(yp[3][0]))) + ((asf(yp[4][0]) + asf(yp[5][0])) + (asf(yp[6][0]) + asf(yp[7][0]))),
                               ((asf(yp[0][1]) + asf(yp[1][1])) + (asf(yp[2][1]) + asf(yp[3][1]))) + ((asf(yp[4][1]) + asf(yp[5][1])) + (asf(yp[6][1]) + asf(yp[7][1]))));
    const unsigned gap = cvtpk(ga0 * lo16(atp), ga1 * hi16(atp));
    const unsigned x1p = cvtpk(xa + lo16(gap), xb + hi16(gap));
    const float x1a = lo16(x1p), x1b = hi16(x1p);
    const float sh = asf(__builtin_amdgcn_readfirstlane(asu(x1a)));
    const float da = x1a - sh, db = x1b - sh;
    const float s1 = wave_sum(da + db), s2 = wave_sum(fmaf(da, da, db * db));
    *lds_pl<f32x2>(lb + L_ST + wave * 8) = f32x2{fmaf(s1, 1.f / 128.f, sh), fmaf(-s1 * (1.f / 128.f), s1, s2)};
    *lds_pl<f32x2>(lb + L_X1 + tid * 8) = f32x2{x1a, x1b};
    wg_barrier();
    if (has_gu) {
      const f32x4 sA = *lds_pl<f32x4>(lb + L_ST), sB = *lds_pl<f32x4>(lb + L_ST + 16);
      const float mean = ((sA[0] + sA[2]) + (sB[0] + sB[2])) * 0.25f;
      const float d0 = sA[0] - mean, d1 = sA[2] - mean, d2 = sB[0] - mean, d3 = sB[2] - mean;
      const float var = fmaxf(((sA[1] + sA[3]) + (sB[1] + sB[3])) + 128.f * ((d0 * d0 + d1 * d1) + (d2 * d2 + d3 * d3)), 0.f) * (1.f / 512.f);
      float mb2, sd2;
      ln_round(mean, var, mb2, sd2);
      const float rs = __builtin_amdgcn_rcpf(sd2);
      const unsigned e0 = cvtpk(x1a - mb2, x1b - mb2), e1 = cvtpk(lo16(e0) * rs, hi16(e0) * rs), e2 = cvtpk(lo16(e1) * t0c, hi16(e1) * t1c);
      const unsigned hq = cvtpk(lo16(e2) + sb0, hi16(e2) + sb1);
      float y[8] = {0.f, 0.f, 0.f, 0.f, 0.f, 0.f, 0.f, 0.f};
      dot8(y, dppu<0x00>(hq), wv[0], wv[1]); dot8(y, dppu<0x55>(hq), wv[2], wv[3]);
      dot8(y, dppu<0xAA>(hq), wv[4], wv[5]); dot8(y, dppu<0xFF>(hq), wv[6], wv[7]);
      float u0, u1;
      rows_reduce8(y, u0, u1);
      u0 += dpp<ROR8>(u0); u1 += dpp<ROR8>(u1); u0 += dpp<ROR4>(u0); u1 += dpp<ROR4>(u1);
      *lds_pl<f32x2>(lb + L_RED3 + wave * 128 + (8 * (c & 3) + 2 * g) * 4) = f32x2{u0, u1};
      wg_barrier();
      const unsigned ra = lb + L_RED3 + c * 4;
      const float gs = rbf((*lds_pl<float>(ra) + *lds_pl<float>(ra + 128)) + (*lds_pl<float>(ra + 256) + *lds_pl<float>(ra + 384)));
      const float us = rbf((*lds_pl<float>(ra + 64) + *lds_pl<float>(ra + 192)) + (*lds_pl<float>(ra + 320) + *lds_pl<float>(ra + 448)));
      const float z = 0.7978845608028654f * fmaf(0.044715f * gs * gs, gs, gs);
      const float th = fmaf(-2.f, __builtin_amdgcn_rcpf(1.f + __expf(2.f * z)), 1.f);
      const float h2 = rbf(rbf(0.5f * gs * (1.f + th)) * us);
      const unsigned hp = __builtin_amdgcn_perm(asu(dpp<XOR1>(h2)), asu(h2), 0x07060302u);
      float d0a = 0.f, d1a = 0.f;
#pragma unroll
      for (int e = 0; e < 16; e += 2) {
        const unsigned he = __builtin_amdgcn_readlane(hp, e);
        d0a = dot2(wdv[e], he, d0a); d1a = dot2(wdv[e + 1], he, d1a);
      }
      st64(ws, WS_DP + up * 2048 + tid * 8, v2u{noempty(asu(d0a)), noempty(asu(d1a))});
      st64(ws, (b | tid) == 0 ? WS_EPOCH : OOB, v2u{tag.lo, tag.hi});
    }
  }
  if (wave != 0) return;
  {
    v4u da = {EMPTY, 0u, 0u, 0u}, db = {lane < NGU - 64 ? EMPTY : 0u, 0u, 0u, 0u};
    const float x1o = *lds_pl<float>(lb + L_X1 + (fc0 + g) * 4);
    for (unsigned it = 0;; ++it) {
      MEMFENCE();
      const bool ma = mx4(0u, da) == EMPTY, mb_ = mx4(0u, db) == EMPTY;
      if (!__any(ma | mb_) || EXPIRED(it)) break;
      const v4u na = ld128<SC1>(ws, ma ? WS_DP + lane * 2048 + fc0 * 4 : OOB), nb = ld128<SC1>(ws, mb_ ? WS_DP + (lane + 64) * 2048 + fc0 * 4 : OOB);
      da = ma ? na : da; db = mb_ ? nb : db;
    }
    float t4[4];
#pragma unroll
    for (int r = 0; r < 4; ++r) t4[r] = asf(da[r]) + asf(db[r]);
    const float r = row_sum(sw16_add(sw32_add(t4[0], t4[2]), sw32_add(t4[1], t4[3])));
    const float res = x1o + rbf(gbf * rbf(r));
    if (c == 0) a.out[fc0 + g] = (u16)(cvtpk(res, res) & 0xffffu);
  }
}
}

extern "C" void kernel_launch(void* const* d_in, const int* in_sizes, int n_in, void* d_out, int out_size, void* d_ws, size_t ws_size,
                              hipStream_t stream) {
  Args a;
  a.x = (const u16*)d_in[0]; a.K = (const u16*)d_in[1]; a.V = (const u16*)d_in[2]; a.wq = (const u16*)d_in[3]; a.wo = (const u16*)d_in[4];
  a.wg = (const u16*)d_in[6]; a.wu = (const u16*)d_in[7]; a.wd = (const u16*)d_in[8]; a.c = (const u16*)d_in[9]; a.wm = (const u16*)d_in[10];
  a.out = (u16*)d_out; a.ws = (unsigned char*)d_ws;
  (void)hipFuncSetAttribute((const void*)block_decode, hipFuncAttributeMaxDynamicSharedMemorySize, (int)LDS_BYTES);
  hipLaunchKernelGGL(block_decode, dim3(NWG), dim3(NTHR), LDS_BYTES, stream, a);
}
```

```cpp
#include <hip/hip_runtime.h>
#include <hip/hip_bf16.h>

#define GAS __attribute__((address_space(1)))
#define LAS __attribute__((address_space(3)))
typedef unsigned short u16;
typedef unsigned long long u64;
typedef unsigned v4u __attribute__((ext_vector_type(4)));
typedef unsigned v2u __attribute__((ext_vector_type(2)));
typedef int v4i __attribute__((ext_vector_type(4)));
typedef float f32x4 __attribute__((ext_vector_type(4)));
typedef __bf16 bf16x8 __attribute__((ext_vector_type(8)));
typedef __bf16 bf16x2 __attribute__((ext_vector_type(2)));
typedef float f32x2 __attribute__((ext_vector_type(2)));

#define PNS 150
#define PWT 20
namespace {
constexpr int DM = 512, SK = 65536, DH = 128, DF = 1792;
constexpr int NWG = 256, NTHR = 384;
constexpr int SLOT = 16384, NSLOT = 8, FH = 1024;
constexpr int SO = 116;
constexpr int NP = 6;
constexpr int POOL0 = 8 * SO, KD0 = POOL0 + 8 * NP, NKT = (FH - KD0) / 4, NPT = 8 * NP / 2;
constexpr int BURST = 3;
constexpr int CAP = 144;
static_assert(NP > BURST && (FH - KD0) % 4 == 0 && SO % 4 == 0 && NP % 2 == 0 && 8 * NP + 16 <= SO && NP <= 8 && 8 * (CAP - 7 - SO) >= FH - KD0, "fill plan");
constexpr int PACE_FROM = 16, PACE_WAIT = 20, PACE_PAUSE = 9;
constexpr unsigned TW = 10800, TD = 110;
constexpr int INF = 0x7FFFFFFF;
constexpr unsigned HEAD_BYTES = (unsigned)SK * DH * 2;
constexpr unsigned EMPTY = 0xFFFFFFFFu;
constexpr unsigned OOB = 0x7FFF0000u;
constexpr u64 TLIMIT = 5000000ull;
constexpr float LOG2E = 1.4426950408889634f, SCALE = 0.08838834764831845f;
constexpr int NGU = DF / 16, REC = 640;
constexpr unsigned WS_EPOCH = 0, WS_P1 = 4096, WS_QS = WS_P1 + 32768, WS_M2P = WS_QS + 131072, WS_M2 = WS_M2P + 131072, WS_CTR = WS_M2 + 16384,
                   WS_MX = WS_CTR + 8192, WS_YP = WS_MX + 2048, WS_PO = WS_YP + 16384, WS_DP = WS_PO + NWG * REC,
                   WS_SG = WS_DP + NGU * 2048, WS_END = WS_SG + NWG * NP * 128;
constexpr unsigned L_RING = 0, L_CTL = NSLOT * SLOT, L_FT = L_CTL, L_SCR = L_CTL + 8, L_NK = L_CTL + 12, L_DONE = L_CTL + 16, L_QCNT = L_CTL + 32,
                   L_AUXPUB = L_CTL + 36, L_QIN = L_CTL + 40, L_ISSUED = L_CTL + 44, L_M2RDY = L_CTL + 48, L_TKC = L_CTL + 52, L_TK = L_CTL + 56,
                   L_KCNT = L_CTL + 64, L_MH = L_CTL + 68, L_MXW = L_CTL + 80, L_LW = L_CTL + 96, L_LIST = L_CTL + 128, L_RED = L_LIST + 128, L_QSC = L_RED + 1024, L_RED3 = L_QSC + 256,
                   L_ST = L_RED3 + 512, L_M2 = L_ST + 64, L_X1 = L_M2 + 4096, L_MRG = L_X1 + 2048, L_SH = L_X1, L_SC = L_MRG + 2048, L_END = L_SC + CAP * 128;
constexpr unsigned LDS_BYTES = L_END;
static_assert(LDS_BYTES <= 160 * 1024 && LDS_BYTES > 80 * 1024, "one workgroup per CU");

struct Args {
  const u16 *x, *K, *V, *wq, *wo, *wg, *wu, *wd, *c, *wm;
  u16* out;
  unsigned char* ws;
};

__device__ __forceinline__ u64 rt() { return __builtin_amdgcn_s_memrealtime(); }
__device__ __forceinline__ float asf(unsigned u) { return __builtin_bit_cast(float, u); }
__device__ __forceinline__ unsigned asu(float f) { return __builtin_bit_cast(unsigned, f); }
__device__ __forceinline__ float lo16(unsigned u) { return asf(u << 16); }
__device__ __forceinline__ float hi16(unsigned u) { return asf(u & 0xffff0000u); }
__device__ __forceinline__ unsigned cvtpk(float a, float b) {
  const f32x2 v = {a, b};
  return __builtin_bit_cast(unsigned, __builtin_convertvector(v, bf16x2));
}
__device__ __forceinline__ float rbf(float f) { return lo16(cvtpk(f, f)); }
__device__ __forceinline__ unsigned noempty(unsigned v) { return v < EMPTY - 1u ? v : EMPTY - 1u; }
__device__ __forceinline__ float dot2(unsigned a, unsigned b, float c) {
  return __builtin_amdgcn_fdot2_f32_bf16(__builtin_bit_cast(bf16x2, a), __builtin_bit_cast(bf16x2, b), c, false);
}
template <int CTRL> __device__ __forceinline__ float dpp(float v) {
  return asf(__builtin_amdgcn_mov_dpp(asu(v), CTRL, 0xf, 0xf, true));
}
constexpr int ROR8 = 0x128, ROR4 = 0x124, ROR2 = 0x122, ROR1 = 0x121, XOR2 = 0x4E, XOR1 = 0xB1;
template <int CTRL> __device__ __forceinline__ unsigned dppu(unsigned v) { return __builtin_amdgcn_mov_dpp(v, CTRL, 0xf, 0xf, true); }
__device__ __forceinline__ float sw32_add(float a, float b) {
  const auto r = __builtin_amdgcn_permlane32_swap(asu(a), asu(b), false, false);
  return asf(r[0]) + asf(r[1]);
}
__device__ __forceinline__ float sw16_add(float a, float b) {
  const auto r = __builtin_amdgcn_permlane16_swap(asu(a), asu(b), false, false);
  return asf(r[0]) + asf(r[1]);
}
__device__ __forceinline__ float xrow_max(float v) {
  const auto r = __builtin_amdgcn_permlane16_swap(asu(v), asu(v), false, false);
  v = fmaxf(asf(r[0]), asf(r[1]));
  const auto s = __builtin_amdgcn_permlane32_swap(asu(v), asu(v), false, false);
  return fmaxf(asf(s[0]), asf(s[1]));
}
__device__ __forceinline__ unsigned pack_halves(float v) {
  const auto r = __builtin_amdgcn_permlane32_swap(asu(v), asu(v), false, false);
  return __builtin_amdgcn_perm(r[1], r[0], 0x07060302u);
}
__device__ __forceinline__ float row_sum(float v) { v += dpp<ROR8>(v); v += dpp<ROR4>(v); v += dpp<ROR2>(v); v += dpp<ROR1>(v); return v; }
__device__ __forceinline__ float row_max(float v) { v = fmaxf(v, dpp<ROR8>(v)); v = fmaxf(v, dpp<ROR4>(v)); v = fmaxf(v, dpp<ROR2>(v)); return fmaxf(v, dpp<ROR1>(v)); }
__device__ __forceinline__ float wave_sum(float v) { v = row_sum(v); v = sw16_add(v, v); return sw32_add(v, v); }
__device__ __forceinline__ void rows_reduce8(const float (&a)[8], float& u0, float& u1) {
  const float r0 = sw32_add(a[0], a[4]), r1 = sw32_add(a[1], a[5]), r2 = sw32_add(a[2], a[6]), r3 = sw32_add(a[3], a[7]);
  u0 = sw16_add(r0, r2); u1 = sw16_add(r1, r3);
}
__device__ __forceinline__ void fma8(float (&a)[8], float h, v4u w) {
#pragma unroll
  for (int e = 0; e < 4; ++e) { a[2 * e] = fmaf(h, lo16(w[e]), a[2 * e]); a[2 * e + 1] = fmaf(h, hi16(w[e]), a[2 * e + 1]); }
}
__device__ __forceinline__ void kpair(v4u& a, v4u& b) {
  const v4u r0 = a, r1 = b;
  a = v4u{__builtin_amdgcn_perm(r1[0], r0[0], 0x05040100u), __builtin_amdgcn_perm(r1[0], r0[0], 0x07060302u),
          __builtin_amdgcn_perm(r1[1], r0[1], 0x05040100u), __builtin_amdgcn_perm(r1[1], r0[1], 0x07060302u)};
  b = v4u{__builtin_amdgcn_perm(r1[2], r0[2], 0x05040100u), __builtin_amdgcn_perm(r1[2], r0[2], 0x07060302u),
          __builtin_amdgcn_perm(r1[3], r0[3], 0x05040100u), __builtin_amdgcn_perm(r1[3], r0[3], 0x07060302u)};
}
__device__ __forceinline__ void dot8(float (&acc)[8], unsigned hp, v4u a, v4u b) {
#pragma unroll
  for (int e = 0; e < 4; ++e) { acc[e] = dot2(a[e], hp, acc[e]); acc[4 + e] = dot2(b[e], hp, acc[4 + e]); }
}
__device__ __forceinline__ f32x4 mfma(v4u a, v4u b, f32x4 c) {
  return __builtin_amdgcn_mfma_f32_16x16x32_bf16(__builtin_bit_cast(bf16x8, a), __builtin_bit_cast(bf16x8, b), c, 0, 0, 0);
}
typedef __amdgpu_buffer_rsrc_t rsrc_t;
__device__ __forceinline__ rsrc_t mkrsrc(const void* p, unsigned bytes) { return __builtin_amdgcn_make_buffer_rsrc((void*)p, 0, bytes, 0x00020000); }
constexpr int SC1 = 16, NT = 2;
template <int AUX> __device__ __forceinline__ v4u ld128(rsrc_t r, unsigned off) { return __builtin_amdgcn_raw_buffer_load_b128(r, off, 0, AUX); }
template <int AUX> __device__ __forceinline__ v2u ld64(rsrc_t r, unsigned off) { return __builtin_amdgcn_raw_buffer_load_b64(r, off, 0, AUX); }
template <int AUX> __device__ __forceinline__ unsigned ld32(rsrc_t r, unsigned off) { return __builtin_amdgcn_raw_buffer_load_b32(r, off, 0, AUX); }
__device__ __forceinline__ void st128(rsrc_t r, unsigned off, v4u v) { __builtin_amdgcn_raw_buffer_store_b128(v, r, off, 0, SC1); }
__device__ __forceinline__ void st64(rsrc_t r, unsigned off, v2u v) { __builtin_amdgcn_raw_buffer_store_b64(v, r, off, 0, SC1); }
__device__ __forceinline__ void st32(rsrc_t r, unsigned off, unsigned v) { __builtin_amdgcn_raw_buffer_store_b32(v, r, off, 0, SC1); }
__device__ __forceinline__ unsigned umax(unsigned a, unsigned b) { return a > b ? a : b; }
__device__ __forceinline__ unsigned mx4(unsigned m, v4u v) { return umax(umax(umax(m, v[0]), v[1]), umax(v[2], v[3])); }
__device__ __forceinline__ unsigned mx2(unsigned m, v2u v) { return umax(umax(m, v[0]), v[1]); }
__device__ __forceinline__ v4u noempty4(f32x4 v) { return v4u{noempty(asu(v[0])), noempty(asu(v[1])), noempty(asu(v[2])), noempty(asu(v[3]))}; }
struct Tag { unsigned lo, hi; };
template <int LB> __device__ __forceinline__ unsigned tg_hi(Tag t) { return LB ? __builtin_amdgcn_alignbit(t.hi, t.lo, LB) : t.lo; }
template <int LB> __device__ __forceinline__ unsigned gr_lo(float v, Tag t) { return ((asu(v) + ((1u << LB) >> 1)) & (~0u << LB)) | (t.lo & ((1u << LB) - 1u)); }
template <int LB> __device__ __forceinline__ v2u gr1(float a, Tag t) { return v2u{gr_lo<LB>(a, t), tg_hi<LB>(t)}; }
template <int LB> __device__ __forceinline__ v4u gr2(float a, float b, Tag t) { return v4u{gr_lo<LB>(a, t), tg_hi<LB>(t), gr_lo<LB>(b, t), tg_hi<LB>(t)}; }
template <int LB> __device__ __forceinline__ unsigned gr_bad(v4u g, Tag t) {
  return (((g[0] ^ t.lo) | (g[2] ^ t.lo)) & ((1u << LB) - 1u)) | (g[1] ^ tg_hi<LB>(t)) | (g[3] ^ tg_hi<LB>(t));
}
template <int LB> __device__ __forceinline__ float gr_val(unsigned lo) { return asf(lo & (~0u << LB)); }
template <int LB> __device__ __forceinline__ float gr_sum(v4u g) { const float s = sw32_add(gr_val<LB>(g[0]), gr_val<LB>(g[2])); return sw16_add(s, s); }
__device__ __forceinline__ Tag next_tag(v2u ep) { const u64 e = (((u64)ep[1] << 32) | ep[0]) + 1ull; return Tag{(unsigned)e, (unsigned)(e >> 32)}; }
__device__ __forceinline__ v4u sel4(bool t, v4u a, v4u b) { return v4u{t ? a[0] : b[0], 0u, t ? a[2] : b[2], 0u}; }
#define MEMFENCE() asm volatile("" ::: "memory")
#define LGKM0() asm volatile("s_waitcnt lgkmcnt(0)" ::: "memory")
#define VM0() asm volatile("s_waitcnt vmcnt(0)" ::: "memory")
#define PIN4(a) asm volatile("" : "+v"((a)[0]), "+v"((a)[1]), "+v"((a)[2]), "+v"((a)[3]))
template <int N> __device__ __forceinline__ void vmwait() { asm volatile("s_waitcnt vmcnt(%0)" ::"n"(N) : "memory"); }
__device__ __forceinline__ void wg_barrier() { LGKM0(); __builtin_amdgcn_s_barrier(); MEMFENCE(); }
template <typename T> __device__ __forceinline__ volatile LAS T* lds_at(unsigned off) { return (volatile LAS T*)(size_t)off; }
template <typename T> __device__ __forceinline__ LAS T* lds_pl(unsigned off) { return (LAS T*)(size_t)off; }
__device__ __forceinline__ void lds_wait_ge(unsigned addr, unsigned need, u64 t0) {
  for (unsigned sp = 0; *lds_at<unsigned>(addr) < need; ++sp) { __builtin_amdgcn_s_sleep(1); if ((sp & 63u) == 63u && rt() - t0 > TLIMIT) break; }
  MEMFENCE();
}

#define EXPIRED(it) (((it) & 63u) == 63u && rt() - t0 > TLIMIT)

#define DMA4(M, O) "s_mov_b32 m0, " M "\n\ts_nop 0\n\t" \
                   "buffer_load_dwordx4 %[v0], %[d], " O " offen nt lds\n\tbuffer_load_dwordx4 %[v1], %[d], " O " offen offset:1024 nt lds\n\t" \
                   "buffer_load_dwordx4 %[v2], %[d], " O " offen offset:2048 nt lds\n\tbuffer_load_dwordx4 %[v3], %[d], " O " offen offset:3072 nt lds\n\t"
template <int WAIT> __device__ __forceinline__ void dma_fill(v4i srd, unsigned o, unsigned sl, unsigned v0, unsigned v1, unsigned v2, unsigned v3) {
  asm volatile("s_waitcnt vmcnt(%[w])\n\t" DMA4("%[m0]", "%[o0]") DMA4("%[m1]", "%[o1]") DMA4("%[m2]", "%[o2]") DMA4("%[m3]", "%[o3]")
               ::[w] "n"(WAIT), [d] "s"(srd), [v0] "v"(v0), [v1] "v"(v1), [v2] "v"(v2), [v3] "v"(v3),
                 [m0] "s"(sl), [m1] "s"(sl + 4096), [m2] "s"(sl + 8192), [m3] "s"(sl + 12288),
                 [o0] "s"(o), [o1] "s"(o + 4096), [o2] "s"(o + 32768), [o3] "s"(o + 36864) : "memory");
}

__device__ __forceinline__ float silu_bf(float v) {
  const float t = -v * LOG2E, r = fmaf(-v, 1.9259629911e-8f, fmaf(-v, LOG2E, -t));
  const float e = __builtin_amdgcn_exp2f(t);
  return rbf(v / (1.f + (e < INFINITY ? fmaf(e * 0.6931471806f, r, e) : e)));
}
__device__ __forceinline__ void ln_round(float mean, float var, float& mb, float& sd) {
  mb = rbf(mean);
  sd = rbf(__builtin_amdgcn_sqrtf(rbf(rbf(var) + 1e-5f)));
}
__device__ __forceinline__ float mod_round(float n, float sc, float sh) { return rbf(rbf(rbf(n) * rbf(1.f + sc)) + sh); }

__global__ void __launch_bounds__(NTHR, 2) block_decode(Args a) {
  extern __shared__ __attribute__((aligned(16))) unsigned char smem[];
  const int tid = threadIdx.x, lane = tid & 63, wave = __builtin_amdgcn_readfirstlane(tid >> 6);
  const int b = blockIdx.x, x = b & 7, j = b >> 3;
  const u64 t0 = rt();
  const unsigned lb = (unsigned)(size_t)(LAS unsigned char*)smem;
  const rsrc_t ws = mkrsrc(a.ws, WS_END);
  const int kq = j >> 3, cb = x + 8 * (j & 7);
  const int gq = j >> 2;
  const bool mlp = (x & 1) == 0;
  const int og = (x & 6) + (j >> 4), oc0 = 32 * (j & 15);
  const bool has_gu = mlp && 4 * gq + (x >> 1) < 28;
  const int up = 4 * (4 * gq + (x >> 1)) + (j & 3);
  const int fc0 = 64 * x + 4 * j;

  if (tid < 20) *lds_pl<unsigned>(lb + L_CTL + 4 * tid) = (tid == 1 || tid == 3) ? (unsigned)INF : 0u;
  asm volatile("" :: "s"(a.K), "s"(a.ws));
  wg_barrier();
  int w_a = wave, w_b = wave;
  asm volatile("" : "+s"(w_a));
  if (w_a == 4) {
    const u64 kb = (u64)(a.K + (size_t)j * SK * DH), vb = (u64)(a.V + (size_t)j * SK * DH);
    const v4i ksrd = {(int)kb, (int)(kb >> 32), (int)HEAD_BYTES, 0x00020000}, vsrd = {(int)vb, (int)(vb >> 32), (int)HEAD_BYTES, 0x00020000};
    const unsigned g = lane >> 4, c = lane & 15;
    const unsigned k0 = g * 256 + ((c ^ g) << 4), k1 = g * 256 + ((c ^ (4 + g)) << 4), k2 = g * 256 + ((c ^ (8 + g)) << 4), k3 = g * 256 + ((c ^ (12 + g)) << 4);
    const unsigned vl = lane * 16;
    unsigned sl = lb + L_RING;
    auto foff = [](int n) { return ((unsigned)n >> 2) * 65536u + ((unsigned)n & 3u) * 8192u; };
    auto step = [&]() { sl = sl + SLOT == lb + L_RING + NSLOT * SLOT ? lb + L_RING : sl + SLOT; };
    volatile LAS u64* FT = lds_at<u64>(lb + L_FT);
    int pub = 0;
    unsigned total = (unsigned)INF;
    auto publish = [&](int n) { pub = n > pub ? n : pub; *FT = (u64)(unsigned)pub | ((u64)total << 32); };
    auto wait_free = [&](int i) {
      const int p = i - NSLOT;
      volatile LAS u64* dn = lds_at<u64>(lb + L_DONE + 8 * (p & 1));
      const unsigned need = (unsigned)(p >> 1) + 1u;
      auto freed = [&]() { const u64 d = *dn; return (unsigned)d >= need && (unsigned)(d >> 32) >= need; };
      if (!freed()) {
        VM0(); publish(i);
        for (unsigned sp = 0; !freed(); ++sp) { __builtin_amdgcn_s_sleep(1); if (EXPIRED(sp)) break; }
      }
      MEMFENCE();
    };
    for (unsigned sp = 0; *lds_at<unsigned>(lb + L_ISSUED) < 5u && sp < 4096u; ++sp) {}
    unsigned period = 0, c0 = (unsigned)__builtin_amdgcn_s_memtime(), r0 = (unsigned)rt();
    auto beat = [&]() {
      vmwait<PWT>();
      const unsigned t1 = (unsigned)__builtin_amdgcn_s_memtime();
      while ((unsigned)__builtin_amdgcn_s_memtime() - t1 < period) {}
    };
    auto measure = [&]() {
      const unsigned c1 = (unsigned)__builtin_amdgcn_s_memtime(), r1 = (unsigned)rt();
      period = __builtin_amdgcn_readfirstlane((unsigned)((float)(c1 - c0) * (PNS * 0.1f) * __builtin_amdgcn_rcpf((float)(r1 - r0))));
      c0 = c1; r0 = r1;
    };
    unsigned tn = 1;
    auto take = [&]() {
      u64 tv;
      for (unsigned sp = 0; (unsigned)((tv = *lds_at<u64>(lb + L_TK)) >> 32) < tn; ++sp) if (EXPIRED(sp)) { tv = EMPTY; break; }
      ++tn;
      return (unsigned)__builtin_amdgcn_readfirstlane((unsigned)tv);
    };
#pragma unroll
    for (int i = 0; i < BURST; ++i) { dma_fill<63>(ksrd, foff(POOL0 + x * NP + i), sl, k0, k1, k2, k3); step(); }
    int i = BURST, nt = 0, next = POOL0 + x * NP + BURST, left = NP - BURST;
    bool asked = true;
    for (;; ++i) {
      if (i < PACE_FROM) vmwait<16>();
      beat();
      dma_fill<63>(ksrd, foff(next), sl, k0, k1, k2, k3);
      if ((i & 15) == 0) measure();
      step();
      ++next; --left;
      vmwait<32>();
      publish(i - 1);
      if (i == NP + SO - 5) *lds_at<unsigned>(lb + L_TKC) = tn;
      if (left == 0) {
        if (i == NP - 1) { next = x * SO; left = SO; }
        else {
          const unsigned t = asked ? take() : EMPTY;
          if (t >= (unsigned)NKT) break;
          left = 4; next = KD0 + 4 * (int)t;
          *lds_at<unsigned>(lb + L_LIST + 4 * nt) = t; ++nt;
          asked = i + 1 - NP + left + 4 <= CAP;
          if (asked) *lds_at<unsigned>(lb + L_TKC) = tn;
        }
      }
      if (i + 1 >= NSLOT) wait_free(i + 1);
    }
    const int nk = i + 1, vown = 2 * nk - NP;
    *lds_at<unsigned>(lb + L_NK) = (unsigned)nk;
    wait_free(i + 1);
    next = x * SO; left = SO;
    int rt_ = 0;
    for (++i;; ++i) {
      beat();
      dma_fill<63>(vsrd, foff(next), sl, vl, vl, vl, vl);
      if ((i & 15) == 0) measure();
      step();
      ++next; --left;
      vmwait<32>();
      publish(i - 1);
      if (i == vown - 3) *lds_at<unsigned>(lb + L_TKC) = tn;
      if (left == 0) {
        if (rt_ < nt) { const unsigned t = __builtin_amdgcn_readfirstlane(*lds_at<unsigned>(lb + L_LIST + 4 * rt_)); ++rt_; left = 4; next = KD0 + 4 * (int)t; }
        else {
          const unsigned t = take();
          if (t >= (unsigned)NPT) break;
          *lds_at<unsigned>(lb + L_TKC) = tn;
          left = 2; next = POOL0 + 2 * (int)t;
        }
      }
      wait_free(i + 1);
    }
    total = (unsigned)i + 1u;
    publish(i - 1);
    vmwait<16>(); publish(i);
    VM0(); publish(i + 1);
    return;
  }

  asm volatile("" : "+s"(w_b));
  if (w_b == 5) {
    __builtin_amdgcn_s_setprio(3);
    const rsrc_t rwm = mkrsrc(a.wm, DM * 3072 * 2), rc = mkrsrc(a.c, DM * 2);
    const int n0 = 512 * (gq & 1) + 64 * x + 16 * (j & 3);
    const int ar = 128 * kq + 4 * (lane >> 1);
    const v2u c4 = ld64<0>(rc, ar * 2);
    MEMFENCE();
    v4u w1[4];
#pragma unroll
    for (int i = 0; i < 4; ++i) w1[i] = ld128<NT>(rwm, (unsigned)((ar + i) * 3072 + n0 + 8 * (lane & 1)) * 2);
    const v2u ep = ld64<SC1>(ws, WS_EPOCH);
    const int mr = 64 * gq + 8 * (lane >> 3), mc = 64 * (x + 8 * (j & 3));
    const v4u cv = ld128<0>(rc, mr * 2);
    MEMFENCE();
    if (lane == 0) __hip_atomic_fetch_add(lds_pl<unsigned>(lb + L_ISSUED), 1u, __ATOMIC_RELAXED, __HIP_MEMORY_SCOPE_WORKGROUP);
    st128(ws, (lane < 8 && !mlp) ? WS_YP + (og * 512 + oc0) * 4 + lane * 16 : OOB, v4u{EMPTY, EMPTY, EMPTY, EMPTY});
    st128(ws, has_gu ? WS_DP + up * 2048 + lane * 16 : OOB, v4u{EMPTY, EMPTY, EMPTY, EMPTY});
    st128(ws, has_gu ? WS_DP + up * 2048 + 1024 + lane * 16 : OOB, v4u{EMPTY, EMPTY, EMPTY, EMPTY});
    st128(ws, lane < 33 ? WS_PO + b * REC + lane * 16 : OOB, v4u{EMPTY, EMPTY, EMPTY, EMPTY});
    st32(ws, (lane < 2 && x == 0) ? WS_CTR + j * 256 + lane * 128 : OOB, 0u);
    const Tag tag = next_tag(ep);
    {
      float s8[8] = {0.f, 0.f, 0.f, 0.f, 0.f, 0.f, 0.f, 0.f};
      fma8(s8, silu_bf(lo16(c4[0])), w1[0]); fma8(s8, silu_bf(hi16(c4[0])), w1[1]);
      fma8(s8, silu_bf(lo16(c4[1])), w1[2]); fma8(s8, silu_bf(hi16(c4[1])), w1[3]);
      float u0, u1;
      rows_reduce8(s8, u0, u1);
      u0 += dpp<ROR8>(u0); u1 += dpp<ROR8>(u1); u0 += dpp<ROR4>(u0); u1 += dpp<ROR4>(u1); u0 += dpp<XOR2>(u0); u1 += dpp<XOR2>(u1);
      const int col = n0 + 8 * (lane & 1) + 2 * (lane >> 4);
      st128(ws, (lane & 15) < 2 ? WS_P1 + (kq * 1024 + col) * 8 : OOB, gr2<0>(u0, u1, tag));
      *lds_at<unsigned>(lb + L_AUXPUB) = 1u;
    }
    __builtin_amdgcn_s_setprio(0);
    auto stream_at = [&](unsigned fillno) {
      for (unsigned sp = 0; *lds_at<unsigned>(lb + L_FT) < fillno; ++sp) { __builtin_amdgcn_s_sleep(8); if ((sp & 63u) == 63u && rt() - t0 > TLIMIT) break; }
      MEMFENCE();
    };
    stream_at(NSLOT + 4);
    {
      v4u w2[8];
#pragma unroll
      for (int i = 0; i < 8; ++i) w2[i] = ld128<NT>(rwm, (unsigned)((mr + i) * 3072 + 1024 + mc + 8 * (lane & 7)) * 2);
      float s8[8] = {0.f, 0.f, 0.f, 0.f, 0.f, 0.f, 0.f, 0.f};
#pragma unroll
      for (int e = 0; e < 4; ++e) { fma8(s8, silu_bf(lo16(cv[e])), w2[2 * e]); fma8(s8, silu_bf(hi16(cv[e])), w2[2 * e + 1]); }
      float u0, u1;
      rows_reduce8(s8, u0, u1);
      u0 += dpp<ROR8>(u0); u1 += dpp<ROR8>(u1);
      st128(ws, (lane & 8) == 0 ? WS_M2P + (gq * 2048 + mc + 8 * (lane & 7) + 2 * (lane >> 4)) * 8 : OOB, gr2<4>(u0, u1, tag));
    }
    stream_at(NSLOT + 16);
    {
      v4u m;
      for (;;) {
        MEMFENCE();
        m = ld128<SC1>(ws, WS_M2P + (((lane >> 2) & 7) * 2048 + 8 * b + 2 * (lane & 3)) * 8);
        if (__all(gr_bad<4>(m, tag) == 0u) || rt() - t0 > TLIMIT) break;
        __builtin_amdgcn_s_sleep(32);
      }
      float u0 = gr_val<4>(m[0]), u1 = gr_val<4>(m[2]);
      u0 = sw16_add(u0, u0); u1 = sw16_add(u1, u1);
      u0 += dpp<ROR8>(u0); u1 += dpp<ROR8>(u1); u0 += dpp<ROR4>(u0); u1 += dpp<ROR4>(u1);
      VM0();
      st128(ws, lane < 4 ? WS_M2 + b * 64 + lane * 16 : OOB, gr2<4>(rbf(u0), rbf(u1), tag));
    }
    stream_at(NSLOT + 28);
#pragma unroll 1
    for (int u = 0; u < 16; ++u) {
      v4u m;
      for (;;) {
        MEMFENCE();
        m = ld128<SC1>(ws, WS_M2 + u * 1024 + lane * 16);
        if (__all(gr_bad<4>(m, tag) == 0u) || rt() - t0 > TLIMIT) break;
        __builtin_amdgcn_s_sleep(32);
      }
      *lds_pl<unsigned>(lb + L_M2 + u * 256 + lane * 4) = __builtin_amdgcn_perm(m[2], m[0], 0x07060302u);
      __builtin_amdgcn_s_sleep(16);
    }
    LGKM0();
    *lds_at<unsigned>(lb + L_M2RDY) = 1u;
    GAS unsigned* ctr = (GAS unsigned*)(a.ws + WS_CTR + j * 256);
    unsigned n = 1;
    auto draw = [&](int which) {
      unsigned v = 0;
      if (lane == 0) v = __hip_atomic_fetch_add(ctr + 32 * which, 1u, __ATOMIC_RELAXED, __HIP_MEMORY_SCOPE_AGENT);
      v = __builtin_amdgcn_readfirstlane(v);
      *lds_at<u64>(lb + L_TK) = ((u64)n << 32) | v;
      ++n;
      return v;
    };
    for (;;) {
      bool over = false;
      for (unsigned sp = 0; *lds_at<unsigned>(lb + L_TKC) < n; ++sp) {
        if (*lds_at<unsigned>(lb + L_NK) != (unsigned)INF || EXPIRED(sp)) { over = true; break; }
        __builtin_amdgcn_s_sleep(2);
      }
      if (over || *lds_at<unsigned>(lb + L_NK) != (unsigned)INF || draw(0) >= (unsigned)NKT) break;
    }
    lds_wait_ge(lb + L_KCNT, 5u, t0);
    __builtin_amdgcn_s_sleep(127);
    {
      v2u mx;
      for (;;) {
        MEMFENCE();
        mx = ld64<SC1>(ws, WS_MX + j * 64 + (lane & 7) * 8);
        if (__all(gr_bad<4>(v4u{mx[0], mx[1], mx[0], mx[1]}, tag) == 0u) || rt() - t0 > TLIMIT) break;
        __builtin_amdgcn_s_sleep(32);
      }
      *lds_at<float>(lb + L_MH) = row_max(gr_val<4>(mx[0]));
      LGKM0();
    }
    {
      unsigned dq = 0, pend = 0;
      v4u sc = {0u, 0u, 0u, 0u};
      auto flush = [&]() {
        if ((unsigned)lane < 8u * pend) *lds_pl<v4u>(lb + L_SC + dq * 128 + lane * 16) = sc;
        dq += pend; pend = 0;
        LGKM0();
        *lds_at<unsigned>(lb + L_SCR) = dq;
      };
      for (;;) {
        if (__builtin_amdgcn_readfirstlane(*lds_at<unsigned>(lb + L_TKC)) < n) flush();
        for (unsigned sp = 0; *lds_at<unsigned>(lb + L_TKC) < n; ++sp) { __builtin_amdgcn_s_sleep(2); if (EXPIRED(sp)) break; }
        const unsigned v = draw(1);
        flush();
        if (v >= (unsigned)NPT || rt() - t0 > TLIMIT) break;
        pend = 2u;
        sc = ld128<SC1 | NT>(ws, lane < 16 ? WS_SG + (j * 8 * NP + 2 * v) * 128 + lane * 16 : OOB);
        MEMFENCE();
      }
    }
    return;
  }

  const int g = lane >> 4, c = lane & 15;
  const rsrc_t rx = mkrsrc(a.x, DM * 2), rwq = mkrsrc(a.wq, DM * 4096 * 2);
  const rsrc_t rwd = mkrsrc(a.wd, DF * DM * 2);
  const int r0 = 128 * kq + 32 * wave;
  v4u wqv[4];
#pragma unroll
  for (int i = 0; i < 4; ++i) wqv[i] = ld128<NT>(rwq, (unsigned)((r0 + 4 * (lane >> 3) + i) * 4096 + 64 * cb + 8 * (lane & 7)) * 2);
  const v2u ep = ld64<SC1>(ws, WS_EPOCH);
  const v4u x8 = ld128<0>(rx, lane * 16);
  const unsigned x2 = ld32<0>(rx, tid * 4);
  const unsigned xh = ld32<0>(rx, (r0 + 2 * c) * 2);
  MEMFENCE();
  if (lane == 0) __hip_atomic_fetch_add(lds_pl<unsigned>(lb + L_ISSUED), 1u, __ATOMIC_RELAXED, __HIP_MEMORY_SCOPE_WORKGROUP);
  float mb, sd;
  {
    float xs[8];
#pragma unroll
    for (int e = 0; e < 4; ++e) { xs[2 * e] = lo16(x8[e]); xs[2 * e + 1] = hi16(x8[e]); }
    float s = 0.f;
#pragma unroll
    for (int e = 0; e < 8; ++e) s += xs[e];
    const float mean = wave_sum(s) * (1.f / 512.f);
    float v = 0.f;
#pragma unroll
    for (int e = 0; e < 8; ++e) { const float d = xs[e] - mean; v = fmaf(d, d, v); }
    ln_round(mean, wave_sum(v) * (1.f / 512.f), mb, sd);
  }
  float nrow = rbf((lane >= 32 ? hi16(xh) : lo16(xh)) - mb) / sd;
  kpair(wqv[0], wqv[1]); kpair(wqv[2], wqv[3]);
  PIN4(wqv); asm volatile("" : "+v"(nrow));
  const Tag tag = next_tag(ep);
  lds_wait_ge(lb + L_AUXPUB, 1u, t0);
  {
    const unsigned o1 = WS_P1 + (g * 1024 + r0 + 2 * c) * 8;
    v4u gs = {0u, 0u, 0u, 0u}, gc = gs;
    bool bad = true;
    __builtin_amdgcn_s_sleep(32);
    for (;;) {
      MEMFENCE();
      const v4u ns_ = ld128<SC1>(ws, bad ? o1 : OOB), nc_ = ld128<SC1>(ws, bad ? o1 + 4096 : OOB);
      const bool tk = bad & ((gr_bad<0>(ns_, tag) | gr_bad<0>(nc_, tag)) == 0u);
      gs = sel4(tk, ns_, gs); gc = sel4(tk, nc_, gc); bad = bad & !tk;
      if (!__any(bad) || rt() - t0 > TLIMIT) break;
    }
    const unsigned hp = pack_halves(mod_round(nrow, rbf(gr_sum<0>(gc)), rbf(gr_sum<0>(gs))));
    float acc[8] = {0.f, 0.f, 0.f, 0.f, 0.f, 0.f, 0.f, 0.f};
    dot8(acc, __builtin_amdgcn_ds_bpermute((lane >> 3) * 8, hp), wqv[0], wqv[1]);
    dot8(acc, __builtin_amdgcn_ds_bpermute((lane >> 3) * 8 + 4, hp), wqv[2], wqv[3]);
    float u0, u1;
    rows_reduce8(acc, u0, u1);
    u0 += dpp<ROR8>(u0); u1 += dpp<ROR8>(u1);
    if ((lane & 8) == 0) *lds_pl<f32x2>(lb + L_RED + wave * 256 + (8 * (lane & 7) + 2 * g) * 4) = f32x2{u0, u1};
    LGKM0();
    unsigned old = 0;
    if (lane == 0) old = __hip_atomic_fetch_add(lds_pl<unsigned>(lb + L_QCNT), 1u, __ATOMIC_RELAXED, __HIP_MEMORY_SCOPE_WORKGROUP);
    old = __builtin_amdgcn_readfirstlane(old);
    MEMFENCE();
    if (old == 3u) {
      const unsigned ra = lb + L_RED + lane * 4;
      const float qp = (*lds_pl<float>(ra) + *lds_pl<float>(ra + 256)) + (*lds_pl<float>(ra + 512) + *lds_pl<float>(ra + 768));
      st64(ws, WS_QS + (kq * 4096 + 64 * cb + lane) * 8, gr1<2>(qp, tag));
    }
  }
  const int r8 = 8 * (tid >> 2);
  v4u wv[8];
  unsigned wdv[16];
#pragma unroll
  for (int i = 0; i < 8; ++i) { wv[i] = v4u{0u, 0u, 0u, 0u}; wdv[2 * i] = 0u; wdv[2 * i + 1] = 0u; }
  const GAS v4u* wp = (const GAS v4u*)(mlp ? ((tid & 2) ? a.wu : a.wg) + (r8 * DF + 16 * (has_gu ? up : 0) + 8 * (tid & 1)) : a.wo + ((512 * og + r8) * DM + oc0 + 8 * (tid & 3)));
  const int wstride = mlp ? DF / 8 : DM / 8;
  const unsigned wdo = has_gu ? (unsigned)(16 * up * DM + 2 * tid) * 2 : OOB;

  const unsigned key = 4 * (c & 3) + (c >> 2);
  unsigned ka[4], msk[4];
#pragma unroll
  for (int st = 0; st < 4; ++st) {
    ka[st] = lb + L_RING + (wave & 1) * 8192 + key * 256 + (((st + 4 * g) ^ key) << 4);
    msk[st] = ((c & 7) >> 1) == st ? ((c & 1) ? 0xffff0000u : 0x0000ffffu) : 0u;
  }
  const unsigned va = lb + L_RING + (wave & 1) * 8192 + lane * 16;
  const bool is_hi = c < 8;
  volatile LAS unsigned* DN = lds_at<unsigned>(lb + L_DONE + 4 * wave);
  unsigned so = (wave >> 1) * SLOT;
  v4u sv[8], sw[8], ud[8], qf[4];
#pragma unroll
  for (int i = 0; i < 8; ++i) { sv[i] = v4u{0u, 0u, 0u, 0u}; sw[i] = sv[i]; ud[i] = sv[i]; }
  int ns = 0;
  auto release = [&](v4u (&d)[8], int i) {
    LGKM0();
    *DN = (unsigned)(i >> 1) + 1u;
    asm volatile("" : "+v"(d[0]), "+v"(d[1]), "+v"(d[2]), "+v"(d[3]), "+v"(d[4]), "+v"(d[5]), "+v"(d[6]), "+v"(d[7]) :: "memory");
    so = (so + 2 * SLOT) & (NSLOT * SLOT - 1);
  };
  auto read_k = [&](v4u (&d)[8], int i) {
#pragma unroll
    for (int st = 0; st < 4; ++st) { d[st] = *lds_pl<v4u>(ka[st] + so); d[4 + st] = *lds_pl<v4u>(ka[st] + so + 4096); }
    release(d, i);
  };
  int total = INF, nk = INF;
  auto wait_full = [&](int i, bool in_k) {
    for (unsigned sp = 0;; ++sp) {
      const v4u ct = *(volatile LAS v4u*)(size_t)(lb + L_FT);
      total = __builtin_amdgcn_readfirstlane((int)ct[1]);
      nk = __builtin_amdgcn_readfirstlane((int)ct[3]);
      if (EXPIRED(sp)) { total = 0; nk = nk < i ? nk : i; break; }
      if (__builtin_amdgcn_readfirstlane((int)ct[0]) > i || (in_k && nk <= i)) break;
      if (total == INF) __builtin_amdgcn_s_sleep(1);
    }
    MEMFENCE();
  };
  float mraw = -INFINITY;
  auto scores = [&](const v4u (&d)[8], int i) {
    f32x4 sa = {0.f, 0.f, 0.f, 0.f}, sb = {0.f, 0.f, 0.f, 0.f};
#pragma unroll
    for (int st = 0; st < 4; ++st) { sa = mfma(d[st], qf[st], sa); sb = mfma(d[4 + st], qf[st], sb); }
    mraw = fmaxf(fmaxf(fmaxf(mraw, sa[0]), fmaxf(sa[1], sa[2])), fmaxf(fmaxf(sa[3], sb[0]), fmaxf(fmaxf(sb[1], sb[2]), sb[3])));
    v4u pk = {cvtpk(sa[0], sa[1]), cvtpk(sa[2], sa[3]), cvtpk(sb[0], sb[1]), cvtpk(sb[2], sb[3])};
#pragma unroll
    for (int u = 0; u < 4; ++u) pk[u] = cvtpk(lo16(pk[u]) * SCALE, hi16(pk[u]) * SCALE);
    const unsigned so_ = (wave & 1) * 64 + g * 16;
    const unsigned dst = i < NP ? L_SH + i * 128 : L_SC + (i - NP) * 128;
    if (c == 0) *lds_pl<v4u>(lb + dst + so_) = pk;
  };
  auto stash = [&]() {
    const int full = __builtin_amdgcn_readfirstlane((int)*lds_at<unsigned>(lb + L_FT)), i0 = wave >> 1;
    MEMFENCE();
    if (ns == 0 && full > i0) { read_k(wv, i0); ns = 1; }
    if (ns == 1 && full > i0 + 2) { read_k(sv, i0 + 2); ns = 2; }
    if (ns == 2 && full > i0 + 4) { read_k(sw, i0 + 4); ns = 3; }
    if (ns == 3 && full > i0 + 6) { read_k(ud, i0 + 6); ns = 4; }
  };
  {
    const unsigned o2 = WS_QS + (g * 4096 + 128 * j + 32 * wave + 2 * c) * 8;
    v4u gq4 = {0u, 0u, 0u, 0u};
    bool bad = true;
    stash();
    {
      const v4u nq = ld128<SC1>(ws, o2);
      bad = gr_bad<2>(nq, tag) != 0u;
      gq4 = nq;
    }
    asm volatile("" : "+v"(gq4) :: "memory");
    stash();
    for (unsigned it = 0; __any(bad) && !EXPIRED(it); ++it) {
      MEMFENCE();
      const v4u nq = ld128<SC1>(ws, bad ? o2 : OOB);
      const bool tk = bad & (gr_bad<2>(nq, tag) == 0u);
      gq4 = sel4(tk, nq, gq4); bad = bad & !tk;
    }
    *lds_pl<unsigned>(lb + L_QSC + (16 * wave + c) * 4) = pack_halves(rbf(gr_sum<2>(gq4)));
    LGKM0();
    if (lane == 0) __hip_atomic_fetch_add(lds_pl<unsigned>(lb + L_QIN), 1u, __ATOMIC_RELAXED, __HIP_MEMORY_SCOPE_WORKGROUP);
    lds_wait_ge(lb + L_QIN, 4u, t0);
#pragma unroll
    for (int st = 0; st < 4; ++st) qf[st] = *lds_pl<v4u>(lb + L_QSC + (st + 4 * g) * 16);
  }
  if (ns >= 1) scores(wv, wave >> 1);
  if (ns >= 2) scores(sv, (wave >> 1) + 2);
  if (ns >= 3) scores(sw, (wave >> 1) + 4);
  if (ns >= 4) scores(ud, (wave >> 1) + 6);
  int i = (wave >> 1) + 2 * ns;
  for (;; i += 2) {
    wait_full(i, true);
    if (nk <= i) break;
    read_k(ud, i);
    scores(ud, i);
  }
  float m2;
  {
    *lds_pl<float>(lb + L_MXW + wave * 4) = rbf(rbf(xrow_max(mraw)) * SCALE);
    LGKM0();
    unsigned old = 0;
    if (lane == 0) old = __hip_atomic_fetch_add(lds_pl<unsigned>(lb + L_KCNT), 1u, __ATOMIC_RELAXED, __HIP_MEMORY_SCOPE_WORKGROUP);
    old = __builtin_amdgcn_readfirstlane(old);
    lds_wait_ge(lb + L_KCNT, 4u, t0);
    const f32x4 mw = *lds_pl<f32x4>(lb + L_MXW);
    const float mg = fmaxf(fmaxf(mw[0], mw[1]), fmaxf(mw[2], mw[3]));
    if (old == 3u) {
#pragma unroll
      for (int u = 0; u < (NP + 7) / 8; ++u)
        __builtin_amdgcn_raw_buffer_store_b128(*lds_pl<v4u>(lb + L_SH + u * 1024 + lane * 16), ws,
                                               u * 1024 + lane * 16 < NP * 128 ? WS_SG + b * NP * 128 + u * 1024 + lane * 16 : OOB, 0, SC1 | NT);
      VM0();
      st64(ws, lane == 0 ? WS_MX + b * 8 : OOB, gr1<4>(mg, tag));
      *lds_at<unsigned>(lb + L_KCNT) = 5u;
    }
    m2 = mg * LOG2E;
  }
  float lsum = 0.f;
  f32x4 acc = {0.f, 0.f, 0.f, 0.f};
  {
    int wk = (mlp && !has_gu) ? 11 : 0;
    bool done = false, own = true;
    for (;; i += 2) {
      int e = i - nk;
      if (!done && e >= nk - NP) {
        e -= nk - NP;
        for (unsigned sp = 0;; ++sp) {
          const v4u ct = *(volatile LAS v4u*)(size_t)(lb + L_FT);
          if (__builtin_amdgcn_readfirstlane((int)ct[2]) > e) break;
          if (__builtin_amdgcn_readfirstlane((int)ct[1]) <= i || EXPIRED(sp)) { done = true; break; }
          __builtin_amdgcn_s_sleep(1);
        }
        MEMFENCE();
        if (own) {
          const float mh = *lds_at<float>(lb + L_MH) * LOG2E, al = __builtin_amdgcn_exp2f(m2 - mh);
          acc *= al; lsum *= al; m2 = mh; own = false;
        }
      }
      if (!done) {
        const v4u sc = *lds_pl<v4u>(lb + L_SC + e * 128 + (wave & 1) * 64 + g * 16);
        v4u pa[8];
#pragma unroll
        for (int u = 0; u < 8; ++u) {
          const float p = __builtin_amdgcn_exp2f(fmaf((u & 1) ? hi16(sc[u >> 1]) : lo16(sc[u >> 1]), LOG2E, -m2));
          lsum += p;
          const float ph = hi16(asu(p));
          const float xv = is_hi ? ph : p - ph;
          const unsigned pk = cvtpk(xv, xv);
          pa[u] = v4u{pk & msk[0], pk & msk[1], pk & msk[2], pk & msk[3]};
        }
        asm volatile("" : "+v"(pa[0]), "+v"(pa[1]), "+v"(pa[2]), "+v"(pa[3]), "+v"(pa[4]), "+v"(pa[5]), "+v"(pa[6]), "+v"(pa[7]));
        wait_full(i, false);
#pragma unroll
        for (int u = 0; u < 8; ++u) ud[u] = *lds_pl<v4u>(va + so + u * 1024);
        release(ud, i);
#pragma unroll
        for (int u = 0; u < 8; ++u) acc = mfma(pa[u], ud[u], acc);
        done = i + 2 >= total && !own;
      }
      MEMFENCE();
      if (wk <= 10 && (done || rt() - t0 >= (u64)(TW + TD * wk + (wk == 10 ? 400 : 0)))) {
#pragma unroll
        for (int u = 0; u < 8; ++u) if (wk == u) wv[u] = __builtin_nontemporal_load(wp + u * wstride);
#pragma unroll
        for (int v = 0; v < 2; ++v) if (wk == 8 + v) {
#pragma unroll
          for (int u = 8 * v; u < 8 * v + 8; ++u) wdv[u] = ld32<NT>(rwd, wdo + u * (DM * 2));
        }
        if (wk == 10) {
#pragma unroll
          for (int u = 0; u < 8; u += 2) kpair(wv[u], wv[u + 1]);
#pragma unroll
          for (int u = 0; u < 16; u += 2) {
            const unsigned lo = __builtin_amdgcn_perm(wdv[u + 1], wdv[u], 0x05040100u), hi = __builtin_amdgcn_perm(wdv[u + 1], wdv[u], 0x07060302u);
            wdv[u] = lo; wdv[u + 1] = hi;
          }
        }
        ++wk;
      }
      MEMFENCE();
      if (done && wk > 10) break;
    }
  }
  {
    f32x4 o;
#pragma unroll
    for (int r = 0; r < 4; ++r) o[r] = sw32_add(acc[r], acc[r]);
    float l = sw16_add(lsum, lsum); l = sw32_add(l, l);
    *lds_pl<f32x4>(lb + L_MRG + wave * 512 + (8 * c + 4 * (g & 1)) * 4) = o;
    *lds_pl<float>(lb + L_LW + wave * 4) = l;
    wg_barrier();
    if (wave == 0) {
      const unsigned ra = lb + L_MRG + (lane & 31) * 16;
      const f32x4 lw = *lds_pl<f32x4>(lb + L_LW);
      f32x4 O = (*lds_pl<f32x4>(ra) + *lds_pl<f32x4>(ra + 512)) + (*lds_pl<f32x4>(ra + 1024) + *lds_pl<f32x4>(ra + 1536));
      if (lane >= 32) O = f32x4{(lw[0] + lw[1]) + (lw[2] + lw[3]), 0.f, 0.f, 0.f};
      st128(ws, lane < 33 ? WS_PO + b * REC + lane * 16 : OOB, noempty4(O));
    }
  }
  if (!mlp) {
    const unsigned hb = WS_PO + (4 * og + wave) * 8 * REC;
    v2u po[8];
    unsigned ls = EMPTY;
#pragma unroll
    for (int s = 0; s < 8; ++s) po[s] = v2u{0u, 0u};
    unsigned miss = 0x1FFu;
    for (unsigned it = 0;; ++it) {
      MEMFENCE();
      if (miss & 0x100u) ls = ld32<SC1>(ws, (lane & 15) < 8 ? hb + (lane & 15) * REC + 512 : OOB);
#pragma unroll
      for (int s = 0; s < 8; ++s) if (miss & (1u << s)) po[s] = ld64<SC1>(ws, hb + s * REC + lane * 8);
      if ((miss & 0x100u) && __all(ls != EMPTY)) miss &= ~0x100u;
#pragma unroll
      for (int s = 0; s < 8; ++s) if ((miss & (1u << s)) && __all(mx2(0u, po[s]) != EMPTY)) miss &= ~(1u << s);
      if (miss == 0u || EXPIRED(it)) break;
    }
    const float inv = __builtin_amdgcn_rcpf(row_sum(asf(ls)));
    const unsigned op = cvtpk((((asf(po[0][0]) + asf(po[1][0])) + (asf(po[2][0]) + asf(po[3][0]))) + ((asf(po[4][0]) + asf(po[5][0])) + (asf(po[6][0]) + asf(po[7][0])))) * inv,
                              (((asf(po[0][1]) + asf(po[1][1])) + (asf(po[2][1]) + asf(po[3][1]))) + ((asf(po[4][1]) + asf(po[5][1])) + (asf(po[6][1]) + asf(po[7][1])))) * inv);
    float y[8] = {0.f, 0.f, 0.f, 0.f, 0.f, 0.f, 0.f, 0.f};
    dot8(y, dppu<0x00>(op), wv[0], wv[1]); dot8(y, dppu<0x55>(op), wv[2], wv[3]);
    dot8(y, dppu<0xAA>(op), wv[4], wv[5]); dot8(y, dppu<0xFF>(op), wv[6], wv[7]);
    float u0, u1;
    rows_reduce8(y, u0, u1);
    u0 += dpp<ROR8>(u0); u1 += dpp<ROR8>(u1); u0 += dpp<ROR4>(u0); u1 += dpp<ROR4>(u1);
    *lds_pl<f32x2>(lb + L_RED3 + wave * 128 + (8 * (c & 3) + 2 * g) * 4) = f32x2{u0, u1};
    wg_barrier();
    if (wave == 0) {
      const unsigned ra = lb + L_RED3 + (lane & 7) * 16;
      const f32x4 t = (*lds_pl<f32x4>(ra) + *lds_pl<f32x4>(ra + 128)) + (*lds_pl<f32x4>(ra + 256) + *lds_pl<f32x4>(ra + 384));
      st128(ws, lane < 8 ? WS_YP + (og * 512 + oc0) * 4 + lane * 16 : OOB, noempty4(t));
    }
    return;
  }
  lds_wait_ge(lb + L_M2RDY, 1u, t0);
  float gbf = lo16(*lds_pl<u16>(lb + L_M2 + 3072 + (fc0 + g) * 2));
  {
    const unsigned gaw = *lds_pl<unsigned>(lb + L_M2 + tid * 4), sbw = *lds_pl<unsigned>(lb + L_M2 + 1024 + tid * 4), cbw = *lds_pl<unsigned>(lb + L_M2 + 2048 + tid * 4);
    float ga0 = lo16(gaw), ga1 = hi16(gaw), sb0 = lo16(sbw), sb1 = hi16(sbw), t0c = rbf(1.f + lo16(cbw)), t1c = rbf(1.f + hi16(cbw)), xa = lo16(x2), xb = hi16(x2);
    asm volatile("" : "+v"(ga0), "+v"(ga1), "+v"(sb0), "+v"(sb1), "+v"(t0c), "+v"(t1c), "+v"(xa), "+v"(xb), "+v"(gbf));
    v2u yp[8];
#pragma unroll
    for (int s = 0; s < 8; ++s) yp[s] = v2u{0u, 0u};
    unsigned miss = 0xFFu;
    for (unsigned it = 0;; ++it) {
      MEMFENCE();
#pragma unroll
      for (int s = 0; s < 8; ++s) if (miss & (1u << s)) yp[s] = ld64<SC1>(ws, WS_YP + s * 2048 + tid * 8);
#pragma unroll
      for (int s = 0; s < 8; ++s) if ((miss & (1u << s)) && __all(mx2(0u, yp[s]) != EMPTY)) miss &= ~(1u << s);
      if (miss == 0u || EXPIRED(it)) break;
    }
    const unsigned atp = cvtpk(((asf(yp[0][0]) + asf(yp[1][0])) + (asf(yp[2][0]) + asf(yp[3][0]))) + ((asf(yp[4][0]) + asf(yp[5][0])) + (asf(yp[6][0]) + asf(yp[7][0]))),
                               ((asf(yp[0][1]) + asf(yp[1][1])) + (asf(yp[2][1]) + asf(yp[3][1]))) + ((asf(yp[4][1]) + asf(yp[5][1])) + (asf(yp[6][1]) + asf(yp[7][1]))));
    const unsigned gap = cvtpk(ga0 * lo16(atp), ga1 * hi16(atp));
    const unsigned x1p = cvtpk(xa + lo16(gap), xb + hi16(gap));
    const float x1a = lo16(x1p), x1b = hi16(x1p);
    const float sh = asf(__builtin_amdgcn_readfirstlane(asu(x1a)));
    const float da = x1a - sh, db = x1b - sh;
    const float s1 = wave_sum(da + db), s2 = wave_sum(fmaf(da, da, db * db));
    *lds_pl<f32x2>(lb + L_ST + wave * 8) = f32x2{fmaf(s1, 1.f / 128.f, sh), fmaf(-s1 * (1.f / 128.f), s1, s2)};
    *lds_pl<f32x2>(lb + L_X1 + tid * 8) = f32x2{x1a, x1b};
    wg_barrier();
    if (has_gu) {
      const f32x4 sA = *lds_pl<f32x4>(lb + L_ST), sB = *lds_pl<f32x4>(lb + L_ST + 16);
      const float mean = ((sA[0] + sA[2]) + (sB[0] + sB[2])) * 0.25f;
      const float d0 = sA[0] - mean, d1 = sA[2] - mean, d2 = sB[0] - mean, d3 = sB[2] - mean;
      const float var = fmaxf(((sA[1] + sA[3]) + (sB[1] + sB[3])) + 128.f * ((d0 * d0 + d1 * d1) + (d2 * d2 + d3 * d3)), 0.f) * (1.f / 512.f);
      float mb2, sd2;
      ln_round(mean, var, mb2, sd2);
      const float rs = __builtin_amdgcn_rcpf(sd2);
      const unsigned e0 = cvtpk(x1a - mb2, x1b - mb2), e1 = cvtpk(lo16(e0) * rs, hi16(e0) * rs), e2 = cvtpk(lo16(e1) * t0c, hi16(e1) * t1c);
      const unsigned hq = cvtpk(lo16(e2) + sb0, hi16(e2) + sb1);
      float y[8] = {0.f, 0.f, 0.f, 0.f, 0.f, 0.f, 0.f, 0.f};
      dot8(y, dppu<0x00>(hq), wv[0], wv[1]); dot8(y, dppu<0x55>(hq), wv[2], wv[3]);
      dot8(y, dppu<0xAA>(hq), wv[4], wv[5]); dot8(y, dppu<0xFF>(hq), wv[6], wv[7]);
      float u0, u1;
      rows_reduce8(y, u0, u1);
      u0 += dpp<ROR8>(u0); u1 += dpp<ROR8>(u1); u0 += dpp<ROR4>(u0); u1 += dpp<ROR4>(u1);
      *lds_pl<f32x2>(lb + L_RED3 + wave * 128 + (8 * (c & 3) + 2 * g) * 4) = f32x2{u0, u1};
      wg_barrier();
      const unsigned ra = lb + L_RED3 + c * 4;
      const float gs = rbf((*lds_pl<float>(ra) + *lds_pl<float>(ra + 128)) + (*lds_pl<float>(ra + 256) + *lds_pl<float>(ra + 384)));
      const float us = rbf((*lds_pl<float>(ra + 64) + *lds_pl<float>(ra + 192)) + (*lds_pl<float>(ra + 320) + *lds_pl<float>(ra + 448)));
      const float z = 0.7978845608028654f * fmaf(0.044715f * gs * gs, gs, gs);
      const float th = fmaf(-2.f, __builtin_amdgcn_rcpf(1.f + __expf(2.f * z)), 1.f);
      const float h2 = rbf(rbf(0.5f * gs * (1.f + th)) * us);
      const unsigned hp = __builtin_amdgcn_perm(asu(dpp<XOR1>(h2)), asu(h2), 0x07060302u);
      float d0a = 0.f, d1a = 0.f;
#pragma unroll
      for (int e = 0; e < 16; e += 2) {
        const unsigned he = __builtin_amdgcn_readlane(hp, e);
        d0a = dot2(wdv[e], he, d0a); d1a = dot2(wdv[e + 1], he, d1a);
      }
      st64(ws, WS_DP + up * 2048 + tid * 8, v2u{noempty(asu(d0a)), noempty(asu(d1a))});
      st64(ws, (b | tid) == 0 ? WS_EPOCH : OOB, v2u{tag.lo, tag.hi});
    }
  }
  if (wave != 0) return;
  {
    v4u da = {EMPTY, 0u, 0u, 0u}, db = {lane < NGU - 64 ? EMPTY : 0u, 0u, 0u, 0u};
    const float x1o = *lds_pl<float>(lb + L_X1 + (fc0 + g) * 4);
    for (unsigned it = 0;; ++it) {
      MEMFENCE();
      const bool ma = mx4(0u, da) == EMPTY, mb_ = mx4(0u, db) == EMPTY;
      if (!__any(ma | mb_) || EXPIRED(it)) break;
      const v4u na = ld128<SC1>(ws, ma ? WS_DP + lane * 2048 + fc0 * 4 : OOB), nb = ld128<SC1>(ws, mb_ ? WS_DP + (lane + 64) * 2048 + fc0 * 4 : OOB);
      da = ma ? na : da; db = mb_ ? nb : db;
    }
    float t4[4];
#pragma unroll
    for (int r = 0; r < 4; ++r) t4[r] = asf(da[r]) + asf(db[r]);
    const float r = row_sum(sw16_add(sw32_add(t4[0], t4[2]), sw32_add(t4[1], t4[3])));
    const float res = x1o + rbf(gbf * rbf(r));
    if (c == 0) a.out[fc0 + g] = (u16)(cvtpk(res, res) & 0xffffu);
  }
}
}

extern "C" void kernel_launch(void* const* d_in, const int* in_sizes, int n_in, void* d_out, int out_size, void* d_ws, size_t ws_size,
                              hipStream_t stream) {
  Args a;
  a.x = (const u16*)d_in[0]; a.K = (const u16*)d_in[1]; a.V = (const u16*)d_in[2]; a.wq = (const u16*)d_in[3]; a.wo = (const u16*)d_in[4];
  a.wg = (const u16*)d_in[6]; a.wu = (const u16*)d_in[7]; a.wd = (const u16*)d_in[8]; a.c = (const u16*)d_in[9]; a.wm = (const u16*)d_in[10];
  a.out = (u16*)d_out; a.ws = (unsigned char*)d_ws;
  (void)hipFuncSetAttribute((const void*)block_decode, hipFuncAttributeMaxDynamicSharedMemorySize, (int)LDS_BYTES);
  hipLaunchKernelGGL(block_decode, dim3(NWG), dim3(NTHR), LDS_BYTES, stream, a);
}
```

```cpp
#include <hip/hip_runtime.h>
#include <hip/hip_bf16.h>

#define GAS __attribute__((address_space(1)))
#define LAS __attribute__((address_space(3)))
typedef unsigned short u16;
typedef unsigned long long u64;
typedef unsigned v4u __attribute__((ext_vector_type(4)));
typedef unsigned v2u __attribute__((ext_vector_type(2)));
typedef int v4i __attribute__((ext_vector_type(4)));
typedef float f32x4 __attribute__((ext_vector_type(4)));
typedef __bf16 bf16x8 __attribute__((ext_vector_type(8)));
typedef __bf16 bf16x2 __attribute__((ext_vector_type(2)));
typedef float f32x2 __attribute__((ext_vector_type(2)));

namespace {
constexpr int DM = 512, SK = 65536, DH = 128, DF = 1792;
constexpr int NWG = 256, NTHR = 384;
constexpr int SLOT = 16384, NSLOT = 8, FH = 1024;
constexpr int SO = 116;
constexpr int NP = 6;
constexpr int POOL0 = 8 * SO, KD0 = POOL0 + 8 * NP, NKT = (FH - KD0) / 4, NPT = 8 * NP / 2;
constexpr int BURST = 3;
constexpr int CAP = 144;
static_assert(NP > BURST && (FH - KD0) % 4 == 0 && SO % 4 == 0 && NP % 2 == 0 && 8 * NP + 16 <= SO && NP <= 8 && 8 * (CAP - 7 - SO) >= FH - KD0, "fill plan");
constexpr int PACE_FROM = 16, PACE_WAIT = 20;
constexpr float PACE_TICKS = 17.f;
constexpr unsigned TW = 10800, TD = 110;
constexpr int INF = 0x7FFFFFFF;
constexpr unsigned HEAD_BYTES = (unsigned)SK * DH * 2;
constexpr unsigned EMPTY = 0xFFFFFFFFu;
constexpr unsigned OOB = 0x7FFF0000u;
constexpr u64 TLIMIT = 5000000ull;
constexpr float LOG2E = 1.4426950408889634f, SCALE = 0.08838834764831845f;
constexpr int NGU = DF / 16, REC = 640;
constexpr unsigned WS_EPOCH = 0, WS_P1 = 4096, WS_QS = WS_P1 + 32768, WS_M2P = WS_QS + 131072, WS_M2 = WS_M2P + 131072, WS_CTR = WS_M2 + 16384,
                   WS_MX = WS_CTR + 8192, WS_YP = WS_MX + 2048, WS_PO = WS_YP + 16384, WS_DP = WS_PO + NWG * REC,
                   WS_SG = WS_DP + NGU * 2048, WS_END = WS_SG + NWG * NP * 128;
constexpr unsigned L_RING = 0, L_CTL = NSLOT * SLOT, L_FT = L_CTL, L_SCR = L_CTL + 8, L_NK = L_CTL + 12, L_DONE = L_CTL + 16, L_QCNT = L_CTL + 32,
                   L_AUXPUB = L_CTL + 36, L_QIN = L_CTL + 40, L_ISSUED = L_CTL + 44, L_M2RDY = L_CTL + 48, L_TKC = L_CTL + 52, L_TK = L_CTL + 56,
                   L_KCNT = L_CTL + 64, L_MH = L_CTL + 68, L_MXW = L_CTL + 80, L_LW = L_CTL + 96, L_LIST = L_CTL + 128, L_RED = L_LIST + 128, L_QSC = L_RED + 1024, L_RED3 = L_QSC + 256,
                   L_ST = L_RED3 + 512, L_M2 = L_ST + 64, L_X1 = L_M2 + 4096, L_MRG = L_X1 + 2048, L_SH = L_X1, L_SC = L_MRG + 2048, L_END = L_SC + CAP * 128;
constexpr unsigned LDS_BYTES = L_END;
static_assert(LDS_BYTES <= 160 * 1024 && LDS_BYTES > 80 * 1024, "one workgroup per CU");

struct Args {
  const u16 *x, *K, *V, *wq, *wo, *wg, *wu, *wd, *c, *wm;
  u16* out;
  unsigned char* ws;
};

__device__ __forceinline__ u64 rt() { return __builtin_amdgcn_s_memrealtime(); }
__device__ __forceinline__ float asf(unsigned u) { return __builtin_bit_cast(float, u); }
__device__ __forceinline__ unsigned asu(float f) { return __builtin_bit_cast(unsigned, f); }
__device__ __forceinline__ float lo16(unsigned u) { return asf(u << 16); }
__device__ __forceinline__ float hi16(unsigned u) { return asf(u & 0xffff0000u); }
__device__ __forceinline__ unsigned cvtpk(float a, float b) {
  const f32x2 v = {a, b};
  return __builtin_bit_cast(unsigned, __builtin_convertvector(v, bf16x2));
}
__device__ __forceinline__ float rbf(float f) { return lo16(cvtpk(f, f)); }
__device__ __forceinline__ unsigned noempty(unsigned v) { return v < EMPTY - 1u ? v : EMPTY - 1u; }
__device__ __forceinline__ float dot2(unsigned a, unsigned b, float c) {
  return __builtin_amdgcn_fdot2_f32_bf16(__builtin_bit_cast(bf16x2, a), __builtin_bit_cast(bf16x2, b), c, false);
}
template <int CTRL> __device__ __forceinline__ float dpp(float v) {
  return asf(__builtin_amdgcn_mov_dpp(asu(v), CTRL, 0xf, 0xf, true));
}
constexpr int ROR8 = 0x128, ROR4 = 0x124, ROR2 = 0x122, ROR1 = 0x121, XOR2 = 0x4E, XOR1 = 0xB1;
template <int CTRL> __device__ __forceinline__ unsigned dppu(unsigned v) { return __builtin_amdgcn_mov_dpp(v, CTRL, 0xf, 0xf, true); }
__device__ __forceinline__ float sw32_add(float a, float b) {
  const auto r = __builtin_amdgcn_permlane32_swap(asu(a), asu(b), false, false);
  return asf(r[0]) + asf(r[1]);
}
__device__ __forceinline__ float sw16_add(float a, float b) {
  const auto r = __builtin_amdgcn_permlane16_swap(asu(a), asu(b), false, false);
  return asf(r[0]) + asf(r[1]);
}
__device__ __forceinline__ float xrow_max(float v) {
  const auto r = __builtin_amdgcn_permlane16_swap(asu(v), asu(v), false, false);
  v = fmaxf(asf(r[0]), asf(r[1]));
  const auto s = __builtin_amdgcn_permlane32_swap(asu(v), asu(v), false, false);
  return fmaxf(asf(s[0]), asf(s[1]));
}
__device__ __forceinline__ unsigned pack_halves(float v) {
  const auto r = __builtin_amdgcn_permlane32_swap(asu(v), asu(v), false, false);
  return __builtin_amdgcn_perm(r[1], r[0], 0x07060302u);
}
__device__ __forceinline__ float row_sum(float v) { v += dpp<ROR8>(v); v += dpp<ROR4>(v); v += dpp<ROR2>(v); v += dpp<ROR1>(v); return v; }
__device__ __forceinline__ float row_max(float v) { v = fmaxf(v, dpp<ROR8>(v)); v = fmaxf(v, dpp<ROR4>(v)); v = fmaxf(v, dpp<ROR2>(v)); return fmaxf(v, dpp<ROR1>(v)); }
__device__ __forceinline__ float wave_sum(float v) { v = row_sum(v); v = sw16_add(v, v); return sw32_add(v, v); }
__device__ __forceinline__ void rows_reduce8(const float (&a)[8], float& u0, float& u1) {
  const float r0 = sw32_add(a[0], a[4]), r1 = sw32_add(a[1], a[5]), r2 = sw32_add(a[2], a[6]), r3 = sw32_add(a[3], a[7]);
  u0 = sw16_add(r0, r2); u1 = sw16_add(r1, r3);
}
__device__ __forceinline__ void fma8(float (&a)[8], float h, v4u w) {
#pragma unroll
  for (int e = 0; e < 4; ++e) { a[2 * e] = fmaf(h, lo16(w[e]), a[2 * e]); a[2 * e + 1] = fmaf(h, hi16(w[e]), a[2 * e + 1]); }
}
__device__ __forceinline__ void kpair(v4u& a, v4u& b) {
  const v4u r0 = a, r1 = b;
  a = v4u{__builtin_amdgcn_perm(r1[0], r0[0], 0x05040100u), __builtin_amdgcn_perm(r1[0], r0[0], 0x07060302u),
          __builtin_amdgcn_perm(r1[1], r0[1], 0x05040100u), __builtin_amdgcn_perm(r1[1], r0[1], 0x07060302u)};
  b = v4u{__builtin_amdgcn_perm(r1[2], r0[2], 0x05040100u), __builtin_amdgcn_perm(r1[2], r0[2], 0x07060302u),
          __builtin_amdgcn_perm(r1[3], r0[3], 0x05040100u), __builtin_amdgcn_perm(r1[3], r0[3], 0x07060302u)};
}
__device__ __forceinline__ void dot8(float (&acc)[8], unsigned hp, v4u a, v4u b) {
#pragma unroll
  for (int e = 0; e < 4; ++e) { acc[e] = dot2(a[e], hp, acc[e]); acc[4 + e] = dot2(b[e], hp, acc[4 + e]); }
}
__device__ __forceinline__ f32x4 mfma(v4u a, v4u b, f32x4 c) {
  return __builtin_amdgcn_mfma_f32_16x16x32_bf16(__builtin_bit_cast(bf16x8, a), __builtin_bit_cast(bf16x8, b), c, 0, 0, 0);
}
typedef __amdgpu_buffer_rsrc_t rsrc_t;
__device__ __forceinline__ rsrc_t mkrsrc(const void* p, unsigned bytes) { return __builtin_amdgcn_make_buffer_rsrc((void*)p, 0, bytes, 0x00020000); }
constexpr int SC1 = 16, NT = 2;
template <int AUX> __device__ __forceinline__ v4u ld128(rsrc_t r, unsigned off) { return __builtin_amdgcn_raw_buffer_load_b128(r, off, 0, AUX); }
template <int AUX> __device__ __forceinline__ v2u ld64(rsrc_t r, unsigned off) { return __builtin_amdgcn_raw_buffer_load_b64(r, off, 0, AUX); }
template <int AUX> __device__ __forceinline__ unsigned ld32(rsrc_t r, unsigned off) { return __builtin_amdgcn_raw_buffer_load_b32(r, off, 0, AUX); }
__device__ __forceinline__ void st128(rsrc_t r, unsigned off, v4u v) { __builtin_amdgcn_raw_buffer_store_b128(v, r, off, 0, SC1); }
__device__ __forceinline__ void st64(rsrc_t r, unsigned off, v2u v) { __builtin_amdgcn_raw_buffer_store_b64(v, r, off, 0, SC1); }
__device__ __forceinline__ void st32(rsrc_t r, unsigned off, unsigned v) { __builtin_amdgcn_raw_buffer_store_b32(v, r, off, 0, SC1); }
__device__ __forceinline__ unsigned umax(unsigned a, unsigned b) { return a > b ? a : b; }
__device__ __forceinline__ unsigned mx4(unsigned m, v4u v) { return umax(umax(umax(m, v[0]), v[1]), umax(v[2], v[3])); }
__device__ __forceinline__ unsigned mx2(unsigned m, v2u v) { return umax(umax(m, v[0]), v[1]); }
__device__ __forceinline__ v4u noempty4(f32x4 v) { return v4u{noempty(asu(v[0])), noempty(asu(v[1])), noempty(asu(v[2])), noempty(asu(v[3]))}; }
struct Tag { unsigned lo, hi; };
template <int LB> __device__ __forceinline__ unsigned tg_hi(Tag t) { return LB ? __builtin_amdgcn_alignbit(t.hi, t.lo, LB) : t.lo; }
template <int LB> __device__ __forceinline__ unsigned gr_lo(float v, Tag t) { return ((asu(v) + ((1u << LB) >> 1)) & (~0u << LB)) | (t.lo & ((1u << LB) - 1u)); }
template <int LB> __device__ __forceinline__ v2u gr1(float a, Tag t) { return v2u{gr_lo<LB>(a, t), tg_hi<LB>(t)}; }
template <int LB> __device__ __forceinline__ v4u gr2(float a, float b, Tag t) { return v4u{gr_lo<LB>(a, t), tg_hi<LB>(t), gr_lo<LB>(b, t), tg_hi<LB>(t)}; }
template <int LB> __device__ __forceinline__ unsigned gr_bad(v4u g, Tag t) {
  return (((g[0] ^ t.lo) | (g[2] ^ t.lo)) & ((1u << LB) - 1u)) | (g[1] ^ tg_hi<LB>(t)) | (g[3] ^ tg_hi<LB>(t));
}
template <int LB> __device__ __forceinline__ float gr_val(unsigned lo) { return asf(lo & (~0u << LB)); }
template <int LB> __device__ __forceinline__ float gr_sum(v4u g) { const float s = sw32_add(gr_val<LB>(g[0]), gr_val<LB>(g[2])); return sw16_add(s, s); }
__device__ __forceinline__ Tag next_tag(v2u ep) { const u64 e = (((u64)ep[1] << 32) | ep[0]) + 1ull; return Tag{(unsigned)e, (unsigned)(e >> 32)}; }
__device__ __forceinline__ v4u sel4(bool t, v4u a, v4u b) { return v4u{t ? a[0] : b[0], 0u, t ? a[2] : b[2], 0u}; }
#define MEMFENCE() asm volatile("" ::: "memory")
#define LGKM0() asm volatile("s_waitcnt lgkmcnt(0)" ::: "memory")
#define VM0() asm volatile("s_waitcnt vmcnt(0)" ::: "memory")
#define PIN4(a) asm volatile("" : "+v"((a)[0]), "+v"((a)[1]), "+v"((a)[2]), "+v"((a)[3]))
template <int N> __device__ __forceinline__ void vmwait() { asm volatile("s_waitcnt vmcnt(%0)" ::"n"(N) : "memory"); }
__device__ __forceinline__ void wg_barrier() { LGKM0(); __builtin_amdgcn_s_barrier(); MEMFENCE(); }
template <typename T> __device__ __forceinline__ volatile LAS T* lds_at(unsigned off) { return (volatile LAS T*)(size_t)off; }
template <typename T> __device__ __forceinline__ LAS T* lds_pl(unsigned off) { return (LAS T*)(size_t)off; }
__device__ __forceinline__ void lds_wait_ge(unsigned addr, unsigned need, u64 t0) {
  for (unsigned sp = 0; *lds_at<unsigned>(addr) < need; ++sp) { __builtin_amdgcn_s_sleep(1); if ((sp & 63u) == 63u && rt() - t0 > TLIMIT) break; }
  MEMFENCE();
}

#define EXPIRED(it) (((it) & 63u) == 63u && rt() - t0 > TLIMIT)

#define DMA4(M, O) "s_mov_b32 m0, " M "\n\ts_nop 0\n\t" \
                   "buffer_load_dwordx4 %[v0], %[d], " O " offen nt lds\n\tbuffer_load_dwordx4 %[v1], %[d], " O " offen offset:1024 nt lds\n\t" \
                   "buffer_load_dwordx4 %[v2], %[d], " O " offen offset:2048 nt lds\n\tbuffer_load_dwordx4 %[v3], %[d], " O " offen offset:3072 nt lds\n\t"
__device__ __forceinline__ void dma_fill(v4i srd, unsigned o, unsigned sl, unsigned v0, unsigned v1, unsigned v2, unsigned v3) {
  asm volatile(DMA4("%[m0]", "%[o0]") DMA4("%[m1]", "%[o1]") DMA4("%[m2]", "%[o2]") DMA4("%[m3]", "%[o3]")
               ::[d] "s"(srd), [v0] "v"(v0), [v1] "v"(v1), [v2] "v"(v2), [v3] "v"(v3),
                 [m0] "s"(sl), [m1] "s"(sl + 4096), [m2] "s"(sl + 8192), [m3] "s"(sl + 12288),
                 [o0] "s"(o), [o1] "s"(o + 4096), [o2] "s"(o + 32768), [o3] "s"(o + 36864) : "memory");
}

__device__ __forceinline__ float silu_bf(float v) {
  const float t = -v * LOG2E, r = fmaf(-v, 1.9259629911e-8f, fmaf(-v, LOG2E, -t));
  const float e = __builtin_amdgcn_exp2f(t);
  return rbf(v / (1.f + (e < INFINITY ? fmaf(e * 0.6931471806f, r, e) : e)));
}
__device__ __forceinline__ void ln_round(float mean, float var, float& mb, float& sd) {
  mb = rbf(mean);
  sd = rbf(__builtin_amdgcn_sqrtf(rbf(rbf(var) + 1e-5f)));
}
__device__ __forceinline__ float mod_round(float n, float sc, float sh) { return rbf(rbf(rbf(n) * rbf(1.f + sc)) + sh); }

__global__ void __launch_bounds__(NTHR, 2) block_decode(Args a) {
  extern __shared__ __attribute__((aligned(16))) unsigned char smem[];
  const int tid = threadIdx.x, lane = tid & 63, wave = __builtin_amdgcn_readfirstlane(tid >> 6);
  const int b = blockIdx.x, x = b & 7, j = b >> 3;
  const u64 t0 = rt();
  const unsigned lb = (unsigned)(size_t)(LAS unsigned char*)smem;
  const rsrc_t ws = mkrsrc(a.ws, WS_END);
  const int kq = j >> 3, cb = x + 8 * (j & 7);
  const int gq = j >> 2;
  const bool mlp = (x & 1) == 0;
  const int og = (x & 6) + (j >> 4), oc0 = 32 * (j & 15);
  const bool has_gu = mlp && 4 * gq + (x >> 1) < 28;
  const int up = 4 * (4 * gq + (x >> 1)) + (j & 3);
  const int fc0 = 64 * x + 4 * j;

  if (tid < 20) *lds_pl<unsigned>(lb + L_CTL + 4 * tid) = (tid == 1 || tid == 3) ? (unsigned)INF : 0u;
  asm volatile("" :: "s"(a.K), "s"(a.ws));
  wg_barrier();
  int w_a = wave, w_b = wave;
  asm volatile("" : "+s"(w_a));
  if (w_a == 4) {
    const u64 kb = (u64)(a.K + (size_t)j * SK * DH), vb = (u64)(a.V + (size_t)j * SK * DH);
    const v4i ksrd = {(int)kb, (int)(kb >> 32), (int)HEAD_BYTES, 0x00020000}, vsrd = {(int)vb, (int)(vb >> 32), (int)HEAD_BYTES, 0x00020000};
    const unsigned g = lane >> 4, c = lane & 15;
    const unsigned k0 = g * 256 + ((c ^ g) << 4), k1 = g * 256 + ((c ^ (4 + g)) << 4), k2 = g * 256 + ((c ^ (8 + g)) << 4), k3 = g * 256 + ((c ^ (12 + g)) << 4);
    const unsigned vl = lane * 16;
    unsigned sl = lb + L_RING;
    auto foff = [](int n) { return ((unsigned)n >> 2) * 65536u + ((unsigned)n & 3u) * 8192u; };
    auto step = [&]() { sl = sl + SLOT == lb + L_RING + NSLOT * SLOT ? lb + L_RING : sl + SLOT; };
    volatile LAS u64* FT = lds_at<u64>(lb + L_FT);
    int pub = 0;
    unsigned total = (unsigned)INF;
    auto publish = [&](int n) { pub = n > pub ? n : pub; *FT = (u64)(unsigned)pub | ((u64)total << 32); };
    auto wait_free = [&](int i) {
      const int p = i - NSLOT;
      volatile LAS u64* dn = lds_at<u64>(lb + L_DONE + 8 * (p & 1));
      const unsigned need = (unsigned)(p >> 1) + 1u;
      auto freed = [&]() { const u64 d = *dn; return (unsigned)d >= need && (unsigned)(d >> 32) >= need; };
      if (!freed()) {
        VM0(); publish(i);
        for (unsigned sp = 0; !freed(); ++sp) { __builtin_amdgcn_s_sleep(1); if (EXPIRED(sp)) break; }
      }
      MEMFENCE();
    };
    for (unsigned sp = 0; *lds_at<unsigned>(lb + L_ISSUED) < 5u && sp < 4096u; ++sp) {}
    unsigned pause = 0, c0 = (unsigned)__builtin_amdgcn_s_memtime(), r0 = (unsigned)rt();
    auto beat = [&]() {
      vmwait<PACE_WAIT>();
      const unsigned t1 = (unsigned)__builtin_amdgcn_s_memtime();
      while ((unsigned)__builtin_amdgcn_s_memtime() - t1 < pause) {}
    };
    auto measure = [&]() {
      const unsigned c1 = (unsigned)__builtin_amdgcn_s_memtime(), r1 = (unsigned)rt();
      pause = __builtin_amdgcn_readfirstlane((unsigned)fminf((float)(c1 - c0) * PACE_TICKS * __builtin_amdgcn_rcpf((float)(r1 - r0)), 1024.f));
      c0 = c1; r0 = r1;
    };
    unsigned tn = 1;
    auto take = [&]() {
      u64 tv;
      for (unsigned sp = 0; (unsigned)((tv = *lds_at<u64>(lb + L_TK)) >> 32) < tn; ++sp) if (EXPIRED(sp)) { tv = EMPTY; break; }
      ++tn;
      return (unsigned)__builtin_amdgcn_readfirstlane((unsigned)tv);
    };
#pragma unroll
    for (int i = 0; i < BURST; ++i) { dma_fill(ksrd, foff(POOL0 + x * NP + i), sl, k0, k1, k2, k3); step(); }
    int i = BURST, nt = 0, next = POOL0 + x * NP + BURST, left = NP - BURST;
    bool asked = true;
    for (;; ++i) {
      if (i < PACE_FROM) vmwait<16>();
      beat();
      dma_fill(ksrd, foff(next), sl, k0, k1, k2, k3);
      if ((i & 15) == 0) measure();
      step();
      ++next; --left;
      vmwait<32>();
      publish(i - 1);
      if (i == NP + SO - 5) *lds_at<unsigned>(lb + L_TKC) = tn;
      if (left == 0) {
        if (i == NP - 1) { next = x * SO; left = SO; }
        else {
          const unsigned t = asked ? take() : EMPTY;
          if (t >= (unsigned)NKT) break;
          left = 4; next = KD0 + 4 * (int)t;
          *lds_at<unsigned>(lb + L_LIST + 4 * nt) = t; ++nt;
          asked = i + 1 - NP + left + 4 <= CAP;
          if (asked) *lds_at<unsigned>(lb + L_TKC) = tn;
        }
      }
      if (i + 1 >= NSLOT) wait_free(i + 1);
    }
    const int nk = i + 1, vown = 2 * nk - NP;
    *lds_at<unsigned>(lb + L_NK) = (unsigned)nk;
    wait_free(i + 1);
    next = x * SO; left = SO;
    int rt_ = 0;
    for (++i;; ++i) {
      if (i >= vown - 8) { pause = 0; vmwait<16>(); }
      beat();
      dma_fill(vsrd, foff(next), sl, vl, vl, vl, vl);
      if ((i & 15) == 0) measure();
      step();
      ++next; --left;
      vmwait<32>();
      publish(i - 1);
      if (i == vown - 3) *lds_at<unsigned>(lb + L_TKC) = tn;
      if (left == 0) {
        if (rt_ < nt) { const unsigned t = __builtin_amdgcn_readfirstlane(*lds_at<unsigned>(lb + L_LIST + 4 * rt_)); ++rt_; left = 4; next = KD0 + 4 * (int)t; }
        else {
          const unsigned t = take();
          if (t >= (unsigned)NPT) break;
          *lds_at<unsigned>(lb + L_TKC) = tn;
          left = 2; next = POOL0 + 2 * (int)t;
        }
      }
      wait_free(i + 1);
    }
    total = (unsigned)i + 1u;
    publish(i - 1);
    vmwait<16>(); publish(i);
    VM0(); publish(i + 1);
    return;
  }

  asm volatile("" : "+s"(w_b));
  if (w_b == 5) {
    __builtin_amdgcn_s_setprio(3);
    const rsrc_t rwm = mkrsrc(a.wm, DM * 3072 * 2), rc = mkrsrc(a.c, DM * 2);
    const int n0 = 512 * (gq & 1) + 64 * x + 16 * (j & 3);
    const int ar = 128 * kq + 4 * (lane >> 1);
    const v2u c4 = ld64<0>(rc, ar * 2);
    MEMFENCE();
    v4u w1[4];
#pragma unroll
    for (int i = 0; i < 4; ++i) w1[i] = ld128<NT>(rwm, (unsigned)((ar + i) * 3072 + n0 + 8 * (lane & 1)) * 2);
    const v2u ep = ld64<SC1>(ws, WS_EPOCH);
    const int mr = 64 * gq + 8 * (lane >> 3), mc = 64 * (x + 8 * (j & 3));
    const v4u cv = ld128<0>(rc, mr * 2);
    MEMFENCE();
    if (lane == 0) __hip_atomic_fetch_add(lds_pl<unsigned>(lb + L_ISSUED), 1u, __ATOMIC_RELAXED, __HIP_MEMORY_SCOPE_WORKGROUP);
    st128(ws, (lane < 8 && !mlp) ? WS_YP + (og * 512 + oc0) * 4 + lane * 16 : OOB, v4u{EMPTY, EMPTY, EMPTY, EMPTY});
    st128(ws, has_gu ? WS_DP + up * 2048 + lane * 16 : OOB, v4u{EMPTY, EMPTY, EMPTY, EMPTY});
    st128(ws, has_gu ? WS_DP + up * 2048 + 1024 + lane * 16 : OOB, v4u{EMPTY, EMPTY, EMPTY, EMPTY});
    st128(ws, lane < 33 ? WS_PO + b * REC + lane * 16 : OOB, v4u{EMPTY, EMPTY, EMPTY, EMPTY});
    st32(ws, (lane < 2 && x == 0) ? WS_CTR + j * 256 + lane * 128 : OOB, 0u);
    const Tag tag = next_tag(ep);
    {
      float s8[8] = {0.f, 0.f, 0.f, 0.f, 0.f, 0.f, 0.f, 0.f};
      fma8(s8, silu_bf(lo16(c4[0])), w1[0]); fma8(s8, silu_bf(hi16(c4[0])), w1[1]);
      fma8(s8, silu_bf(lo16(c4[1])), w1[2]); fma8(s8, silu_bf(hi16(c4[1])), w1[3]);
      float u0, u1;
      rows_reduce8(s8, u0, u1);
      u0 += dpp<ROR8>(u0); u1 += dpp<ROR8>(u1); u0 += dpp<ROR4>(u0); u1 += dpp<ROR4>(u1); u0 += dpp<XOR2>(u0); u1 += dpp<XOR2>(u1);
      const int col = n0 + 8 * (lane & 1) + 2 * (lane >> 4);
      st128(ws, (lane & 15) < 2 ? WS_P1 + (kq * 1024 + col) * 8 : OOB, gr2<0>(u0, u1, tag));
      *lds_at<unsigned>(lb + L_AUXPUB) = 1u;
    }
    __builtin_amdgcn_s_setprio(0);
    auto stream_at = [&](unsigned fillno) {
      for (unsigned sp = 0; *lds_at<unsigned>(lb + L_FT) < fillno; ++sp) { __builtin_amdgcn_s_sleep(8); if ((sp & 63u) == 63u && rt() - t0 > TLIMIT) break; }
      MEMFENCE();
    };
    stream_at(NSLOT + 4);
    {
      v4u w2[8];
#pragma unroll
      for (int i = 0; i < 8; ++i) w2[i] = ld128<NT>(rwm, (unsigned)((mr + i) * 3072 + 1024 + mc + 8 * (lane & 7)) * 2);
      float s8[8] = {0.f, 0.f, 0.f, 0.f, 0.f, 0.f, 0.f, 0.f};
#pragma unroll
      for (int e = 0; e < 4; ++e) { fma8(s8, silu_bf(lo16(cv[e])), w2[2 * e]); fma8(s8, silu_bf(hi16(cv[e])), w2[2 * e + 1]); }
      float u0, u1;
      rows_reduce8(s8, u0, u1);
      u0 += dpp<ROR8>(u0); u1 += dpp<ROR8>(u1);
      st128(ws, (lane & 8) == 0 ? WS_M2P + (gq * 2048 + mc + 8 * (lane & 7) + 2 * (lane >> 4)) * 8 : OOB, gr2<4>(u0, u1, tag));
    }
    stream_at(NSLOT + 16);
    {
      v4u m;
      for (;;) {
        MEMFENCE();
        m = ld128<SC1>(ws, WS_M2P + (((lane >> 2) & 7) * 2048 + 8 * b + 2 * (lane & 3)) * 8);
        if (__all(gr_bad<4>(m, tag) == 0u) || rt() - t0 > TLIMIT) break;
        __builtin_amdgcn_s_sleep(32);
      }
      float u0 = gr_val<4>(m[0]), u1 = gr_val<4>(m[2]);
      u0 = sw16_add(u0, u0); u1 = sw16_add(u1, u1);
      u0 += dpp<ROR8>(u0); u1 += dpp<ROR8>(u1); u0 += dpp<ROR4>(u0); u1 += dpp<ROR4>(u1);
      VM0();
      st128(ws, lane < 4 ? WS_M2 + b * 64 + lane * 16 : OOB, gr2<4>(rbf(u0), rbf(u1), tag));
    }
    stream_at(NSLOT + 28);
#pragma unroll 1
    for (int u = 0; u < 16; ++u) {
      v4u m;
      for (;;) {
        MEMFENCE();
        m = ld128<SC1>(ws, WS_M2 + u * 1024 + lane * 16);
        if (__all(gr_bad<4>(m, tag) == 0u) || rt() - t0 > TLIMIT) break;
        __builtin_amdgcn_s_sleep(32);
      }
      *lds_pl<unsigned>(lb + L_M2 + u * 256 + lane * 4) = __builtin_amdgcn_perm(m[2], m[0], 0x07060302u);
      __builtin_amdgcn_s_sleep(16);
    }
    LGKM0();
    *lds_at<unsigned>(lb + L_M2RDY) = 1u;
    GAS unsigned* ctr = (GAS unsigned*)(a.ws + WS_CTR + j * 256);
    unsigned n = 1;
    auto draw = [&](int which) {
      unsigned v = 0;
      if (lane == 0) v = __hip_atomic_fetch_add(ctr + 32 * which, 1u, __ATOMIC_RELAXED, __HIP_MEMORY_SCOPE_AGENT);
      v = __builtin_amdgcn_readfirstlane(v);
      *lds_at<u64>(lb + L_TK) = ((u64)n << 32) | v;
      ++n;
      return v;
    };
    for (;;) {
      bool over = false;
      for (unsigned sp = 0; *lds_at<unsigned>(lb + L_TKC) < n; ++sp) {
        if (*lds_at<unsigned>(lb + L_NK) != (unsigned)INF || EXPIRED(sp)) { over = true; break; }
        __builtin_amdgcn_s_sleep(2);
      }
      if (over || *lds_at<unsigned>(lb + L_NK) != (unsigned)INF || draw(0) >= (unsigned)NKT) break;
    }
    lds_wait_ge(lb + L_KCNT, 5u, t0);
    __builtin_amdgcn_s_sleep(127);
    {
      v2u mx;
      for (;;) {
        MEMFENCE();
        mx = ld64<SC1>(ws, WS_MX + j * 64 + (lane & 7) * 8);
        if (__all(gr_bad<4>(v4u{mx[0], mx[1], mx[0], mx[1]}, tag) == 0u) || rt() - t0 > TLIMIT) break;
        __builtin_amdgcn_s_sleep(32);
      }
      *lds_at<float>(lb + L_MH) = row_max(gr_val<4>(mx[0]));
      LGKM0();
    }
    {
      unsigned dq = 0, pend = 0;
      v4u sc = {0u, 0u, 0u, 0u};
      auto flush = [&]() {
        if ((unsigned)lane < 8u * pend) *lds_pl<v4u>(lb + L_SC + dq * 128 + lane * 16) = sc;
        dq += pend; pend = 0;
        LGKM0();
        *lds_at<unsigned>(lb + L_SCR) = dq;
      };
      for (;;) {
        if (__builtin_amdgcn_readfirstlane(*lds_at<unsigned>(lb + L_TKC)) < n) flush();
        for (unsigned sp = 0; *lds_at<unsigned>(lb + L_TKC) < n; ++sp) { __builtin_amdgcn_s_sleep(2); if (EXPIRED(sp)) break; }
        const unsigned v = draw(1);
        flush();
        if (v >= (unsigned)NPT || rt() - t0 > TLIMIT) break;
        pend = 2u;
        sc = ld128<SC1 | NT>(ws, lane < 16 ? WS_SG + (j * 8 * NP + 2 * v) * 128 + lane * 16 : OOB);
        MEMFENCE();
      }
    }
    return;
  }

  const int g = lane >> 4, c = lane & 15;
  const rsrc_t rx = mkrsrc(a.x, DM * 2), rwq = mkrsrc(a.wq, DM * 4096 * 2);
  const rsrc_t rwd = mkrsrc(a.wd, DF * DM * 2);
  const int r0 = 128 * kq + 32 * wave;
  v4u wqv[4];
#pragma unroll
  for (int i = 0; i < 4; ++i) wqv[i] = ld128<NT>(rwq, (unsigned)((r0 + 4 * (lane >> 3) + i) * 4096 + 64 * cb + 8 * (lane & 7)) * 2);
  const v2u ep = ld64<SC1>(ws, WS_EPOCH);
  const v4u x8 = ld128<0>(rx, lane * 16);
  const unsigned x2 = ld32<0>(rx, tid * 4);
  const unsigned xh = ld32<0>(rx, (r0 + 2 * c) * 2);
  MEMFENCE();
  if (lane == 0) __hip_atomic_fetch_add(lds_pl<unsigned>(lb + L_ISSUED), 1u, __ATOMIC_RELAXED, __HIP_MEMORY_SCOPE_WORKGROUP);
  float mb, sd;
  {
    float xs[8];
#pragma unroll
    for (int e = 0; e < 4; ++e) { xs[2 * e] = lo16(x8[e]); xs[2 * e + 1] = hi16(x8[e]); }
    float s = 0.f;
#pragma unroll
    for (int e = 0; e < 8; ++e) s += xs[e];
    const float mean = wave_sum(s) * (1.f / 512.f);
    float v = 0.f;
#pragma unroll
    for (int e = 0; e < 8; ++e) { const float d = xs[e] - mean; v = fmaf(d, d, v); }
    ln_round(mean, wave_sum(v) * (1.f / 512.f), mb, sd);
  }
  float nrow = rbf((lane >= 32 ? hi16(xh) : lo16(xh)) - mb) / sd;
  kpair(wqv[0], wqv[1]); kpair(wqv[2], wqv[3]);
  PIN4(wqv); asm volatile("" : "+v"(nrow));
  const Tag tag = next_tag(ep);
  lds_wait_ge(lb + L_AUXPUB, 1u, t0);
  {
    const unsigned o1 = WS_P1 + (g * 1024 + r0 + 2 * c) * 8;
    v4u gs = {0u, 0u, 0u, 0u}, gc = gs;
    bool bad = true;
    __builtin_amdgcn_s_sleep(32);
    for (;;) {
      MEMFENCE();
      const v4u ns_ = ld128<SC1>(ws, bad ? o1 : OOB), nc_ = ld128<SC1>(ws, bad ? o1 + 4096 : OOB);
      const bool tk = bad & ((gr_bad<0>(ns_, tag) | gr_bad<0>(nc_, tag)) == 0u);
      gs = sel4(tk, ns_, gs); gc = sel4(tk, nc_, gc); bad = bad & !tk;
      if (!__any(bad) || rt() - t0 > TLIMIT) break;
    }
    const unsigned hp = pack_halves(mod_round(nrow, rbf(gr_sum<0>(gc)), rbf(gr_sum<0>(gs))));
    float acc[8] = {0.f, 0.f, 0.f, 0.f, 0.f, 0.f, 0.f, 0.f};
    dot8(acc, __builtin_amdgcn_ds_bpermute((lane >> 3) * 8, hp), wqv[0], wqv[1]);
    dot8(acc, __builtin_amdgcn_ds_bpermute((lane >> 3) * 8 + 4, hp), wqv[2], wqv[3]);
    float u0, u1;
    rows_reduce8(acc, u0, u1);
    u0 += dpp<ROR8>(u0); u1 += dpp<ROR8>(u1);
    if ((lane & 8) == 0) *lds_pl<f32x2>(lb + L_RED + wave * 256 + (8 * (lane & 7) + 2 * g) * 4) = f32x2{u0, u1};
    LGKM0();
    unsigned old = 0;
    if (lane == 0) old = __hip_atomic_fetch_add(lds_pl<unsigned>(lb + L_QCNT), 1u, __ATOMIC_RELAXED, __HIP_MEMORY_SCOPE_WORKGROUP);
    old = __builtin_amdgcn_readfirstlane(old);
    MEMFENCE();
    if (old == 3u) {
      const unsigned ra = lb + L_RED + lane * 4;
      const float qp = (*lds_pl<float>(ra) + *lds_pl<float>(ra + 256)) + (*lds_pl<float>(ra + 512) + *lds_pl<float>(ra + 768));
      st64(ws, WS_QS + (kq * 4096 + 64 * cb + lane) * 8, gr1<2>(qp, tag));
    }
  }
  const int r8 = 8 * (tid >> 2);
  v4u wv[8];
  unsigned wdv[16];
#pragma unroll
  for (int i = 0; i < 8; ++i) { wv[i] = v4u{0u, 0u, 0u, 0u}; wdv[2 * i] = 0u; wdv[2 * i + 1] = 0u; }
  const GAS v4u* wp = (const GAS v4u*)(mlp ? ((tid & 2) ? a.wu : a.wg) + (r8 * DF + 16 * (has_gu ? up : 0) + 8 * (tid & 1)) : a.wo + ((512 * og + r8) * DM + oc0 + 8 * (tid & 3)));
  const int wstride = mlp ? DF / 8 : DM / 8;
  const unsigned wdo = has_gu ? (unsigned)(16 * up * DM + 2 * tid) * 2 : OOB;

  const unsigned key = 4 * (c & 3) + (c >> 2);
  unsigned ka[4], msk[4];
#pragma unroll
  for (int st = 0; st < 4; ++st) {
    ka[st] = lb + L_RING + (wave & 1) * 8192 + key * 256 + (((st + 4 * g) ^ key) << 4);
    msk[st] = ((c & 7) >> 1) == st ? ((c & 1) ? 0xffff0000u : 0x0000ffffu) : 0u;
  }
  const unsigned va = lb + L_RING + (wave & 1) * 8192 + lane * 16;
  const bool is_hi = c < 8;
  volatile LAS unsigned* DN = lds_at<unsigned>(lb + L_DONE + 4 * wave);
  unsigned so = (wave >> 1) * SLOT;
  v4u sv[8], sw[8], ud[8], qf[4];
#pragma unroll
  for (int i = 0; i < 8; ++i) { sv[i] = v4u{0u, 0u, 0u, 0u}; sw[i] = sv[i]; ud[i] = sv[i]; }
  int ns = 0;
  auto release = [&](v4u (&d)[8], int i) {
    LGKM0();
    *DN = (unsigned)(i >> 1) + 1u;
    asm volatile("" : "+v"(d[0]), "+v"(d[1]), "+v"(d[2]), "+v"(d[3]), "+v"(d[4]), "+v"(d[5]), "+v"(d[6]), "+v"(d[7]) :: "memory");
    so = (so + 2 * SLOT) & (NSLOT * SLOT - 1);
  };
  auto read_k = [&](v4u (&d)[8], int i) {
#pragma unroll
    for (int st = 0; st < 4; ++st) { d[st] = *lds_pl<v4u>(ka[st] + so); d[4 + st] = *lds_pl<v4u>(ka[st] + so + 4096); }
    release(d, i);
  };
  int total = INF, nk = INF;
  auto wait_full = [&](int i, bool in_k) {
    for (unsigned sp = 0;; ++sp) {
      const v4u ct = *(volatile LAS v4u*)(size_t)(lb + L_FT);
      total = __builtin_amdgcn_readfirstlane((int)ct[1]);
      nk = __builtin_amdgcn_readfirstlane((int)ct[3]);
      if (EXPIRED(sp)) { total = 0; nk = nk < i ? nk : i; break; }
      if (__builtin_amdgcn_readfirstlane((int)ct[0]) > i || (in_k && nk <= i)) break;
      if (total == INF) __builtin_amdgcn_s_sleep(1);
    }
    MEMFENCE();
  };
  float mraw = -INFINITY;
  auto scores = [&](const v4u (&d)[8], int i) {
    f32x4 sa = {0.f, 0.f, 0.f, 0.f}, sb = {0.f, 0.f, 0.f, 0.f};
#pragma unroll
    for (int st = 0; st < 4; ++st) { sa = mfma(d[st], qf[st], sa); sb = mfma(d[4 + st], qf[st], sb); }
    mraw = fmaxf(fmaxf(fmaxf(mraw, sa[0]), fmaxf(sa[1], sa[2])), fmaxf(fmaxf(sa[3], sb[0]), fmaxf(fmaxf(sb[1], sb[2]), sb[3])));
    v4u pk = {cvtpk(sa[0], sa[1]), cvtpk(sa[2], sa[3]), cvtpk(sb[0], sb[1]), cvtpk(sb[2], sb[3])};
#pragma unroll
    for (int u = 0; u < 4; ++u) pk[u] = cvtpk(lo16(pk[u]) * SCALE, hi16(pk[u]) * SCALE);
    const unsigned so_ = (wave & 1) * 64 + g * 16;
    const unsigned dst = i < NP ? L_SH + i * 128 : L_SC + (i - NP) * 128;
    if (c == 0) *lds_pl<v4u>(lb + dst + so_) = pk;
  };
  auto stash = [&]() {
    const int full = __builtin_amdgcn_readfirstlane((int)*lds_at<unsigned>(lb + L_FT)), i0 = wave >> 1;
    MEMFENCE();
    if (ns == 0 && full > i0) { read_k(wv, i0); ns = 1; }
    if (ns == 1 && full > i0 + 2) { read_k(sv, i0 + 2); ns = 2; }
    if (ns == 2 && full > i0 + 4) { read_k(sw, i0 + 4); ns = 3; }
    if (ns == 3 && full > i0 + 6) { read_k(ud, i0 + 6); ns = 4; }
  };
  {
    const unsigned o2 = WS_QS + (g * 4096 + 128 * j + 32 * wave + 2 * c) * 8;
    v4u gq4 = {0u, 0u, 0u, 0u};
    bool bad = true;
    stash();
    {
      const v4u nq = ld128<SC1>(ws, o2);
      bad = gr_bad<2>(nq, tag) != 0u;
      gq4 = nq;
    }
    asm volatile("" : "+v"(gq4) :: "memory");
    stash();
    for (unsigned it = 0; __any(bad) && !EXPIRED(it); ++it) {
      MEMFENCE();
      const v4u nq = ld128<SC1>(ws, bad ? o2 : OOB);
      const bool tk = bad & (gr_bad<2>(nq, tag) == 0u);
      gq4 = sel4(tk, nq, gq4); bad = bad & !tk;
    }
    *lds_pl<unsigned>(lb + L_QSC + (16 * wave + c) * 4) = pack_halves(rbf(gr_sum<2>(gq4)));
    LGKM0();
    if (lane == 0) __hip_atomic_fetch_add(lds_pl<unsigned>(lb + L_QIN), 1u, __ATOMIC_RELAXED, __HIP_MEMORY_SCOPE_WORKGROUP);
    lds_wait_ge(lb + L_QIN, 4u, t0);
#pragma unroll
    for (int st = 0; st < 4; ++st) qf[st] = *lds_pl<v4u>(lb + L_QSC + (st + 4 * g) * 16);
  }
  if (ns >= 1) scores(wv, wave >> 1);
  if (ns >= 2) scores(sv, (wave >> 1) + 2);
  if (ns >= 3) scores(sw, (wave >> 1) + 4);
  if (ns >= 4) scores(ud, (wave >> 1) + 6);
  int i = (wave >> 1) + 2 * ns;
  for (;; i += 2) {
    wait_full(i, true);
    if (nk <= i) break;
    read_k(ud, i);
    scores(ud, i);
  }
  float m2;
  {
    *lds_pl<float>(lb + L_MXW + wave * 4) = rbf(rbf(xrow_max(mraw)) * SCALE);
    LGKM0();
    unsigned old = 0;
    if (lane == 0) old = __hip_atomic_fetch_add(lds_pl<unsigned>(lb + L_KCNT), 1u, __ATOMIC_RELAXED, __HIP_MEMORY_SCOPE_WORKGROUP);
    old = __builtin_amdgcn_readfirstlane(old);
    lds_wait_ge(lb + L_KCNT, 4u, t0);
    const f32x4 mw = *lds_pl<f32x4>(lb + L_MXW);
    const float mg = fmaxf(fmaxf(mw[0], mw[1]), fmaxf(mw[2], mw[3]));
    if (old == 3u) {
#pragma unroll
      for (int u = 0; u < (NP + 7) / 8; ++u)
        __builtin_amdgcn_raw_buffer_store_b128(*lds_pl<v4u>(lb + L_SH + u * 1024 + lane * 16), ws,
                                               u * 1024 + lane * 16 < NP * 128 ? WS_SG + b * NP * 128 + u * 1024 + lane * 16 : OOB, 0, SC1 | NT);
      VM0();
      st64(ws, lane == 0 ? WS_MX + b * 8 : OOB, gr1<4>(mg, tag));
      *lds_at<unsigned>(lb + L_KCNT) = 5u;
    }
    m2 = mg * LOG2E;
  }
  float lsum = 0.f;
  f32x4 acc = {0.f, 0.f, 0.f, 0.f};
  {
    int wk = (mlp && !has_gu) ? 11 : 0;
    bool done = false, own = true;
    for (;; i += 2) {
      int e = i - nk;
      if (!done && e >= nk - NP) {
        e -= nk - NP;
        for (unsigned sp = 0;; ++sp) {
          const v4u ct = *(volatile LAS v4u*)(size_t)(lb + L_FT);
          if (__builtin_amdgcn_readfirstlane((int)ct[2]) > e) break;
          if (__builtin_amdgcn_readfirstlane((int)ct[1]) <= i || EXPIRED(sp)) { done = true; break; }
          __builtin_amdgcn_s_sleep(1);
        }
        MEMFENCE();
        if (own) {
          const float mh = *lds_at<float>(lb + L_MH) * LOG2E, al = __builtin_amdgcn_exp2f(m2 - mh);
          acc *= al; lsum *= al; m2 = mh; own = false;
        }
      }
      if (!done) {
        const v4u sc = *lds_pl<v4u>(lb + L_SC + e * 128 + (wave & 1) * 64 + g * 16);
        v4u pa[8];
#pragma unroll
        for (int u = 0; u < 8; ++u) {
          const float p = __builtin_amdgcn_exp2f(fmaf((u & 1) ? hi16(sc[u >> 1]) : lo16(sc[u >> 1]), LOG2E, -m2));
          lsum += p;
          const float ph = hi16(asu(p));
          const float xv = is_hi ? ph : p - ph;
          const unsigned pk = cvtpk(xv, xv);
          pa[u] = v4u{pk & msk[0], pk & msk[1], pk & msk[2], pk & msk[3]};
        }
        asm volatile("" : "+v"(pa[0]), "+v"(pa[1]), "+v"(pa[2]), "+v"(pa[3]), "+v"(pa[4]), "+v"(pa[5]), "+v"(pa[6]), "+v"(pa[7]));
        wait_full(i, false);
#pragma unroll
        for (int u = 0; u < 8; ++u) ud[u] = *lds_pl<v4u>(va + so + u * 1024);
        release(ud, i);
#pragma unroll
        for (int u = 0; u < 8; ++u) acc = mfma(pa[u], ud[u], acc);
        done = i + 2 >= total && !own;
      }
      MEMFENCE();
      if (wk <= 10 && (done || rt() - t0 >= (u64)(TW + TD * wk + (wk == 10 ? 400 : 0)))) {
#pragma unroll
        for (int u = 0; u < 8; ++u) if (wk == u) wv[u] = __builtin_nontemporal_load(wp + u * wstride);
#pragma unroll
        for (int v = 0; v < 2; ++v) if (wk == 8 + v) {
#pragma unroll
          for (int u = 8 * v; u < 8 * v + 8; ++u) wdv[u] = ld32<NT>(rwd, wdo + u * (DM * 2));
        }
        if (wk == 10) {
#pragma unroll
          for (int u = 0; u < 8; u += 2) kpair(wv[u], wv[u + 1]);
#pragma unroll
          for (int u = 0; u < 16; u += 2) {
            const unsigned lo = __builtin_amdgcn_perm(wdv[u + 1], wdv[u], 0x05040100u), hi = __builtin_amdgcn_perm(wdv[u + 1], wdv[u], 0x07060302u);
            wdv[u] = lo; wdv[u + 1] = hi;
          }
        }
        ++wk;
      }
      MEMFENCE();
      if (done && wk > 10) break;
    }
  }
  {
    f32x4 o;
#pragma unroll
    for (int r = 0; r < 4; ++r) o[r] = sw32_add(acc[r], acc[r]);
    float l = sw16_add(lsum, lsum); l = sw32_add(l, l);
    *lds_pl<f32x4>(lb + L_MRG + wave * 512 + (8 * c + 4 * (g & 1)) * 4) = o;
    *lds_pl<float>(lb + L_LW + wave * 4) = l;
    wg_barrier();
    if (wave == 0) {
      const unsigned ra = lb + L_MRG + (lane & 31) * 16;
      const f32x4 lw = *lds_pl<f32x4>(lb + L_LW);
      f32x4 O = (*lds_pl<f32x4>(ra) + *lds_pl<f32x4>(ra + 512)) + (*lds_pl<f32x4>(ra + 1024) + *lds_pl<f32x4>(ra + 1536));
      if (lane >= 32) O = f32x4{(lw[0] + lw[1]) + (lw[2] + lw[3]), 0.f, 0.f, 0.f};
      st128(ws, lane < 33 ? WS_PO + b * REC + lane * 16 : OOB, noempty4(O));
    }
  }
  if (!mlp) {
    const unsigned hb = WS_PO + (4 * og + wave) * 8 * REC;
    v2u po[8];
    unsigned ls = EMPTY;
#pragma unroll
    for (int s = 0; s < 8; ++s) po[s] = v2u{0u, 0u};
    unsigned miss = 0x1FFu;
    for (unsigned it = 0;; ++it) {
      MEMFENCE();
      if (miss & 0x100u) ls = ld32<SC1>(ws, (lane & 15) < 8 ? hb + (lane & 15) * REC + 512 : OOB);
#pragma unroll
      for (int s = 0; s < 8; ++s) if (miss & (1u << s)) po[s] = ld64<SC1>(ws, hb + s * REC + lane * 8);
      if ((miss & 0x100u) && __all(ls != EMPTY)) miss &= ~0x100u;
#pragma unroll
      for (int s = 0; s < 8; ++s) if ((miss & (1u << s)) && __all(mx2(0u, po[s]) != EMPTY)) miss &= ~(1u << s);
      if (miss == 0u || EXPIRED(it)) break;
    }
    const float inv = __builtin_amdgcn_rcpf(row_sum(asf(ls)));
    const unsigned op = cvtpk((((asf(po[0][0]) + asf(po[1][0])) + (asf(po[2][0]) + asf(po[3][0]))) + ((asf(po[4][0]) + asf(po[5][0])) + (asf(po[6][0]) + asf(po[7][0])))) * inv,
                              (((asf(po[0][1]) + asf(po[1][1])) + (asf(po[2][1]) + asf(po[3][1]))) + ((asf(po[4][1]) + asf(po[5][1])) + (asf(po[6][1]) + asf(po[7][1])))) * inv);
    float y[8] = {0.f, 0.f, 0.f, 0.f, 0.f, 0.f, 0.f, 0.f};
    dot8(y, dppu<0x00>(op), wv[0], wv[1]); dot8(y, dppu<0x55>(op), wv[2], wv[3]);
    dot8(y, dppu<0xAA>(op), wv[4], wv[5]); dot8(y, dppu<0xFF>(op), wv[6], wv[7]);
    float u0, u1;
    rows_reduce8(y, u0, u1);
    u0 += dpp<ROR8>(u0); u1 += dpp<ROR8>(u1); u0 += dpp<ROR4>(u0); u1 += dpp<ROR4>(u1);
    *lds_pl<f32x2>(lb + L_RED3 + wave * 128 + (8 * (c & 3) + 2 * g) * 4) = f32x2{u0, u1};
    wg_barrier();
    if (wave == 0) {
      const unsigned ra = lb + L_RED3 + (lane & 7) * 16;
      const f32x4 t = (*lds_pl<f32x4>(ra) + *lds_pl<f32x4>(ra + 128)) + (*lds_pl<f32x4>(ra + 256) + *lds_pl<f32x4>(ra + 384));
      st128(ws, lane < 8 ? WS_YP + (og * 512 + oc0) * 4 + lane * 16 : OOB, noempty4(t));
    }
    return;
  }
  lds_wait_ge(lb + L_M2RDY, 1u, t0);
  float gbf = lo16(*lds_pl<u16>(lb + L_M2 + 3072 + (fc0 + g) * 2));
  {
    const unsigned gaw = *lds_pl<unsigned>(lb + L_M2 + tid * 4), sbw = *lds_pl<unsigned>(lb + L_M2 + 1024 + tid * 4), cbw = *lds_pl<unsigned>(lb + L_M2 + 2048 + tid * 4);
    float ga0 = lo16(gaw), ga1 = hi16(gaw), sb0 = lo16(sbw), sb1 = hi16(sbw), t0c = rbf(1.f + lo16(cbw)), t1c = rbf(1.f + hi16(cbw)), xa = lo16(x2), xb = hi16(x2);
    asm volatile("" : "+v"(ga0), "+v"(ga1), "+v"(sb0), "+v"(sb1), "+v"(t0c), "+v"(t1c), "+v"(xa), "+v"(xb), "+v"(gbf));
    v2u yp[8];
#pragma unroll
    for (int s = 0; s < 8; ++s) yp[s] = v2u{0u, 0u};
    unsigned miss = 0xFFu;
    for (unsigned it = 0;; ++it) {
      MEMFENCE();
#pragma unroll
      for (int s = 0; s < 8; ++s) if (miss & (1u << s)) yp[s] = ld64<SC1>(ws, WS_YP + s * 2048 + tid * 8);
#pragma unroll
      for (int s = 0; s < 8; ++s) if ((miss & (1u << s)) && __all(mx2(0u, yp[s]) != EMPTY)) miss &= ~(1u << s);
      if (miss == 0u || EXPIRED(it)) break;
    }
    const unsigned atp = cvtpk(((asf(yp[0][0]) + asf(yp[1][0])) + (asf(yp[2][0]) + asf(yp[3][0]))) + ((asf(yp[4][0]) + asf(yp[5][0])) + (asf(yp[6][0]) + asf(yp[7][0]))),
                               ((asf(yp[0][1]) + asf(yp[1][1])) + (asf(yp[2][1]) + asf(yp[3][1]))) + ((asf(yp[4][1]) + asf(yp[5][1])) + (asf(yp[6][1]) + asf(yp[7][1]))));
    const unsigned gap = cvtpk(ga0 * lo16(atp), ga1 * hi16(atp));
    const unsigned x1p = cvtpk(xa + lo16(gap), xb + hi16(gap));
    const float x1a = lo16(x1p), x1b = hi16(x1p);
    const float sh = asf(__builtin_amdgcn_readfirstlane(asu(x1a)));
    const float da = x1a - sh, db = x1b - sh;
    const float s1 = wave_sum(da + db), s2 = wave_sum(fmaf(da, da, db * db));
    *lds_pl<f32x2>(lb + L_ST + wave * 8) = f32x2{fmaf(s1, 1.f / 128.f, sh), fmaf(-s1 * (1.f / 128.f), s1, s2)};
    *lds_pl<f32x2>(lb + L_X1 + tid * 8) = f32x2{x1a, x1b};
    wg_barrier();
    if (has_gu) {
      const f32x4 sA = *lds_pl<f32x4>(lb + L_ST), sB = *lds_pl<f32x4>(lb + L_ST + 16);
      const float mean = ((sA[0] + sA[2]) + (sB[0] + sB[2])) * 0.25f;
      const float d0 = sA[0] - mean, d1 = sA[2] - mean, d2 = sB[0] - mean, d3 = sB[2] - mean;
      const float var = fmaxf(((sA[1] + sA[3]) + (sB[1] + sB[3])) + 128.f * ((d0 * d0 + d1 * d1) + (d2 * d2 + d3 * d3)), 0.f) * (1.f / 512.f);
      float mb2, sd2;
      ln_round(mean, var, mb2, sd2);
      const float rs = __builtin_amdgcn_rcpf(sd2);
      const unsigned e0 = cvtpk(x1a - mb2, x1b - mb2), e1 = cvtpk(lo16(e0) * rs, hi16(e0) * rs), e2 = cvtpk(lo16(e1) * t0c, hi16(e1) * t1c);
      const unsigned hq = cvtpk(lo16(e2) + sb0, hi16(e2) + sb1);
      float y[8] = {0.f, 0.f, 0.f, 0.f, 0.f, 0.f, 0.f, 0.f};
      dot8(y, dppu<0x00>(hq), wv[0], wv[1]); dot8(y, dppu<0x55>(hq), wv[2], wv[3]);
      dot8(y, dppu<0xAA>(hq), wv[4], wv[5]); dot8(y, dppu<0xFF>(hq), wv[6], wv[7]);
      float u0, u1;
      rows_reduce8(y, u0, u1);
      u0 += dpp<ROR8>(u0); u1 += dpp<ROR8>(u1); u0 += dpp<ROR4>(u0); u1 += dpp<ROR4>(u1);
      *lds_pl<f32x2>(lb + L_RED3 + wave * 128 + (8 * (c & 3) + 2 * g) * 4) = f32x2{u0, u1};
      wg_barrier();
      const unsigned ra = lb + L_RED3 + c * 4;
      const float gs = rbf((*lds_pl<float>(ra) + *lds_pl<float>(ra + 128)) + (*lds_pl<float>(ra + 256) + *lds_pl<float>(ra + 384)));
      const float us = rbf((*lds_pl<float>(ra + 64) + *lds_pl<float>(ra + 192)) + (*lds_pl<float>(ra + 320) + *lds_pl<float>(ra + 448)));
      const float z = 0.7978845608028654f * fmaf(0.044715f * gs * gs, gs, gs);
      const float th = fmaf(-2.f, __builtin_amdgcn_rcpf(1.f + __expf(2.f * z)), 1.f);
      const float h2 = rbf(rbf(0.5f * gs * (1.f + th)) * us);
      const unsigned hp = __builtin_amdgcn_perm(asu(dpp<XOR1>(h2)), asu(h2), 0x07060302u);
      float d0a = 0.f, d1a = 0.f;
#pragma unroll
      for (int e = 0; e < 16; e += 2) {
        const unsigned he = __builtin_amdgcn_readlane(hp, e);
        d0a = dot2(wdv[e], he, d0a); d1a = dot2(wdv[e + 1], he, d1a);
      }
      st64(ws, WS_DP + up * 2048 + tid * 8, v2u{noempty(asu(d0a)), noempty(asu(d1a))});
      st64(ws, (b | tid) == 0 ? WS_EPOCH : OOB, v2u{tag.lo, tag.hi});
    }
  }
  if (wave != 0) return;
  {
    v4u da = {EMPTY, 0u, 0u, 0u}, db = {lane < NGU - 64 ? EMPTY : 0u, 0u, 0u, 0u};
    const float x1o = *lds_pl<float>(lb + L_X1 + (fc0 + g) * 4);
    for (unsigned it = 0;; ++it) {
      MEMFENCE();
      const bool ma = mx4(0u, da) == EMPTY, mb_ = mx4(0u, db) == EMPTY;
      if (!__any(ma | mb_) || EXPIRED(it)) break;
      const v4u na = ld128<SC1>(ws, ma ? WS_DP + lane * 2048 + fc0 * 4 : OOB), nb = ld128<SC1>(ws, mb_ ? WS_DP + (lane + 64) * 2048 + fc0 * 4 : OOB);
      da = ma ? na : da; db = mb_ ? nb : db;
    }
    float t4[4];
#pragma unroll
    for (int r = 0; r < 4; ++r) t4[r] = asf(da[r]) + asf(db[r]);
    const float r = row_sum(sw16_add(sw32_add(t4[0], t4[2]), sw32_add(t4[1], t4[3])));
    const float res = x1o + rbf(gbf * rbf(r));
    if (c == 0) a.out[fc0 + g] = (u16)(cvtpk(res, res) & 0xffffu);
  }
}
}

extern "C" void kernel_launch(void* const* d_in, const int* in_sizes, int n_in, void* d_out, int out_size, void* d_ws, size_t ws_size,
                              hipStream_t stream) {
  Args a;
  a.x = (const u16*)d_in[0]; a.K = (const u16*)d_in[1]; a.V = (const u16*)d_in[2]; a.wq = (const u16*)d_in[3]; a.wo = (const u16*)d_in[4];
  a.wg = (const u16*)d_in[6]; a.wu = (const u16*)d_in[7]; a.wd = (const u16*)d_in[8]; a.c = (const u16*)d_in[9]; a.wm = (const u16*)d_in[10];
  a.out = (u16*)d_out; a.ws = (unsigned char*)d_ws;
  (void)hipFuncSetAttribute((const void*)block_decode, hipFuncAttributeMaxDynamicSharedMemorySize, (int)LDS_BYTES);
  hipLaunchKernelGGL(block_decode, dim3(NWG), dim3(NTHR), LDS_BYTES, stream, a);
}
```

```cpp
#include <hip/hip_runtime.h>
#include <hip/hip_bf16.h>

#define GAS __attribute__((address_space(1)))
#define LAS __attribute__((address_space(3)))
typedef unsigned short u16;
typedef unsigned long long u64;
typedef unsigned v4u __attribute__((ext_vector_type(4)));
typedef unsigned v2u __attribute__((ext_vector_type(2)));
typedef int v4i __attribute__((ext_vector_type(4)));
typedef float f32x4 __attribute__((ext_vector_type(4)));
typedef __bf16 bf16x8 __attribute__((ext_vector_type(8)));
typedef __bf16 bf16x2 __attribute__((ext_vector_type(2)));
typedef float f32x2 __attribute__((ext_vector_type(2)));

namespace {
constexpr int DM = 512, SK = 65536, DH = 128, DF = 1792;
constexpr int NWG = 256, NTHR = 384;
constexpr int SLOT = 16384, NSLOT = 8, FH = 1024;
constexpr int SO = 116;
constexpr int NP = 6;
constexpr int POOL0 = 8 * SO, KD0 = POOL0 + 8 * NP, NKT = (FH - KD0) / 4, NPT = 8 * NP / 2;
constexpr int BURST = 3;
constexpr int CAP = 144;
static_assert(NP > BURST && (FH - KD0) % 4 == 0 && SO % 4 == 0 && NP % 2 == 0 && 8 * NP + 16 <= SO && NP <= 8 && 8 * (CAP - 7 - SO) >= FH - KD0, "fill plan");
constexpr int PACE_FROM = 16, PACE_WAIT = 20;
constexpr float PACE_PERIOD = 49.f, PACE_BACKOFF = 17.f;
constexpr unsigned TW = 10800, TD = 110;
constexpr int INF = 0x7FFFFFFF;
constexpr unsigned HEAD_BYTES = (unsigned)SK * DH * 2;
constexpr unsigned EMPTY = 0xFFFFFFFFu;
constexpr unsigned OOB = 0x7FFF0000u;
constexpr u64 TLIMIT = 5000000ull;
constexpr float LOG2E = 1.4426950408889634f, SCALE = 0.08838834764831845f;
constexpr int NGU = DF / 16, REC = 640;
constexpr unsigned WS_EPOCH = 0, WS_P1 = 4096, WS_QS = WS_P1 + 32768, WS_M2P = WS_QS + 131072, WS_M2 = WS_M2P + 131072, WS_CTR = WS_M2 + 16384,
                   WS_MX = WS_CTR + 8192, WS_YP = WS_MX + 2048, WS_PO = WS_YP + 16384, WS_DP = WS_PO + NWG * REC,
                   WS_SG = WS_DP + NGU * 2048, WS_END = WS_SG + NWG * NP * 128;
constexpr unsigned L_RING = 0, L_CTL = NSLOT * SLOT, L_FT = L_CTL, L_SCR = L_CTL + 8, L_NK = L_CTL + 12, L_DONE = L_CTL + 16, L_QCNT = L_CTL + 32,
                   L_AUXPUB = L_CTL + 36, L_QIN = L_CTL + 40, L_ISSUED = L_CTL + 44, L_M2RDY = L_CTL + 48, L_TKC = L_CTL + 52, L_TK = L_CTL + 56,
                   L_KCNT = L_CTL + 64, L_MH = L_CTL + 68, L_MXW = L_CTL + 80, L_LW = L_CTL + 96, L_LIST = L_CTL + 128, L_RED = L_LIST + 128, L_QSC = L_RED + 1024, L_RED3 = L_QSC + 256,
                   L_ST = L_RED3 + 512, L_M2 = L_ST + 64, L_X1 = L_M2 + 4096, L_MRG = L_X1 + 2048, L_SH = L_X1, L_SC = L_MRG + 2048, L_END = L_SC + CAP * 128;
constexpr unsigned LDS_BYTES = L_END;
static_assert(LDS_BYTES <= 160 * 1024 && LDS_BYTES > 80 * 1024, "one workgroup per CU");

struct Args {
  const u16 *x, *K, *V, *wq, *wo, *wg, *wu, *wd, *c, *wm;
  u16* out;
  unsigned char* ws;
};

__device__ __forceinline__ u64 rt() { return __builtin_amdgcn_s_memrealtime(); }
__device__ __forceinline__ float asf(unsigned u) { return __builtin_bit_cast(float, u); }
__device__ __forceinline__ unsigned asu(float f) { return __builtin_bit_cast(unsigned, f); }
__device__ __forceinline__ float lo16(unsigned u) { return asf(u << 16); }
__device__ __forceinline__ float hi16(unsigned u) { return asf(u & 0xffff0000u); }
__device__ __forceinline__ unsigned cvtpk(float a, float b) {
  const f32x2 v = {a, b};
  return __builtin_bit_cast(unsigned, __builtin_convertvector(v, bf16x2));
}
__device__ __forceinline__ float rbf(float f) { return lo16(cvtpk(f, f)); }
__device__ __forceinline__ unsigned noempty(unsigned v) { return v < EMPTY - 1u ? v : EMPTY - 1u; }
__device__ __forceinline__ float dot2(unsigned a, unsigned b, float c) {
  return __builtin_amdgcn_fdot2_f32_bf16(__builtin_bit_cast(bf16x2, a), __builtin_bit_cast(bf16x2, b), c, false);
}
template <int CTRL> __device__ __forceinline__ float dpp(float v) {
  return asf(__builtin_amdgcn_mov_dpp(asu(v), CTRL, 0xf, 0xf, true));
}
constexpr int ROR8 = 0x128, ROR4 = 0x124, ROR2 = 0x122, ROR1 = 0x121, XOR2 = 0x4E, XOR1 = 0xB1;
template <int CTRL> __device__ __forceinline__ unsigned dppu(unsigned v) { return __builtin_amdgcn_mov_dpp(v, CTRL, 0xf, 0xf, true); }
__device__ __forceinline__ float sw32_add(float a, float b) {
  const auto r = __builtin_amdgcn_permlane32_swap(asu(a), asu(b), false, false);
  return asf(r[0]) + asf(r[1]);
}
__device__ __forceinline__ float sw16_add(float a, float b) {
  const auto r = __builtin_amdgcn_permlane16_swap(asu(a), asu(b), false, false);
  return asf(r[0]) + asf(r[1]);
}
__device__ __forceinline__ float xrow_max(float v) {
  const auto r = __builtin_amdgcn_permlane16_swap(asu(v), asu(v), false, false);
  v = fmaxf(asf(r[0]), asf(r[1]));
  const auto s = __builtin_amdgcn_permlane32_swap(asu(v), asu(v), false, false);
  return fmaxf(asf(s[0]), asf(s[1]));
}
__device__ __forceinline__ unsigned pack_halves(float v) {
  const auto r = __builtin_amdgcn_permlane32_swap(asu(v), asu(v), false, false);
  return __builtin_amdgcn_perm(r[1], r[0], 0x07060302u);
}
__device__ __forceinline__ float row_sum(float v) { v += dpp<ROR8>(v); v += dpp<ROR4>(v); v += dpp<ROR2>(v); v += dpp<ROR1>(v); return v; }
__device__ __forceinline__ float row_max(float v) { v = fmaxf(v, dpp<ROR8>(v)); v = fmaxf(v, dpp<ROR4>(v)); v = fmaxf(v, dpp<ROR2>(v)); return fmaxf(v, dpp<ROR1>(v)); }
__device__ __forceinline__ float wave_sum(float v) { v = row_sum(v); v = sw16_add(v, v); return sw32_add(v, v); }
__device__ __forceinline__ void rows_reduce8(const float (&a)[8], float& u0, float& u1) {
  const float r0 = sw32_add(a[0], a[4]), r1 = sw32_add(a[1], a[5]), r2 = sw32_add(a[2], a[6]), r3 = sw32_add(a[3], a[7]);
  u0 = sw16_add(r0, r2); u1 = sw16_add(r1, r3);
}
__device__ __forceinline__ void fma8(float (&a)[8], float h, v4u w) {
#pragma unroll
  for (int e = 0; e < 4; ++e) { a[2 * e] = fmaf(h, lo16(w[e]), a[2 * e]); a[2 * e + 1] = fmaf(h, hi16(w[e]), a[2 * e + 1]); }
}
__device__ __forceinline__ void kpair(v4u& a, v4u& b) {
  const v4u r0 = a, r1 = b;
  a = v4u{__builtin_amdgcn_perm(r1[0], r0[0], 0x05040100u), __builtin_amdgcn_perm(r1[0], r0[0], 0x07060302u),
          __builtin_amdgcn_perm(r1[1], r0[1], 0x05040100u), __builtin_amdgcn_perm(r1[1], r0[1], 0x07060302u)};
  b = v4u{__builtin_amdgcn_perm(r1[2], r0[2], 0x05040100u), __builtin_amdgcn_perm(r1[2], r0[2], 0x07060302u),
          __builtin_amdgcn_perm(r1[3], r0[3], 0x05040100u), __builtin_amdgcn_perm(r1[3], r0[3], 0x07060302u)};
}
__device__ __forceinline__ void dot8(float (&acc)[8], unsigned hp, v4u a, v4u b) {
#pragma unroll
  for (int e = 0; e < 4; ++e) { acc[e] = dot2(a[e], hp, acc[e]); acc[4 + e] = dot2(b[e], hp, acc[4 + e]); }
}
__device__ __forceinline__ f32x4 mfma(v4u a, v4u b, f32x4 c) {
  return __builtin_amdgcn_mfma_f32_16x16x32_bf16(__builtin_bit_cast(bf16x8, a), __builtin_bit_cast(bf16x8, b), c, 0, 0, 0);
}
typedef __amdgpu_buffer_rsrc_t rsrc_t;
__device__ __forceinline__ rsrc_t mkrsrc(const void* p, unsigned bytes) { return __builtin_amdgcn_make_buffer_rsrc((void*)p, 0, bytes, 0x00020000); }
constexpr int SC1 = 16, NT = 2;
template <int AUX> __device__ __forceinline__ v4u ld128(rsrc_t r, unsigned off) { return __builtin_amdgcn_raw_buffer_load_b128(r, off, 0, AUX); }
template <int AUX> __device__ __forceinline__ v2u ld64(rsrc_t r, unsigned off) { return __builtin_amdgcn_raw_buffer_load_b64(r, off, 0, AUX); }
template <int AUX> __device__ __forceinline__ unsigned ld32(rsrc_t r, unsigned off) { return __builtin_amdgcn_raw_buffer_load_b32(r, off, 0, AUX); }
__device__ __forceinline__ void st128(rsrc_t r, unsigned off, v4u v) { __builtin_amdgcn_raw_buffer_store_b128(v, r, off, 0, SC1); }
__device__ __forceinline__ void st64(rsrc_t r, unsigned off, v2u v) { __builtin_amdgcn_raw_buffer_store_b64(v, r, off, 0, SC1); }
__device__ __forceinline__ void st32(rsrc_t r, unsigned off, unsigned v) { __builtin_amdgcn_raw_buffer_store_b32(v, r, off, 0, SC1); }
__device__ __forceinline__ unsigned umax(unsigned a, unsigned b) { return a > b ? a : b; }
__device__ __forceinline__ unsigned mx4(unsigned m, v4u v) { return umax(umax(umax(m, v[0]), v[1]), umax(v[2], v[3])); }
__device__ __forceinline__ unsigned mx2(unsigned m, v2u v) { return umax(umax(m, v[0]), v[1]); }
__device__ __forceinline__ v4u noempty4(f32x4 v) { return v4u{noempty(asu(v[0])), noempty(asu(v[1])), noempty(asu(v[2])), noempty(asu(v[3]))}; }
struct Tag { unsigned lo, hi; };
template <int LB> __device__ __forceinline__ unsigned tg_hi(Tag t) { return LB ? __builtin_amdgcn_alignbit(t.hi, t.lo, LB) : t.lo; }
template <int LB> __device__ __forceinline__ unsigned gr_lo(float v, Tag t) { return ((asu(v) + ((1u << LB) >> 1)) & (~0u << LB)) | (t.lo & ((1u << LB) - 1u)); }
template <int LB> __device__ __forceinline__ v2u gr1(float a, Tag t) { return v2u{gr_lo<LB>(a, t), tg_hi<LB>(t)}; }
template <int LB> __device__ __forceinline__ v4u gr2(float a, float b, Tag t) { return v4u{gr_lo<LB>(a, t), tg_hi<LB>(t), gr_lo<LB>(b, t), tg_hi<LB>(t)}; }
template <int LB> __device__ __forceinline__ unsigned gr_bad(v4u g, Tag t) {
  return (((g[0] ^ t.lo) | (g[2] ^ t.lo)) & ((1u << LB) - 1u)) | (g[1] ^ tg_hi<LB>(t)) | (g[3] ^ tg_hi<LB>(t));
}
template <int LB> __device__ __forceinline__ float gr_val(unsigned lo) { return asf(lo & (~0u << LB)); }
template <int LB> __device__ __forceinline__ float gr_sum(v4u g) { const float s = sw32_add(gr_val<LB>(g[0]), gr_val<LB>(g[2])); return sw16_add(s, s); }
__device__ __forceinline__ Tag next_tag(v2u ep) { const u64 e = (((u64)ep[1] << 32) | ep[0]) + 1ull; return Tag{(unsigned)e, (unsigned)(e >> 32)}; }
__device__ __forceinline__ v4u sel4(bool t, v4u a, v4u b) { return v4u{t ? a[0] : b[0], 0u, t ? a[2] : b[2], 0u}; }
#define MEMFENCE() asm volatile("" ::: "memory")
#define LGKM0() asm volatile("s_waitcnt lgkmcnt(0)" ::: "memory")
#define VM0() asm volatile("s_waitcnt vmcnt(0)" ::: "memory")
#define PIN4(a) asm volatile("" : "+v"((a)[0]), "+v"((a)[1]), "+v"((a)[2]), "+v"((a)[3]))
template <int N> __device__ __forceinline__ void vmwait() { asm volatile("s_waitcnt vmcnt(%0)" ::"n"(N) : "memory"); }
__device__ __forceinline__ void wg_barrier() { LGKM0(); __builtin_amdgcn_s_barrier(); MEMFENCE(); }
template <typename T> __device__ __forceinline__ volatile LAS T* lds_at(unsigned off) { return (volatile LAS T*)(size_t)off; }
template <typename T> __device__ __forceinline__ LAS T* lds_pl(unsigned off) { return (LAS T*)(size_t)off; }
__device__ __forceinline__ void lds_wait_ge(unsigned addr, unsigned need, u64 t0) {
  for (unsigned sp = 0; *lds_at<unsigned>(addr) < need; ++sp) { __builtin_amdgcn_s_sleep(1); if ((sp & 63u) == 63u && rt() - t0 > TLIMIT) break; }
  MEMFENCE();
}

#define EXPIRED(it) (((it) & 63u) == 63u && rt() - t0 > TLIMIT)

#define DMA4(M, O) "s_mov_b32 m0, " M "\n\ts_nop 0\n\t" \
                   "buffer_load_dwordx4 %[v0], %[d], " O " offen nt lds\n\tbuffer_load_dwordx4 %[v1], %[d], " O " offen offset:1024 nt lds\n\t" \
                   "buffer_load_dwordx4 %[v2], %[d], " O " offen offset:2048 nt lds\n\tbuffer_load_dwordx4 %[v3], %[d], " O " offen offset:3072 nt lds\n\t"
__device__ __forceinline__ void dma_fill(v4i srd, unsigned o, unsigned sl, unsigned v0, unsigned v1, unsigned v2, unsigned v3) {
  asm volatile(DMA4("%[m0]", "%[o0]") DMA4("%[m1]", "%[o1]") DMA4("%[m2]", "%[o2]") DMA4("%[m3]", "%[o3]")
               ::[d] "s"(srd), [v0] "v"(v0), [v1] "v"(v1), [v2] "v"(v2), [v3] "v"(v3),
                 [m0] "s"(sl), [m1] "s"(sl + 4096), [m2] "s"(sl + 8192), [m3] "s"(sl + 12288),
                 [o0] "s"(o), [o1] "s"(o + 4096), [o2] "s"(o + 32768), [o3] "s"(o + 36864) : "memory");
}

__device__ __forceinline__ float silu_bf(float v) {
  const float t = -v * LOG2E, r = fmaf(-v, 1.9259629911e-8f, fmaf(-v, LOG2E, -t));
  const float e = __builtin_amdgcn_exp2f(t);
  return rbf(v / (1.f + (e < INFINITY ? fmaf(e * 0.6931471806f, r, e) : e)));
}
__device__ __forceinline__ void ln_round(float mean, float var, float& mb, float& sd) {
  mb = rbf(mean);
  sd = rbf(__builtin_amdgcn_sqrtf(rbf(rbf(var) + 1e-5f)));
}
__device__ __forceinline__ float mod_round(float n, float sc, float sh) { return rbf(rbf(rbf(n) * rbf(1.f + sc)) + sh); }

__global__ void __launch_bounds__(NTHR, 2) block_decode(Args a) {
  extern __shared__ __attribute__((aligned(16))) unsigned char smem[];
  const int tid = threadIdx.x, lane = tid & 63, wave = __builtin_amdgcn_readfirstlane(tid >> 6);
  const int b = blockIdx.x, x = b & 7, j = b >> 3;
  const u64 t0 = rt();
  const unsigned lb = (unsigned)(size_t)(LAS unsigned char*)smem;
  const rsrc_t ws = mkrsrc(a.ws, WS_END);
  const int kq = j >> 3, cb = x + 8 * (j & 7);
  const int gq = j >> 2;
  const bool mlp = (x & 1) == 0;
  const int og = (x & 6) + (j >> 4), oc0 = 32 * (j & 15);
  const bool has_gu = mlp && 4 * gq + (x >> 1) < 28;
  const int up = 4 * (4 * gq + (x >> 1)) + (j & 3);
  const int fc0 = 64 * x + 4 * j;

  if (tid < 20) *lds_pl<unsigned>(lb + L_CTL + 4 * tid) = (tid == 1 || tid == 3) ? (unsigned)INF : 0u;
  asm volatile("" :: "s"(a.K), "s"(a.ws));
  wg_barrier();
  int w_a = wave, w_b = wave;
  asm volatile("" : "+s"(w_a));
  if (w_a == 4) {
    const u64 kb = (u64)(a.K + (size_t)j * SK * DH), vb = (u64)(a.V + (size_t)j * SK * DH);
    const v4i ksrd = {(int)kb, (int)(kb >> 32), (int)HEAD_BYTES, 0x00020000}, vsrd = {(int)vb, (int)(vb >> 32), (int)HEAD_BYTES, 0x00020000};
    const unsigned g = lane >> 4, c = lane & 15;
    const unsigned k0 = g * 256 + ((c ^ g) << 4), k1 = g * 256 + ((c ^ (4 + g)) << 4), k2 = g * 256 + ((c ^ (8 + g)) << 4), k3 = g * 256 + ((c ^ (12 + g)) << 4);
    const unsigned vl = lane * 16;
    unsigned sl = lb + L_RING;
    auto foff = [](int n) { return ((unsigned)n >> 2) * 65536u + ((unsigned)n & 3u) * 8192u; };
    auto step = [&]() { sl = sl + SLOT == lb + L_RING + NSLOT * SLOT ? lb + L_RING : sl + SLOT; };
    volatile LAS u64* FT = lds_at<u64>(lb + L_FT);
    int pub = 0;
    unsigned total = (unsigned)INF;
    auto publish = [&](int n) { pub = n > pub ? n : pub; *FT = (u64)(unsigned)pub | ((u64)total << 32); };
    auto wait_free = [&](int i) {
      const int p = i - NSLOT;
      volatile LAS u64* dn = lds_at<u64>(lb + L_DONE + 8 * (p & 1));
      const unsigned need = (unsigned)(p >> 1) + 1u;
      auto freed = [&]() { const u64 d = *dn; return (unsigned)d >= need && (unsigned)(d >> 32) >= need; };
      if (!freed()) {
        VM0(); publish(i);
        for (unsigned sp = 0; !freed(); ++sp) { __builtin_amdgcn_s_sleep(1); if (EXPIRED(sp)) break; }
      }
      MEMFENCE();
    };
    for (unsigned sp = 0; *lds_at<unsigned>(lb + L_ISSUED) < 5u && sp < 4096u; ++sp) {}
    unsigned period = 0, pause = 0, last = 0, c0 = (unsigned)__builtin_amdgcn_s_memtime(), r0 = (unsigned)rt();
    auto beat = [&]() {
      const unsigned ta = (unsigned)__builtin_amdgcn_s_memtime();
      vmwait<PACE_WAIT>();
      const unsigned tb = (unsigned)__builtin_amdgcn_s_memtime();
      unsigned due = last + period;
      if (tb - ta > 96u && (int)(tb + pause - due) > 0) due = tb + pause;
      do last = (unsigned)__builtin_amdgcn_s_memtime(); while (period && (int)(last - due) < 0);
    };
    auto measure = [&]() {
      const unsigned c1 = (unsigned)__builtin_amdgcn_s_memtime(), r1 = (unsigned)rt();
      const float cpt = fminf((float)(c1 - c0) * __builtin_amdgcn_rcpf((float)(r1 - r0)), 32.f);
      period = __builtin_amdgcn_readfirstlane((unsigned)(cpt * PACE_PERIOD));
      pause = __builtin_amdgcn_readfirstlane((unsigned)(cpt * PACE_BACKOFF));
      c0 = c1; r0 = r1;
    };
    unsigned tn = 1;
    auto take = [&]() {
      u64 tv;
      for (unsigned sp = 0; (unsigned)((tv = *lds_at<u64>(lb + L_TK)) >> 32) < tn; ++sp) if (EXPIRED(sp)) { tv = EMPTY; break; }
      ++tn;
      return (unsigned)__builtin_amdgcn_readfirstlane((unsigned)tv);
    };
#pragma unroll
    for (int i = 0; i < BURST; ++i) { dma_fill(ksrd, foff(POOL0 + x * NP + i), sl, k0, k1, k2, k3); step(); }
    int i = BURST, nt = 0, next = POOL0 + x * NP + BURST, left = NP - BURST;
    bool asked = true;
    for (;; ++i) {
      if (i < PACE_FROM) vmwait<16>();
      beat();
      dma_fill(ksrd, foff(next), sl, k0, k1, k2, k3);
      if ((i & 15) == 0) measure();
      step();
      ++next; --left;
      vmwait<32>();
      publish(i - 1);
      if (i == NP + SO - 5) *lds_at<unsigned>(lb + L_TKC) = tn;
      if (left == 0) {
        if (i == NP - 1) { next = x * SO; left = SO; }
        else {
          const unsigned t = asked ? take() : EMPTY;
          if (t >= (unsigned)NKT) break;
          left = 4; next = KD0 + 4 * (int)t;
          *lds_at<unsigned>(lb + L_LIST + 4 * nt) = t; ++nt;
          asked = i + 1 - NP + left + 4 <= CAP;
          if (asked) *lds_at<unsigned>(lb + L_TKC) = tn;
        }
      }
      if (i + 1 >= NSLOT) wait_free(i + 1);
    }
    const int nk = i + 1, vown = 2 * nk - NP;
    *lds_at<unsigned>(lb + L_NK) = (unsigned)nk;
    wait_free(i + 1);
    next = x * SO; left = SO;
    int rt_ = 0;
    for (++i;; ++i) {
      if (i >= vown - 8) { period = 0; vmwait<16>(); }
      beat();
      dma_fill(vsrd, foff(next), sl, vl, vl, vl, vl);
      if ((i & 15) == 0) measure();
      step();
      ++next; --left;
      vmwait<32>();
      publish(i - 1);
      if (i == vown - 3) *lds_at<unsigned>(lb + L_TKC) = tn;
      if (left == 0) {
        if (rt_ < nt) { const unsigned t = __builtin_amdgcn_readfirstlane(*lds_at<unsigned>(lb + L_LIST + 4 * rt_)); ++rt_; left = 4; next = KD0 + 4 * (int)t; }
        else {
          const unsigned t = take();
          if (t >= (unsigned)NPT) break;
          *lds_at<unsigned>(lb + L_TKC) = tn;
          left = 2; next = POOL0 + 2 * (int)t;
        }
      }
      wait_free(i + 1);
    }
    total = (unsigned)i + 1u;
    publish(i - 1);
    vmwait<16>(); publish(i);
    VM0(); publish(i + 1);
    return;
  }

  asm volatile("" : "+s"(w_b));
  if (w_b == 5) {
    __builtin_amdgcn_s_setprio(3);
    const rsrc_t rwm = mkrsrc(a.wm, DM * 3072 * 2), rc = mkrsrc(a.c, DM * 2);
    const int n0 = 512 * (gq & 1) + 64 * x + 16 * (j & 3);
    const int ar = 128 * kq + 4 * (lane >> 1);
    const v2u c4 = ld64<0>(rc, ar * 2);
    MEMFENCE();
    v4u w1[4];
#pragma unroll
    for (int i = 0; i < 4; ++i) w1[i] = ld128<NT>(rwm, (unsigned)((ar + i) * 3072 + n0 + 8 * (lane & 1)) * 2);
    const v2u ep = ld64<SC1>(ws, WS_EPOCH);
    const int mr = 64 * gq + 8 * (lane >> 3), mc = 64 * (x + 8 * (j & 3));
    const v4u cv = ld128<0>(rc, mr * 2);
    MEMFENCE();
    if (lane == 0) __hip_atomic_fetch_add(lds_pl<unsigned>(lb + L_ISSUED), 1u, __ATOMIC_RELAXED, __HIP_MEMORY_SCOPE_WORKGROUP);
    st128(ws, (lane < 8 && !mlp) ? WS_YP + (og * 512 + oc0) * 4 + lane * 16 : OOB, v4u{EMPTY, EMPTY, EMPTY, EMPTY});
    st128(ws, has_gu ? WS_DP + up * 2048 + lane * 16 : OOB, v4u{EMPTY, EMPTY, EMPTY, EMPTY});
    st128(ws, has_gu ? WS_DP + up * 2048 + 1024 + lane * 16 : OOB, v4u{EMPTY, EMPTY, EMPTY, EMPTY});
    st128(ws, lane < 33 ? WS_PO + b * REC + lane * 16 : OOB, v4u{EMPTY, EMPTY, EMPTY, EMPTY});
    st32(ws, (lane < 2 && x == 0) ? WS_CTR + j * 256 + lane * 128 : OOB, 0u);
    const Tag tag = next_tag(ep);
    {
      float s8[8] = {0.f, 0.f, 0.f, 0.f, 0.f, 0.f, 0.f, 0.f};
      fma8(s8, silu_bf(lo16(c4[0])), w1[0]); fma8(s8, silu_bf(hi16(c4[0])), w1[1]);
      fma8(s8, silu_bf(lo16(c4[1])), w1[2]); fma8(s8, silu_bf(hi16(c4[1])), w1[3]);
      float u0, u1;
      rows_reduce8(s8, u0, u1);
      u0 += dpp<ROR8>(u0); u1 += dpp<ROR8>(u1); u0 += dpp<ROR4>(u0); u1 += dpp<ROR4>(u1); u0 += dpp<XOR2>(u0); u1 += dpp<XOR2>(u1);
      const int col = n0 + 8 * (lane & 1) + 2 * (lane >> 4);
      st128(ws, (lane & 15) < 2 ? WS_P1 + (kq * 1024 + col) * 8 : OOB, gr2<0>(u0, u1, tag));
      *lds_at<unsigned>(lb + L_AUXPUB) = 1u;
    }
    __builtin_amdgcn_s_setprio(0);
    auto stream_at = [&](unsigned fillno) {
      for (unsigned sp = 0; *lds_at<unsigned>(lb + L_FT) < fillno; ++sp) { __builtin_amdgcn_s_sleep(8); if ((sp & 63u) == 63u && rt() - t0 > TLIMIT) break; }
      MEMFENCE();
    };
    stream_at(NSLOT + 4);
    {
      v4u w2[8];
#pragma unroll
      for (int i = 0; i < 8; ++i) w2[i] = ld128<NT>(rwm, (unsigned)((mr + i) * 3072 + 1024 + mc + 8 * (lane & 7)) * 2);
      float s8[8] = {0.f, 0.f, 0.f, 0.f, 0.f, 0.f, 0.f, 0.f};
#pragma unroll
      for (int e = 0; e < 4; ++e) { fma8(s8, silu_bf(lo16(cv[e])), w2[2 * e]); fma8(s8, silu_bf(hi16(cv[e])), w2[2 * e + 1]); }
      float u0, u1;
      rows_reduce8(s8, u0, u1);
      u0 += dpp<ROR8>(u0); u1 += dpp<ROR8>(u1);
      st128(ws, (lane & 8) == 0 ? WS_M2P + (gq * 2048 + mc + 8 * (lane & 7) + 2 * (lane >> 4)) * 8 : OOB, gr2<4>(u0, u1, tag));
    }
    stream_at(NSLOT + 16);
    {
      v4u m;
      for (;;) {
        MEMFENCE();
        m = ld128<SC1>(ws, WS_M2P + (((lane >> 2) & 7) * 2048 + 8 * b + 2 * (lane & 3)) * 8);
        if (__all(gr_bad<4>(m, tag) == 0u) || rt() - t0 > TLIMIT) break;
        __builtin_amdgcn_s_sleep(32);
      }
      float u0 = gr_val<4>(m[0]), u1 = gr_val<4>(m[2]);
      u0 = sw16_add(u0, u0); u1 = sw16_add(u1, u1);
      u0 += dpp<ROR8>(u0); u1 += dpp<ROR8>(u1); u0 += dpp<ROR4>(u0); u1 += dpp<ROR4>(u1);
      VM0();
      st128(ws, lane < 4 ? WS_M2 + b * 64 + lane * 16 : OOB, gr2<4>(rbf(u0), rbf(u1), tag));
    }
    stream_at(NSLOT + 28);
#pragma unroll 1
    for (int u = 0; u < 16; ++u) {
      v4u m;
      for (;;) {
        MEMFENCE();
        m = ld128<SC1>(ws, WS_M2 + u * 1024 + lane * 16);
        if (__all(gr_bad<4>(m, tag) == 0u) || rt() - t0 > TLIMIT) break;
        __builtin_amdgcn_s_sleep(32);
      }
      *lds_pl<unsigned>(lb + L_M2 + u * 256 + lane * 4) = __builtin_amdgcn_perm(m[2], m[0], 0x07060302u);
      __builtin_amdgcn_s_sleep(16);
    }
    LGKM0();
    *lds_at<unsigned>(lb + L_M2RDY) = 1u;
    GAS unsigned* ctr = (GAS unsigned*)(a.ws + WS_CTR + j * 256);
    unsigned n = 1;
    auto draw = [&](int which) {
      unsigned v = 0;
      if (lane == 0) v = __hip_atomic_fetch_add(ctr + 32 * which, 1u, __ATOMIC_RELAXED, __HIP_MEMORY_SCOPE_AGENT);
      v = __builtin_amdgcn_readfirstlane(v);
      *lds_at<u64>(lb + L_TK) = ((u64)n << 32) | v;
      ++n;
      return v;
    };
    for (;;) {
      bool over = false;
      for (unsigned sp = 0; *lds_at<unsigned>(lb + L_TKC) < n; ++sp) {
        if (*lds_at<unsigned>(lb + L_NK) != (unsigned)INF || EXPIRED(sp)) { over = true; break; }
        __builtin_amdgcn_s_sleep(2);
      }
      if (over || *lds_at<unsigned>(lb + L_NK) != (unsigned)INF || draw(0) >= (unsigned)NKT) break;
    }
    lds_wait_ge(lb + L_KCNT, 5u, t0);
    __builtin_amdgcn_s_sleep(127);
    {
      v2u mx;
      for (;;) {
        MEMFENCE();
        mx = ld64<SC1>(ws, WS_MX + j * 64 + (lane & 7) * 8);
        if (__all(gr_bad<4>(v4u{mx[0], mx[1], mx[0], mx[1]}, tag) == 0u) || rt() - t0 > TLIMIT) break;
        __builtin_amdgcn_s_sleep(32);
      }
      *lds_at<float>(lb + L_MH) = row_max(gr_val<4>(mx[0]));
      LGKM0();
    }
    {
      unsigned dq = 0, pend = 0;
      v4u sc = {0u, 0u, 0u, 0u};
      auto flush = [&]() {
        if ((unsigned)lane < 8u * pend) *lds_pl<v4u>(lb + L_SC + dq * 128 + lane * 16) = sc;
        dq += pend; pend = 0;
        LGKM0();
        *lds_at<unsigned>(lb + L_SCR) = dq;
      };
      for (;;) {
        if (__builtin_amdgcn_readfirstlane(*lds_at<unsigned>(lb + L_TKC)) < n) flush();
        for (unsigned sp = 0; *lds_at<unsigned>(lb + L_TKC) < n; ++sp) { __builtin_amdgcn_s_sleep(2); if (EXPIRED(sp)) break; }
        const unsigned v = draw(1);
        flush();
        if (v >= (unsigned)NPT || rt() - t0 > TLIMIT) break;
        pend = 2u;
        sc = ld128<SC1 | NT>(ws, lane < 16 ? WS_SG + (j * 8 * NP + 2 * v) * 128 + lane * 16 : OOB);
        MEMFENCE();
      }
    }
    return;
  }

  const int g = lane >> 4, c = lane & 15;
  const rsrc_t rx = mkrsrc(a.x, DM * 2), rwq = mkrsrc(a.wq, DM * 4096 * 2);
  const rsrc_t rwd = mkrsrc(a.wd, DF * DM * 2);
  const int r0 = 128 * kq + 32 * wave;
  v4u wqv[4];
#pragma unroll
  for (int i = 0; i < 4; ++i) wqv[i] = ld128<NT>(rwq, (unsigned)((r0 + 4 * (lane >> 3) + i) * 4096 + 64 * cb + 8 * (lane & 7)) * 2);
  const v2u ep = ld64<SC1>(ws, WS_EPOCH);
  const v4u x8 = ld128<0>(rx, lane * 16);
  const unsigned x2 = ld32<0>(rx, tid * 4);
  const unsigned xh = ld32<0>(rx, (r0 + 2 * c) * 2);
  MEMFENCE();
  if (lane == 0) __hip_atomic_fetch_add(lds_pl<unsigned>(lb + L_ISSUED), 1u, __ATOMIC_RELAXED, __HIP_MEMORY_SCOPE_WORKGROUP);
  float mb, sd;
  {
    float xs[8];
#pragma unroll
    for (int e = 0; e < 4; ++e) { xs[2 * e] = lo16(x8[e]); xs[2 * e + 1] = hi16(x8[e]); }
    float s = 0.f;
#pragma unroll
    for (int e = 0; e < 8; ++e) s += xs[e];
    const float mean = wave_sum(s) * (1.f / 512.f);
    float v = 0.f;
#pragma unroll
    for (int e = 0; e < 8; ++e) { const float d = xs[e] - mean; v = fmaf(d, d, v); }
    ln_round(mean, wave_sum(v) * (1.f / 512.f), mb, sd);
  }
  float nrow = rbf((lane >= 32 ? hi16(xh) : lo16(xh)) - mb) / sd;
  kpair(wqv[0], wqv[1]); kpair(wqv[2], wqv[3]);
  PIN4(wqv); asm volatile("" : "+v"(nrow));
  const Tag tag = next_tag(ep);
  lds_wait_ge(lb + L_AUXPUB, 1u, t0);
  {
    const unsigned o1 = WS_P1 + (g * 1024 + r0 + 2 * c) * 8;
    v4u gs = {0u, 0u, 0u, 0u}, gc = gs;
    bool bad = true;
    __builtin_amdgcn_s_sleep(32);
    for (;;) {
      MEMFENCE();
      const v4u ns_ = ld128<SC1>(ws, bad ? o1 : OOB), nc_ = ld128<SC1>(ws, bad ? o1 + 4096 : OOB);
      const bool tk = bad & ((gr_bad<0>(ns_, tag) | gr_bad<0>(nc_, tag)) == 0u);
      gs = sel4(tk, ns_, gs); gc = sel4(tk, nc_, gc); bad = bad & !tk;
      if (!__any(bad) || rt() - t0 > TLIMIT) break;
    }
    const unsigned hp = pack_halves(mod_round(nrow, rbf(gr_sum<0>(gc)), rbf(gr_sum<0>(gs))));
    float acc[8] = {0.f, 0.f, 0.f, 0.f, 0.f, 0.f, 0.f, 0.f};
    dot8(acc, __builtin_amdgcn_ds_bpermute((lane >> 3) * 8, hp), wqv[0], wqv[1]);
    dot8(acc, __builtin_amdgcn_ds_bpermute((lane >> 3) * 8 + 4, hp), wqv[2], wqv[3]);
    float u0, u1;
    rows_reduce8(acc, u0, u1);
    u0 += dpp<ROR8>(u0); u1 += dpp<ROR8>(u1);
    if ((lane & 8) == 0) *lds_pl<f32x2>(lb + L_RED + wave * 256 + (8 * (lane & 7) + 2 * g) * 4) = f32x2{u0, u1};
    LGKM0();
    unsigned old = 0;
    if (lane == 0) old = __hip_atomic_fetch_add(lds_pl<unsigned>(lb + L_QCNT), 1u, __ATOMIC_RELAXED, __HIP_MEMORY_SCOPE_WORKGROUP);
    old = __builtin_amdgcn_readfirstlane(old);
    MEMFENCE();
    if (old == 3u) {
      const unsigned ra = lb + L_RED + lane * 4;
      const float qp = (*lds_pl<float>(ra) + *lds_pl<float>(ra + 256)) + (*lds_pl<float>(ra + 512) + *lds_pl<float>(ra + 768));
      st64(ws, WS_QS + (kq * 4096 + 64 * cb + lane) * 8, gr1<2>(qp, tag));
    }
  }
  const int r8 = 8 * (tid >> 2);
  v4u wv[8];
  unsigned wdv[16];
#pragma unroll
  for (int i = 0; i < 8; ++i) { wv[i] = v4u{0u, 0u, 0u, 0u}; wdv[2 * i] = 0u; wdv[2 * i + 1] = 0u; }
  const GAS v4u* wp = (const GAS v4u*)(mlp ? ((tid & 2) ? a.wu : a.wg) + (r8 * DF + 16 * (has_gu ? up : 0) + 8 * (tid & 1)) : a.wo + ((512 * og + r8) * DM + oc0 + 8 * (tid & 3)));
  const int wstride = mlp ? DF / 8 : DM / 8;
  const unsigned wdo = has_gu ? (unsigned)(16 * up * DM + 2 * tid) * 2 : OOB;

  const unsigned key = 4 * (c & 3) + (c >> 2);
  unsigned ka[4], msk[4];
#pragma unroll
  for (int st = 0; st < 4; ++st) {
    ka[st] = lb + L_RING + (wave & 1) * 8192 + key * 256 + (((st + 4 * g) ^ key) << 4);
    msk[st] = ((c & 7) >> 1) == st ? ((c & 1) ? 0xffff0000u : 0x0000ffffu) : 0u;
  }
  const unsigned va = lb + L_RING + (wave & 1) * 8192 + lane * 16;
  const bool is_hi = c < 8;
  volatile LAS unsigned* DN = lds_at<unsigned>(lb + L_DONE + 4 * wave);
  unsigned so = (wave >> 1) * SLOT;
  v4u sv[8], sw[8], ud[8], qf[4];
#pragma unroll
  for (int i = 0; i < 8; ++i) { sv[i] = v4u{0u, 0u, 0u, 0u}; sw[i] = sv[i]; ud[i] = sv[i]; }
  int ns = 0;
  auto release = [&](v4u (&d)[8], int i) {
    LGKM0();
    *DN = (unsigned)(i >> 1) + 1u;
    asm volatile("" : "+v"(d[0]), "+v"(d[1]), "+v"(d[2]), "+v"(d[3]), "+v"(d[4]), "+v"(d[5]), "+v"(d[6]), "+v"(d[7]) :: "memory");
    so = (so + 2 * SLOT) & (NSLOT * SLOT - 1);
  };
  auto read_k = [&](v4u (&d)[8], int i) {
#pragma unroll
    for (int st = 0; st < 4; ++st) { d[st] = *lds_pl<v4u>(ka[st] + so); d[4 + st] = *lds_pl<v4u>(ka[st] + so + 4096); }
    release(d, i);
  };
  int total = INF, nk = INF;
  auto wait_full = [&](int i, bool in_k) {
    for (unsigned sp = 0;; ++sp) {
      const v4u ct = *(volatile LAS v4u*)(size_t)(lb + L_FT);
      total = __builtin_amdgcn_readfirstlane((int)ct[1]);
      nk = __builtin_amdgcn_readfirstlane((int)ct[3]);
      if (EXPIRED(sp)) { total = 0; nk = nk < i ? nk : i; break; }
      if (__builtin_amdgcn_readfirstlane((int)ct[0]) > i || (in_k && nk <= i)) break;
      if (total == INF) __builtin_amdgcn_s_sleep(1);
    }
    MEMFENCE();
  };
  float mraw = -INFINITY;
  auto scores = [&](const v4u (&d)[8], int i) {
    f32x4 sa = {0.f, 0.f, 0.f, 0.f}, sb = {0.f, 0.f, 0.f, 0.f};
#pragma unroll
    for (int st = 0; st < 4; ++st) { sa = mfma(d[st], qf[st], sa); sb = mfma(d[4 + st], qf[st], sb); }
    mraw = fmaxf(fmaxf(fmaxf(mraw, sa[0]), fmaxf(sa[1], sa[2])), fmaxf(fmaxf(sa[3], sb[0]), fmaxf(fmaxf(sb[1], sb[2]), sb[3])));
    v4u pk = {cvtpk(sa[0], sa[1]), cvtpk(sa[2], sa[3]), cvtpk(sb[0], sb[1]), cvtpk(sb[2], sb[3])};
#pragma unroll
    for (int u = 0; u < 4; ++u) pk[u] = cvtpk(lo16(pk[u]) * SCALE, hi16(pk[u]) * SCALE);
    const unsigned so_ = (wave & 1) * 64 + g * 16;
    const unsigned dst = i < NP ? L_SH + i * 128 : L_SC + (i - NP) * 128;
    if (c == 0) *lds_pl<v4u>(lb + dst + so_) = pk;
  };
  auto stash = [&]() {
    const int full = __builtin_amdgcn_readfirstlane((int)*lds_at<unsigned>(lb + L_FT)), i0 = wave >> 1;
    MEMFENCE();
    if (ns == 0 && full > i0) { read_k(wv, i0); ns = 1; }
    if (ns == 1 && full > i0 + 2) { read_k(sv, i0 + 2); ns = 2; }
    if (ns == 2 && full > i0 + 4) { read_k(sw, i0 + 4); ns = 3; }
    if (ns == 3 && full > i0 + 6) { read_k(ud, i0 + 6); ns = 4; }
  };
  {
    const unsigned o2 = WS_QS + (g * 4096 + 128 * j + 32 * wave + 2 * c) * 8;
    v4u gq4 = {0u, 0u, 0u, 0u};
    bool bad = true;
    stash();
    {
      const v4u nq = ld128<SC1>(ws, o2);
      bad = gr_bad<2>(nq, tag) != 0u;
      gq4 = nq;
    }
    asm volatile("" : "+v"(gq4) :: "memory");
    stash();
    for (unsigned it = 0; __any(bad) && !EXPIRED(it); ++it) {
      MEMFENCE();
      const v4u nq = ld128<SC1>(ws, bad ? o2 : OOB);
      const bool tk = bad & (gr_bad<2>(nq, tag) == 0u);
      gq4 = sel4(tk, nq, gq4); bad = bad & !tk;
    }
    *lds_pl<unsigned>(lb + L_QSC + (16 * wave + c) * 4) = pack_halves(rbf(gr_sum<2>(gq4)));
    LGKM0();
    if (lane == 0) __hip_atomic_fetch_add(lds_pl<unsigned>(lb + L_QIN), 1u, __ATOMIC_RELAXED, __HIP_MEMORY_SCOPE_WORKGROUP);
    lds_wait_ge(lb + L_QIN, 4u, t0);
#pragma unroll
    for (int st = 0; st < 4; ++st) qf[st] = *lds_pl<v4u>(lb + L_QSC + (st + 4 * g) * 16);
  }
  if (ns >= 1) scores(wv, wave >> 1);
  if (ns >= 2) scores(sv, (wave >> 1) + 2);
  if (ns >= 3) scores(sw, (wave >> 1) + 4);
  if (ns >= 4) scores(ud, (wave >> 1) + 6);
  int i = (wave >> 1) + 2 * ns;
  for (;; i += 2) {
    wait_full(i, true);
    if (nk <= i) break;
    read_k(ud, i);
    scores(ud, i);
  }
  float m2;
  {
    *lds_pl<float>(lb + L_MXW + wave * 4) = rbf(rbf(xrow_max(mraw)) * SCALE);
    LGKM0();
    unsigned old = 0;
    if (lane == 0) old = __hip_atomic_fetch_add(lds_pl<unsigned>(lb + L_KCNT), 1u, __ATOMIC_RELAXED, __HIP_MEMORY_SCOPE_WORKGROUP);
    old = __builtin_amdgcn_readfirstlane(old);
    lds_wait_ge(lb + L_KCNT, 4u, t0);
    const f32x4 mw = *lds_pl<f32x4>(lb + L_MXW);
    const float mg = fmaxf(fmaxf(mw[0], mw[1]), fmaxf(mw[2], mw[3]));
    if (old == 3u) {
#pragma unroll
      for (int u = 0; u < (NP + 7) / 8; ++u)
        __builtin_amdgcn_raw_buffer_store_b128(*lds_pl<v4u>(lb + L_SH + u * 1024 + lane * 16), ws,
                                               u * 1024 + lane * 16 < NP * 128 ? WS_SG + b * NP * 128 + u * 1024 + lane * 16 : OOB, 0, SC1 | NT);
      VM0();
      st64(ws, lane == 0 ? WS_MX + b * 8 : OOB, gr1<4>(mg, tag));
      *lds_at<unsigned>(lb + L_KCNT) = 5u;
    }
    m2 = mg * LOG2E;
  }
  float lsum = 0.f;
  f32x4 acc = {0.f, 0.f, 0.f, 0.f};
  {
    int wk = (mlp && !has_gu) ? 11 : 0;
    bool done = false, own = true;
    for (;; i += 2) {
      int e = i - nk;
      if (!done && e >= nk - NP) {
        e -= nk - NP;
        for (unsigned sp = 0;; ++sp) {
          const v4u ct = *(volatile LAS v4u*)(size_t)(lb + L_FT);
          if (__builtin_amdgcn_readfirstlane((int)ct[2]) > e) break;
          if (__builtin_amdgcn_readfirstlane((int)ct[1]) <= i || EXPIRED(sp)) { done = true; break; }
          __builtin_amdgcn_s_sleep(1);
        }
        MEMFENCE();
        if (own) {
          const float mh = *lds_at<float>(lb + L_MH) * LOG2E, al = __builtin_amdgcn_exp2f(m2 - mh);
          acc *= al; lsum *= al; m2 = mh; own = false;
        }
      }
      if (!done) {
        const v4u sc = *lds_pl<v4u>(lb + L_SC + e * 128 + (wave & 1) * 64 + g * 16);
        v4u pa[8];
#pragma unroll
        for (int u = 0; u < 8; ++u) {
          const float p = __builtin_amdgcn_exp2f(fmaf((u & 1) ? hi16(sc[u >> 1]) : lo16(sc[u >> 1]), LOG2E, -m2));
          lsum += p;
          const float ph = hi16(asu(p));
          const float xv = is_hi ? ph : p - ph;
          const unsigned pk = cvtpk(xv, xv);
          pa[u] = v4u{pk & msk[0], pk & msk[1], pk & msk[2], pk & msk[3]};
        }
        asm volatile("" : "+v"(pa[0]), "+v"(pa[1]), "+v"(pa[2]), "+v"(pa[3]), "+v"(pa[4]), "+v"(pa[5]), "+v"(pa[6]), "+v"(pa[7]));
        wait_full(i, false);
#pragma unroll
        for (int u = 0; u < 8; ++u) ud[u] = *lds_pl<v4u>(va + so + u * 1024);
        release(ud, i);
#pragma unroll
        for (int u = 0; u < 8; ++u) acc = mfma(pa[u], ud[u], acc);
        done = i + 2 >= total && !own;
      }
      MEMFENCE();
      if (wk <= 10 && (done || rt() - t0 >= (u64)(TW + TD * wk + (wk == 10 ? 400 : 0)))) {
#pragma unroll
        for (int u = 0; u < 8; ++u) if (wk == u) wv[u] = __builtin_nontemporal_load(wp + u * wstride);
#pragma unroll
        for (int v = 0; v < 2; ++v) if (wk == 8 + v) {
#pragma unroll
          for (int u = 8 * v; u < 8 * v + 8; ++u) wdv[u] = ld32<NT>(rwd, wdo + u * (DM * 2));
        }
        if (wk == 10) {
#pragma unroll
          for (int u = 0; u < 8; u += 2) kpair(wv[u], wv[u + 1]);
#pragma unroll
          for (int u = 0; u < 16; u += 2) {
            const unsigned lo = __builtin_amdgcn_perm(wdv[u + 1], wdv[u], 0x05040100u), hi = __builtin_amdgcn_perm(wdv[u + 1], wdv[u], 0x07060302u);
            wdv[u] = lo; wdv[u + 1] = hi;
          }
        }
        ++wk;
      }
      MEMFENCE();
      if (done && wk > 10) break;
    }
  }
  {
    f32x4 o;
#pragma unroll
    for (int r = 0; r < 4; ++r) o[r] = sw32_add(acc[r], acc[r]);
    float l = sw16_add(lsum, lsum); l = sw32_add(l, l);
    *lds_pl<f32x4>(lb + L_MRG + wave * 512 + (8 * c + 4 * (g & 1)) * 4) = o;
    *lds_pl<float>(lb + L_LW + wave * 4) = l;
    wg_barrier();
    if (wave == 0) {
      const unsigned ra = lb + L_MRG + (lane & 31) * 16;
      const f32x4 lw = *lds_pl<f32x4>(lb + L_LW);
      f32x4 O = (*lds_pl<f32x4>(ra) + *lds_pl<f32x4>(ra + 512)) + (*lds_pl<f32x4>(ra + 1024) + *lds_pl<f32x4>(ra + 1536));
      if (lane >= 32) O = f32x4{(lw[0] + lw[1]) + (lw[2] + lw[3]), 0.f, 0.f, 0.f};
      st128(ws, lane < 33 ? WS_PO + b * REC + lane * 16 : OOB, noempty4(O));
    }
  }
  if (!mlp) {
    const unsigned hb = WS_PO + (4 * og + wave) * 8 * REC;
    v2u po[8];
    unsigned ls = EMPTY;
#pragma unroll
    for (int s = 0; s < 8; ++s) po[s] = v2u{0u, 0u};
    unsigned miss = 0x1FFu;
    for (unsigned it = 0;; ++it) {
      MEMFENCE();
      if (miss & 0x100u) ls = ld32<SC1>(ws, (lane & 15) < 8 ? hb + (lane & 15) * REC + 512 : OOB);
#pragma unroll
      for (int s = 0; s < 8; ++s) if (miss & (1u << s)) po[s] = ld64<SC1>(ws, hb + s * REC + lane * 8);
      if ((miss & 0x100u) && __all(ls != EMPTY)) miss &= ~0x100u;
#pragma unroll
      for (int s = 0; s < 8; ++s) if ((miss & (1u << s)) && __all(mx2(0u, po[s]) != EMPTY)) miss &= ~(1u << s);
      if (miss == 0u || EXPIRED(it)) break;
    }
    const float inv = __builtin_amdgcn_rcpf(row_sum(asf(ls)));
    const unsigned op = cvtpk((((asf(po[0][0]) + asf(po[1][0])) + (asf(po[2][0]) + asf(po[3][0]))) + ((asf(po[4][0]) + asf(po[5][0])) + (asf(po[6][0]) + asf(po[7][0])))) * inv,
                              (((asf(po[0][1]) + asf(po[1][1])) + (asf(po[2][1]) + asf(po[3][1]))) + ((asf(po[4][1]) + asf(po[5][1])) + (asf(po[6][1]) + asf(po[7][1])))) * inv);
    float y[8] = {0.f, 0.f, 0.f, 0.f, 0.f, 0.f, 0.f, 0.f};
    dot8(y, dppu<0x00>(op), wv[0], wv[1]); dot8(y, dppu<0x55>(op), wv[2], wv[3]);
    dot8(y, dppu<0xAA>(op), wv[4], wv[5]); dot8(y, dppu<0xFF>(op), wv[6], wv[7]);
    float u0, u1;
    rows_reduce8(y, u0, u1);
    u0 += dpp<ROR8>(u0); u1 += dpp<ROR8>(u1); u0 += dpp<ROR4>(u0); u1 += dpp<ROR4>(u1);
    *lds_pl<f32x2>(lb + L_RED3 + wave * 128 + (8 * (c & 3) + 2 * g) * 4) = f32x2{u0, u1};
    wg_barrier();
    if (wave == 0) {
      const unsigned ra = lb + L_RED3 + (lane & 7) * 16;
      const f32x4 t = (*lds_pl<f32x4>(ra) + *lds_pl<f32x4>(ra + 128)) + (*lds_pl<f32x4>(ra + 256) + *lds_pl<f32x4>(ra + 384));
      st128(ws, lane < 8 ? WS_YP + (og * 512 + oc0) * 4 + lane * 16 : OOB, noempty4(t));
    }
    return;
  }
  lds_wait_ge(lb + L_M2RDY, 1u, t0);
  float gbf = lo16(*lds_pl<u16>(lb + L_M2 + 3072 + (fc0 + g) * 2));
  {
    const unsigned gaw = *lds_pl<unsigned>(lb + L_M2 + tid * 4), sbw = *lds_pl<unsigned>(lb + L_M2 + 1024 + tid * 4), cbw = *lds_pl<unsigned>(lb + L_M2 + 2048 + tid * 4);
    float ga0 = lo16(gaw), ga1 = hi16(gaw), sb0 = lo16(sbw), sb1 = hi16(sbw), t0c = rbf(1.f + lo16(cbw)), t1c = rbf(1.f + hi16(cbw)), xa = lo16(x2), xb = hi16(x2);
    asm volatile("" : "+v"(ga0), "+v"(ga1), "+v"(sb0), "+v"(sb1), "+v"(t0c), "+v"(t1c), "+v"(xa), "+v"(xb), "+v"(gbf));
    v2u yp[8];
#pragma unroll
    for (int s = 0; s < 8; ++s) yp[s] = v2u{0u, 0u};
    unsigned miss = 0xFFu;
    for (unsigned it = 0;; ++it) {
      MEMFENCE();
#pragma unroll
      for (int s = 0; s < 8; ++s) if (miss & (1u << s)) yp[s] = ld64<SC1>(ws, WS_YP + s * 2048 + tid * 8);
#pragma unroll
      for (int s = 0; s < 8; ++s) if ((miss & (1u << s)) && __all(mx2(0u, yp[s]) != EMPTY)) miss &= ~(1u << s);
      if (miss == 0u || EXPIRED(it)) break;
    }
    const unsigned atp = cvtpk(((asf(yp[0][0]) + asf(yp[1][0])) + (asf(yp[2][0]) + asf(yp[3][0]))) + ((asf(yp[4][0]) + asf(yp[5][0])) + (asf(yp[6][0]) + asf(yp[7][0]))),
                               ((asf(yp[0][1]) + asf(yp[1][1])) + (asf(yp[2][1]) + asf(yp[3][1]))) + ((asf(yp[4][1]) + asf(yp[5][1])) + (asf(yp[6][1]) + asf(yp[7][1]))));
    const unsigned gap = cvtpk(ga0 * lo16(atp), ga1 * hi16(atp));
    const unsigned x1p = cvtpk(xa + lo16(gap), xb + hi16(gap));
    const float x1a = lo16(x1p), x1b = hi16(x1p);
    const float sh = asf(__builtin_amdgcn_readfirstlane(asu(x1a)));
    const float da = x1a - sh, db = x1b - sh;
    const float s1 = wave_sum(da + db), s2 = wave_sum(fmaf(da, da, db * db));
    *lds_pl<f32x2>(lb + L_ST + wave * 8) = f32x2{fmaf(s1, 1.f / 128.f, sh), fmaf(-s1 * (1.f / 128.f), s1, s2)};
    *lds_pl<f32x2>(lb + L_X1 + tid * 8) = f32x2{x1a, x1b};
    wg_barrier();
    if (has_gu) {
      const f32x4 sA = *lds_pl<f32x4>(lb + L_ST), sB = *lds_pl<f32x4>(lb + L_ST + 16);
      const float mean = ((sA[0] + sA[2]) + (sB[0] + sB[2])) * 0.25f;
      const float d0 = sA[0] - mean, d1 = sA[2] - mean, d2 = sB[0] - mean, d3 = sB[2] - mean;
      const float var = fmaxf(((sA[1] + sA[3]) + (sB[1] + sB[3])) + 128.f * ((d0 * d0 + d1 * d1) + (d2 * d2 + d3 * d3)), 0.f) * (1.f / 512.f);
      float mb2, sd2;
      ln_round(mean, var, mb2, sd2);
      const float rs = __builtin_amdgcn_rcpf(sd2);
      const unsigned e0 = cvtpk(x1a - mb2, x1b - mb2), e1 = cvtpk(lo16(e0) * rs, hi16(e0) * rs), e2 = cvtpk(lo16(e1) * t0c, hi16(e1) * t1c);
      const unsigned hq = cvtpk(lo16(e2) + sb0, hi16(e2) + sb1);
      float y[8] = {0.f, 0.f, 0.f, 0.f, 0.f, 0.f, 0.f, 0.f};
      dot8(y, dppu<0x00>(hq), wv[0], wv[1]); dot8(y, dppu<0x55>(hq), wv[2], wv[3]);
      dot8(y, dppu<0xAA>(hq), wv[4], wv[5]); dot8(y, dppu<0xFF>(hq), wv[6], wv[7]);
      float u0, u1;
      rows_reduce8(y, u0, u1);
      u0 += dpp<ROR8>(u0); u1 += dpp<ROR8>(u1); u0 += dpp<ROR4>(u0); u1 += dpp<ROR4>(u1);
      *lds_pl<f32x2>(lb + L_RED3 + wave * 128 + (8 * (c & 3) + 2 * g) * 4) = f32x2{u0, u1};
      wg_barrier();
      const unsigned ra = lb + L_RED3 + c * 4;
      const float gs = rbf((*lds_pl<float>(ra) + *lds_pl<float>(ra + 128)) + (*lds_pl<float>(ra + 256) + *lds_pl<float>(ra + 384)));
      const float us = rbf((*lds_pl<float>(ra + 64) + *lds_pl<float>(ra + 192)) + (*lds_pl<float>(ra + 320) + *lds_pl<float>(ra + 448)));
      const float z = 0.7978845608028654f * fmaf(0.044715f * gs * gs, gs, gs);
      const float th = fmaf(-2.f, __builtin_amdgcn_rcpf(1.f + __expf(2.f * z)), 1.f);
      const float h2 = rbf(rbf(0.5f * gs * (1.f + th)) * us);
      const unsigned hp = __builtin_amdgcn_perm(asu(dpp<XOR1>(h2)), asu(h2), 0x07060302u);
      float d0a = 0.f, d1a = 0.f;
#pragma unroll
      for (int e = 0; e < 16; e += 2) {
        const unsigned he = __builtin_amdgcn_readlane(hp, e);
        d0a = dot2(wdv[e], he, d0a); d1a = dot2(wdv[e + 1], he, d1a);
      }
      st64(ws, WS_DP + up * 2048 + tid * 8, v2u{noempty(asu(d0a)), noempty(asu(d1a))});
      st64(ws, (b | tid) == 0 ? WS_EPOCH : OOB, v2u{tag.lo, tag.hi});
    }
  }
  if (wave != 0) return;
  {
    v4u da = {EMPTY, 0u, 0u, 0u}, db = {lane < NGU - 64 ? EMPTY : 0u, 0u, 0u, 0u};
    const float x1o = *lds_pl<float>(lb + L_X1 + (fc0 + g) * 4);
    for (unsigned it = 0;; ++it) {
      MEMFENCE();
      const bool ma = mx4(0u, da) == EMPTY, mb_ = mx4(0u, db) == EMPTY;
      if (!__any(ma | mb_) || EXPIRED(it)) break;
      const v4u na = ld128<SC1>(ws, ma ? WS_DP + lane * 2048 + fc0 * 4 : OOB), nb = ld128<SC1>(ws, mb_ ? WS_DP + (lane + 64) * 2048 + fc0 * 4 : OOB);
      da = ma ? na : da; db = mb_ ? nb : db;
    }
    float t4[4];
#pragma unroll
    for (int r = 0; r < 4; ++r) t4[r] = asf(da[r]) + asf(db[r]);
    const float r = row_sum(sw16_add(sw32_add(t4[0], t4[2]), sw32_add(t4[1], t4[3])));
    const float res = x1o + rbf(gbf * rbf(r));
    if (c == 0) a.out[fc0 + g] = (u16)(cvtpk(res, res) & 0xffffu);
  }
}
}

extern "C" void kernel_launch(void* const* d_in, const int* in_sizes, int n_in, void* d_out, int out_size, void* d_ws, size_t ws_size,
                              hipStream_t stream) {
  Args a;
  a.x = (const u16*)d_in[0]; a.K = (const u16*)d_in[1]; a.V = (const u16*)d_in[2]; a.wq = (const u16*)d_in[3]; a.wo = (const u16*)d_in[4];
  a.wg = (const u16*)d_in[6]; a.wu = (const u16*)d_in[7]; a.wd = (const u16*)d_in[8]; a.c = (const u16*)d_in[9]; a.wm = (const u16*)d_in[10];
  a.out = (u16*)d_out; a.ws = (unsigned char*)d_ws;
  (void)hipFuncSetAttribute((const void*)block_decode, hipFuncAttributeMaxDynamicSharedMemorySize, (int)LDS_BYTES);
  hipLaunchKernelGGL(block_decode, dim3(NWG), dim3(NTHR), LDS_BYTES, stream, a);
}
```
